```python
import torch

def _softmax(x, axis=-1):
    return torch.softmax(x, dim=axis)
def _gelu(x):
    return torch.nn.functional.gelu(x, approximate="tanh")
def _silu(x):
    return torch.nn.functional.silu(x)
def _sigmoid(x):
    return torch.sigmoid(x)
def _layernorm(x, axis=-1, eps=1e-5):
    m = x.mean(dim=axis, keepdim=True)
    v = x.var(dim=axis, keepdim=True, unbiased=False)
    return (x - m) / torch.sqrt(v + eps)
def _scatter_add(tgt, idx, src, axis=0):
    return tgt.clone().index_add_(axis, idx, src)
def _segment_sum(src, seg, n):
    out = torch.zeros((n,) + src.shape[1:], dtype=src.dtype)
    return out.index_add_(0, seg, src)
def _pad(x, pw):
    flat = tuple(v for pair in reversed(pw) for v in pair)
    return torch.nn.functional.pad(x, flat)

kernel_name = 'concat_heads__sliding_block_cached__mha_block_cached__decode'

def setup_inputs():

    _w = (lambda a: (lambda t: t.to(torch.bfloat16) if t.is_floating_point() else t)(torch.as_tensor(a)))
    def _A_setup():
        B, Sq, D = 2, 1, 576
        Hq, Hkv, Sk, Dh = 32, 16, 8192, 128
        assert Hq % Hkv == 0, (Hq, Hkv)
        return {
            "x": _w((lambda *s: torch.randn(*s, dtype=torch.bfloat16) * 0.8)(B, Sq, D)),
            "k_cache": _w((lambda *s: torch.randn(*s, dtype=torch.bfloat16) * 1.6)(B, Hkv, Sk, Dh)),
            "v_cache": _w((lambda *s: torch.randn(*s, dtype=torch.bfloat16) * 0.2)(B, Hkv, Sk, Dh)),
            "wq": _w((lambda *s: torch.randn(*s, dtype=torch.bfloat16) * 1.6)(D, Hq * Dh) * (D ** -0.5)),
            "wo": _w((lambda *s: torch.randn(*s, dtype=torch.bfloat16) * 0.2)(Hq * Dh, D) * ((Hq * Dh) ** -0.5)),
        }
    _A_in = _A_setup()

    def _B_setup():
        B, Sq, D = 2, 1, 576
        H, Sk, Dh = 32, 8192, 128
        return {
            "x": _w((lambda *s: torch.randn(*s, dtype=torch.bfloat16) * 0.8)(B, Sq, D)),
            "k_cache": _w((lambda *s: torch.randn(*s, dtype=torch.bfloat16) * 1.6)(B, H, Sk, Dh)),
            "v_cache": _w((lambda *s: torch.randn(*s, dtype=torch.bfloat16) * 0.2)(B, H, Sk, Dh)),
            "wq": _w((lambda *s: torch.randn(*s, dtype=torch.bfloat16) * 1.6)(D, H * Dh) * (D ** -0.5)),
            "wo": _w((lambda *s: torch.randn(*s, dtype=torch.bfloat16) * 0.2)(H * Dh, D) * ((H * Dh) ** -0.5)),
        }
    _B_in = _B_setup()

    return {
        "A_x": _A_in["x"],
        "A_k_cache": _A_in["k_cache"],
        "A_v_cache": _A_in["v_cache"],
        "A_wq": _A_in["wq"],
        "A_wo": _A_in["wo"],
        "B_x": _B_in["x"],
        "B_k_cache": _B_in["k_cache"],
        "B_v_cache": _B_in["v_cache"],
        "B_wq": _B_in["wq"],
        "B_wo": _B_in["wo"],
    }

def reference(A_x, A_k_cache, A_v_cache, A_wq, A_wo, B_x, B_k_cache, B_v_cache, B_wq, B_wo):
    def _A_ref(x, k_cache, v_cache, wq, wo):
        B, Sq, D = x.shape
        Hq, Dh = 32, 128
        Hkv, Sk = k_cache.shape[1], k_cache.shape[2]
        W = min(4096, max(256, Sk // 4))
        rep = Hq // Hkv
        q = (lambda x, *d: x.permute(*d))((x @ wq).reshape(B, Sq, Hq, Dh), 0, 2, 1, 3)
        k = (lambda x, n, axis: torch.repeat_interleave(x, n, dim=axis))(k_cache[:, :, Sk - W:, :], rep, axis=1)
        v = (lambda x, n, axis: torch.repeat_interleave(x, n, dim=axis))(v_cache[:, :, Sk - W:, :], rep, axis=1)
        scores = torch.einsum("bhqd,bhkd->bhqk", q, k) * (Dh ** -0.5)
        p = _softmax(scores, axis=-1)
        o = torch.einsum("bhqk,bhkd->bhqd", p, v)
        return (lambda x, *d: x.permute(*d))(o, 0, 2, 1, 3).reshape(B, Sq, Hq * Dh) @ wo

    def _B_ref(x, k_cache, v_cache, wq, wo):
        B, Sq, D = x.shape
        H, Dh = 32, 128
        q = (lambda x, *d: x.permute(*d))((x @ wq).reshape(B, Sq, H, Dh), 0, 2, 1, 3)
        scores = torch.einsum("bhqd,bhkd->bhqk", q, k_cache) * (Dh ** -0.5)
        p = _softmax(scores, axis=-1)
        o = torch.einsum("bhqk,bhkd->bhqd", p, v_cache)
        return (lambda x, *d: x.permute(*d))(o, 0, 2, 1, 3).reshape(B, Sq, H * Dh) @ wo

    a = _A_ref(A_x, A_k_cache, A_v_cache, A_wq, A_wo)
    b = _B_ref(B_x, B_k_cache, B_v_cache, B_wq, B_wo)
    return torch.concatenate([a, b], axis=-1)
```

```cpp
#include <hip/hip_runtime.h>
#include <hip/hip_bf16.h>
#include <cstdint>

typedef unsigned u32x4 __attribute__((ext_vector_type(4)));
typedef float f32x2 __attribute__((ext_vector_type(2)));
typedef float f32x4 __attribute__((ext_vector_type(4)));
typedef __attribute__((ext_vector_type(2))) __bf16 bf16x2;

constexpr int D = 576, DH = 128, NH = 32, SK = 8192, WIN = 2048, HKV = 16;

__device__ __forceinline__ float bf_lo(unsigned u) { return __builtin_bit_cast(float, u << 16); }
__device__ __forceinline__ float bf_hi(unsigned u) { return __builtin_bit_cast(float, u & 0xffff0000u); }
__device__ __forceinline__ float dot2(unsigned k, unsigned q, float acc) {
  return __builtin_amdgcn_fdot2_f32_bf16(__builtin_bit_cast(bf16x2, k), __builtin_bit_cast(bf16x2, q), acc, false);
}
template <int CTRL> __device__ __forceinline__ float dpp(float x) {
  return __builtin_bit_cast(float, __builtin_amdgcn_mov_dpp(__builtin_bit_cast(int, x), CTRL, 0xf, 0xf, true));
}
__device__ __forceinline__ float rowsum16(float x) {
  x += dpp<0x128>(x);
  x += dpp<0x124>(x);
  x += dpp<0x122>(x);
  x += dpp<0x121>(x);
  return x;
}
__device__ __forceinline__ unsigned short f2bf(float f) {
  __hip_bfloat16 b = __float2bfloat16(f);
  return __builtin_bit_cast(unsigned short, b);
}

struct Args {
  const unsigned short *xA, *kA, *vA, *wqA, *woA, *xB, *kB, *vB, *wqB, *woB;
  unsigned short* out;
  unsigned char* ws;
};
constexpr size_t WS_Q = 0;
constexpr size_t WS_PB = 65536;
constexpr size_t WS_PA = WS_PB + 256 * 132 * 4;
constexpr size_t WS_Y = WS_PA + 512 * 132 * 4;

__global__ void __launch_bounds__(256) k_qproj(Args a) {
  __shared__ float red[32][2][64];
  const int br = blockIdx.x >> 6, cb = (blockIdx.x & 63) * 64;
  const unsigned short* w = br ? a.wqB : a.wqA;
  const unsigned short* x = br ? a.xB : a.xA;
  const int rg = threadIdx.x >> 3, c8 = threadIdx.x & 7;
  float acc[2][8] = {};
  for (int r = rg; r < D; r += 32) {
    u32x4 wv = *reinterpret_cast<const u32x4*>(w + (size_t)r * 4096 + cb + c8 * 8);
    float x0 = __builtin_bit_cast(float, (unsigned)x[r] << 16), x1 = __builtin_bit_cast(float, (unsigned)x[D + r] << 16);
#pragma unroll
    for (int k = 0; k < 4; ++k) {
      float lo = bf_lo(wv[k]), hi = bf_hi(wv[k]);
      acc[0][2 * k] += x0 * lo; acc[0][2 * k + 1] += x0 * hi;
      acc[1][2 * k] += x1 * lo; acc[1][2 * k + 1] += x1 * hi;
    }
  }
#pragma unroll
  for (int k = 0; k < 8; ++k) { red[rg][0][c8 * 8 + k] = acc[0][k]; red[rg][1][c8 * 8 + k] = acc[1][k]; }
  __syncthreads();
  if (threadIdx.x < 128) {
    int b = threadIdx.x >> 6, c = threadIdx.x & 63;
    float s = 0;
    for (int r = 0; r < 32; ++r) s += red[r][b][c];
    unsigned short* q = reinterpret_cast<unsigned short*>(a.ws + WS_Q);
    q[(br * 2 + b) * 4096 + cb + c] = f2bf(s);
  }
}

constexpr float C2 = 0.088388347648318440f * 1.4426950408889634f;
constexpr int NW = 8;
constexpr int T = 4;

template <int G>
__device__ __forceinline__ void run(const unsigned short* __restrict__ kp, const unsigned short* __restrict__ vp, int nkeys, const unsigned short* q,
                                    float* lds, float* outp, int lane, int wave) {
  const int r = lane >> 4, c = lane & 15;
  u32x4 qv[G];
#pragma unroll
  for (int g = 0; g < G; ++g) qv[g] = *reinterpret_cast<const u32x4*>(q + g * DH + c * 8);
  float m[G], l[G], acc[G][8];
#pragma unroll
  for (int g = 0; g < G; ++g) { m[g] = -INFINITY; l[g] = 0; for (int k = 0; k < 8; ++k) acc[g][k] = 0; }
  const int ntiles = nkeys / (4 * T * NW);
  for (int i = 0; i < ntiles; ++i) {
    const size_t base = ((size_t)(i * NW + wave) * (4 * T)) * DH + lane * 8;
    u32x4 K[T], V[T];
#pragma unroll
    for (int t = 0; t < T; ++t) K[t] = __builtin_nontemporal_load(reinterpret_cast<const u32x4*>(kp + base + t * 512));
#pragma unroll
    for (int t = 0; t < T; ++t) V[t] = __builtin_nontemporal_load(reinterpret_cast<const u32x4*>(vp + base + t * 512));
#pragma unroll
    for (int g = 0; g < G; ++g) {
      float s[T];
      float mx = m[g];
#pragma unroll
      for (int t = 0; t < T; ++t) {
        float d = 0;
#pragma unroll
        for (int k = 0; k < 4; ++k) d = dot2(K[t][k], qv[g][k], d);
        s[t] = rowsum16(d) * C2;
        mx = fmaxf(mx, s[t]);
      }
      const float alpha = __builtin_amdgcn_exp2f(m[g] - mx);
      m[g] = mx;
      l[g] *= alpha;
#pragma unroll
      for (int k = 0; k < 8; ++k) acc[g][k] *= alpha;
#pragma unroll
      for (int t = 0; t < T; ++t) {
        const float p = __builtin_amdgcn_exp2f(s[t] - mx);
        l[g] += p;
#pragma unroll
        for (int k = 0; k < 4; ++k) { acc[g][2 * k] += p * bf_lo(V[t][k]); acc[g][2 * k + 1] += p * bf_hi(V[t][k]); }
      }
    }
  }
#pragma unroll
  for (int g = 0; g < G; ++g) {
    float* rec = lds + ((wave * 4 + r) * G + g) * 132;
#pragma unroll
    for (int k = 0; k < 8; ++k) rec[4 + c * 8 + k] = acc[g][k];
    if (c == 0) { rec[0] = m[g]; rec[1] = l[g]; }
  }
  __syncthreads();
  const int tid = wave * 64 + lane;
  if (tid < 128 * G) {
    const int g = tid >> 7, d = tid & 127;
    float M = -INFINITY;
    for (int j = 0; j < NW * 4; ++j) M = fmaxf(M, lds[(j * G + g) * 132]);
    float L = 0, o = 0;
    for (int j = 0; j < NW * 4; ++j) {
      const float* rec = lds + (j * G + g) * 132;
      const float w = __builtin_amdgcn_exp2f(rec[0] - M);
      L += w * rec[1]; o += w * rec[4 + d];
    }
    outp[g * 132 + 4 + d] = o;
    if (d == 0) { outp[g * 132] = M; outp[g * 132 + 1] = L; }
  }
  __syncthreads();
}

__global__ void __launch_bounds__(NW * 64) k_attn(Args a) {
  __shared__ float lds[NW * 4 * 2 * 132];
  const int wg = blockIdx.x, lane = threadIdx.x & 63, wave = __builtin_amdgcn_readfirstlane(threadIdx.x >> 6);
  const unsigned short* q = reinterpret_cast<const unsigned short*>(a.ws + WS_Q);
  {
    const int bk = wg >> 3, sp = wg & 7, b = bk >> 4, kvh = bk & 15;
    const size_t off = ((size_t)bk * SK + (SK - WIN) + sp * 256) * DH;
    run<2>(a.kA + off, a.vA + off, 256, q + (0 * 2 + b) * 4096 + kvh * 2 * DH, lds, reinterpret_cast<float*>(a.ws + WS_PA) + wg * 2 * 132, lane, wave);
  }
  {
    const int bh = wg >> 2, sp = wg & 3, b = bh >> 5, h = bh & 31;
    const size_t off = ((size_t)bh * SK + sp * 2048) * DH;
    run<1>(a.kB + off, a.vB + off, 2048, q + (1 * 2 + b) * 4096 + h * DH, lds, reinterpret_cast<float*>(a.ws + WS_PB) + wg * 132, lane, wave);
  }
}

__global__ void __launch_bounds__(576) k_oproj(Args a) {
  __shared__ float o[2][128];
  __shared__ float red[8][2][576];
  const int br = blockIdx.x >> 5, h = blockIdx.x & 31;
  const int tid = threadIdx.x;
  if (tid < 256) {
    const int b = tid >> 7, d = tid & 127;
    float M = -INFINITY, L = 0, acc = 0;
    if (br == 0) {
      const float* p = reinterpret_cast<const float*>(a.ws + WS_PA) + (size_t)(((b * 16 + (h >> 1)) * 8) * 2 + (h & 1)) * 132;
      for (int s = 0; s < 8; ++s) M = fmaxf(M, p[s * 264]);
      for (int s = 0; s < 8; ++s) { float w = __builtin_amdgcn_exp2f(p[s * 264] - M); L += w * p[s * 264 + 1]; acc += w * p[s * 264 + 4 + d]; }
    } else {
      const float* p = reinterpret_cast<const float*>(a.ws + WS_PB) + (size_t)((b * 32 + h) * 4) * 132;
      for (int s = 0; s < 4; ++s) M = fmaxf(M, p[s * 132]);
      for (int s = 0; s < 4; ++s) { float w = __builtin_amdgcn_exp2f(p[s * 132] - M); L += w * p[s * 132 + 1]; acc += w * p[s * 132 + 4 + d]; }
    }
    o[b][d] = acc / L;
  }
  __syncthreads();
  const unsigned short* wo = (br ? a.woB : a.woA) + (size_t)h * 128 * D;
  const int rg = tid / 72, c8 = tid % 72;
  float acc[2][8] = {};
  for (int r = rg; r < 128; r += 8) {
    u32x4 wv = *reinterpret_cast<const u32x4*>(wo + (size_t)r * D + c8 * 8);
    float x0 = o[0][r], x1 = o[1][r];
#pragma unroll
    for (int k = 0; k < 4; ++k) {
      float lo = bf_lo(wv[k]), hi = bf_hi(wv[k]);
      acc[0][2 * k] += x0 * lo; acc[0][2 * k + 1] += x0 * hi;
      acc[1][2 * k] += x1 * lo; acc[1][2 * k + 1] += x1 * hi;
    }
  }
#pragma unroll
  for (int k = 0; k < 8; ++k) { red[rg][0][c8 * 8 + k] = acc[0][k]; red[rg][1][c8 * 8 + k] = acc[1][k]; }
  __syncthreads();
  float* y = reinterpret_cast<float*>(a.ws + WS_Y) + (size_t)(br * 32 + h) * 2 * D;
  for (int b = 0; b < 2; ++b) {
    float s = 0;
    for (int r = 0; r < 8; ++r) s += red[r][b][tid];
    y[b * D + tid] = s;
  }
}

__global__ void __launch_bounds__(576) k_final(Args a) {
  const int br = blockIdx.x >> 1, b = blockIdx.x & 1, n = threadIdx.x;
  const float* y = reinterpret_cast<const float*>(a.ws + WS_Y) + (size_t)(br * 32) * 2 * D + b * D + n;
  float s = 0;
  for (int h = 0; h < 32; ++h) s += y[h * 2 * D];
  a.out[b * 1152 + br * 576 + n] = f2bf(s);
}

extern "C" void kernel_launch(void* const* d_in, const int* in_sizes, int n_in, void* d_out, int out_size, void* d_ws, size_t ws_size,
                              hipStream_t stream) {
  Args a;
  a.xA = (const unsigned short*)d_in[0]; a.kA = (const unsigned short*)d_in[1]; a.vA = (const unsigned short*)d_in[2];
  a.wqA = (const unsigned short*)d_in[3]; a.woA = (const unsigned short*)d_in[4];
  a.xB = (const unsigned short*)d_in[5]; a.kB = (const unsigned short*)d_in[6]; a.vB = (const unsigned short*)d_in[7];
  a.wqB = (const unsigned short*)d_in[8]; a.woB = (const unsigned short*)d_in[9];
  a.out = (unsigned short*)d_out; a.ws = (unsigned char*)d_ws;
  k_qproj<<<128, 256, 0, stream>>>(a);
  k_attn<<<256, NW * 64, 0, stream>>>(a);
  k_oproj<<<64, 576, 0, stream>>>(a);
  k_final<<<4, 576, 0, stream>>>(a);
}
```

```cpp
#include <hip/hip_runtime.h>
#include <stdint.h>

typedef unsigned v4u __attribute__((ext_vector_type(4)));
typedef unsigned v3u __attribute__((ext_vector_type(3)));
typedef unsigned v2u __attribute__((ext_vector_type(2)));
typedef float v4f __attribute__((ext_vector_type(4)));
typedef float v2f __attribute__((ext_vector_type(2)));
typedef __bf16 bf16x2 __attribute__((ext_vector_type(2)));
#define LAS __attribute__((address_space(3)))
#define GAS __attribute__((address_space(1)))
#define DI __device__ __forceinline__

#define NSTAT 8
#define WINU 8
#define AHEAD 2
constexpr int NWAVE = 9, NTHR = NWAVE * 64;
constexpr int RU = 16;
constexpr int NS = 4 + 16 + NSTAT;
constexpr int POOL = (16 - NSTAT) * 8;
constexpr unsigned SPIN_CAP = 1u << 20;
constexpr int L_RK = 0, L_RV = RU * 4096, L_CTL = 2 * RU * 4096, L_Q = L_CTL + 1024, L_QP = L_Q + 1024, L_REC = L_QP + 1536, REC_B = 528,
              L_END = L_REC + 16 * REC_B;
enum { C_FULLK = 0, C_FULLV = 1, C_END = 2, C_QRDY = 3, C_DONE = 4, C_WANT = 8, C_QCNT = 9, C_TAGA = 10, C_TAGB = 11, C_CNT = 12, C_YPUB = 15, C_TICK = 16, C_WORDS = 16 + POOL + 8 };
static_assert(C_WORDS * 4 <= 1024, "");
constexpr int W_X = 0, W_CTR = 4096, W_QA = 16384, W_QB = W_QA + 32768, W_P = W_QB + 32768, P_STR = 9216, W_Y = W_P + 128 * P_STR, Y_STR = 4608,
              W_ENDB = W_Y + 4 * 256 * Y_STR, W_STAMP = (W_ENDB + 4095) / 4096 * 4096;
constexpr float SCALE = 0.088388347648318440f, L2E = 1.4426950408889634f;

struct Args {
  const unsigned char *xA, *kA, *vA, *wqA, *woA, *xB, *kB, *vB, *wqB, *woB;
  unsigned char *out, *ws;
};

DI float dot2(unsigned a, unsigned b, float c) { return __builtin_amdgcn_fdot2_f32_bf16(__builtin_bit_cast(bf16x2, a), __builtin_bit_cast(bf16x2, b), c, false); }
template <int CTRL> DI float dpp(float x) { return __builtin_bit_cast(float, __builtin_amdgcn_mov_dpp(__builtin_bit_cast(int, x), CTRL, 0xf, 0xf, true)); }
DI float rowsum16(float x) { x += dpp<0x128>(x); x += dpp<0x124>(x); x += dpp<0x122>(x); x += dpp<0x121>(x); return x; }
DI unsigned pk_bf16(float lo, float hi) { return __builtin_bit_cast(unsigned, bf16x2{(__bf16)lo, (__bf16)hi}); }
DI float bf_lo(unsigned u) { return __builtin_bit_cast(float, u << 16); }
DI float bf_hi(unsigned u) { return __builtin_bit_cast(float, u & 0xffff0000u); }
DI float fbits(unsigned u) { return __builtin_bit_cast(float, u); }
DI unsigned ubits(float f) { return __builtin_bit_cast(unsigned, f); }
DI unsigned rfl(unsigned x) { return __builtin_amdgcn_readfirstlane(x); }
DI float ex2(float x) { return __builtin_amdgcn_exp2f(x); }
DI float rs32(float x, float y) { auto s = __builtin_amdgcn_permlane32_swap(ubits(x), ubits(y), false, false); return fbits(s[0]) + fbits(s[1]); }
DI float rs16(float x, float y) { auto s = __builtin_amdgcn_permlane16_swap(ubits(x), ubits(y), false, false); return fbits(s[0]) + fbits(s[1]); }
DI float xsum(float x) { x = rs16(x, x); return rs32(x, x); }
DI float xmax(float x) {
  auto s = __builtin_amdgcn_permlane16_swap(ubits(x), ubits(x), false, false); x = fmaxf(fbits(s[0]), fbits(s[1]));
  auto t = __builtin_amdgcn_permlane32_swap(ubits(x), ubits(x), false, false); return fmaxf(fbits(t[0]), fbits(t[1]));
}
DI void lgkm0() { asm volatile("s_waitcnt lgkmcnt(0)" ::: "memory"); }
template <int N> DI void waitvm() { asm volatile("s_waitcnt vmcnt(%0)" ::"n"(N) : "memory"); }
DI __amdgpu_buffer_rsrc_t mkrs(const void* p, unsigned bytes) { return __builtin_amdgcn_make_buffer_rsrc((void*)p, 0, bytes, 0x00020000); }
DI void dma4(__amdgpu_buffer_rsrc_t rs, unsigned voff, unsigned soff, unsigned lds) {
  unsigned keep;
  asm volatile("s_mov_b32 %0, m0\n\ts_mov_b32 m0, %4\n\ts_nop 3\n\t"
               "buffer_load_dwordx4 %1, %2, %3 offen nt lds\n\t"
               "buffer_load_dwordx4 %1, %2, %3 offen offset:1024 nt lds\n\t"
               "buffer_load_dwordx4 %1, %2, %3 offen offset:2048 nt lds\n\t"
               "buffer_load_dwordx4 %1, %2, %3 offen offset:3072 nt lds\n\t"
               "s_mov_b32 m0, %0"
               : "=&s"(keep) : "v"(voff), "s"(rs), "s"(soff), "s"(lds) : "memory");
}
constexpr int SC1 = 16, NT = 2;
DI v4u gld(__amdgpu_buffer_rsrc_t rs, int off) { return __builtin_amdgcn_raw_buffer_load_b128(rs, off, 0, SC1); }
DI void gst(__amdgpu_buffer_rsrc_t rs, int off, unsigned a, unsigned b, unsigned ta, unsigned tb) { __builtin_amdgcn_raw_buffer_store_b128(v4u{a, ta, b, tb}, rs, off, 0, SC1); }
DI bool gok(v4u g, unsigned ta, unsigned tb) { return g.y == ta && g.w == tb; }

#define ST(i) do { } while (0)

typedef volatile LAS unsigned* ctl_t;
DI bool wait_ge(ctl_t ctl, int idx, unsigned v, int slp) {
  for (unsigned s = 0; rfl(ctl[idx]) < v; ++s) {
    if (s > SPIN_CAP) return false;
    if (slp == 1) __builtin_amdgcn_s_sleep(1); else __builtin_amdgcn_s_sleep(8);
  }
  asm volatile("" ::: "memory");
  return true;
}

template <int G> struct St { float m[G], l[G], acc[G][8]; };
template <int G> DI void st_init(St<G>& s) {
#pragma unroll
  for (int g = 0; g < G; ++g) { s.m[g] = -INFINITY; s.l[g] = 0.f;
#pragma unroll
    for (int k = 0; k < 8; ++k) s.acc[g][k] = 0.f; }
}
template <int G> DI bool unit(LAS unsigned char* L, ctl_t ctl, int n, int cw, int lane, const v4u (&q)[G], St<G>& s) {
  const unsigned u = n * 4 + cw;
  for (unsigned sp = 0; rfl(ctl[C_FULLK]) <= u; ++sp) {
    if ((unsigned)n >= rfl(ctl[C_END]) || sp > SPIN_CAP) return false;
    __builtin_amdgcn_s_sleep(1);
  }
  asm volatile("" ::: "memory");
  const LAS v4u* kp = (const LAS v4u*)(L + L_RK + (u % RU) * 4096) + lane;
  v4u K[4], V[4];
#pragma unroll
  for (int t = 0; t < 4; ++t) K[t] = kp[t * 64];
  float p[G][4], al[G];
#pragma unroll
  for (int g = 0; g < G; ++g) {
    float d[4];
#pragma unroll
    for (int t = 0; t < 4; ++t) {
      float x = dot2(K[t].x, q[g].x, 0.f); x = dot2(K[t].y, q[g].y, x); x = dot2(K[t].z, q[g].z, x); x = dot2(K[t].w, q[g].w, x);
      d[t] = rowsum16(x);
    }
    const unsigned a01 = pk_bf16(d[0], d[1]), a23 = pk_bf16(d[2], d[3]);
    const unsigned b01 = pk_bf16(bf_lo(a01) * SCALE, bf_hi(a01) * SCALE), b23 = pk_bf16(bf_lo(a23) * SCALE, bf_hi(a23) * SCALE);
    d[0] = bf_lo(b01) * L2E; d[1] = bf_hi(b01) * L2E; d[2] = bf_lo(b23) * L2E; d[3] = bf_hi(b23) * L2E;
    const float mx = fmaxf(fmaxf(fmaxf(d[0], d[1]), fmaxf(d[2], d[3])), s.m[g]);
    al[g] = ex2(s.m[g] - mx);
    s.m[g] = mx;
#pragma unroll
    for (int t = 0; t < 4; ++t) p[g][t] = ex2(d[t] - mx);
    s.l[g] = s.l[g] * al[g] + ((p[g][0] + p[g][1]) + (p[g][2] + p[g][3]));
#pragma unroll
    for (int k = 0; k < 8; ++k) s.acc[g][k] *= al[g];
  }
  if (!wait_ge(ctl, C_FULLV, u + 1, 1)) return false;
  const LAS v4u* vp = (const LAS v4u*)(L + L_RV + (u % RU) * 4096) + lane;
#pragma unroll
  for (int t = 0; t < 4; ++t) V[t] = vp[t * 64];
  lgkm0();
  ctl[C_DONE + cw] = n + 1;
  asm volatile("" : "+v"(V[0]), "+v"(V[1]), "+v"(V[2]), "+v"(V[3])::"memory");
#pragma unroll
  for (int t = 0; t < 4; ++t) {
#pragma unroll
    for (int k = 0; k < 4; ++k) {
      const float lo = bf_lo(V[t][k]), hi = bf_hi(V[t][k]);
#pragma unroll
      for (int g = 0; g < G; ++g) { s.acc[g][2 * k] = fmaf(p[g][t], lo, s.acc[g][2 * k]); s.acc[g][2 * k + 1] = fmaf(p[g][t], hi, s.acc[g][2 * k + 1]); }
    }
  }
  return true;
}
DI void put_rec(LAS unsigned char* rec, int lane, float m, float l, const float (&acc)[8]) {
  const float M = xmax(m), f = ex2(m - M);
  const float Ls = xsum(l * f);
  float b[4];
#pragma unroll
  for (int k = 0; k < 4; ++k) b[k] = rs32(acc[k] * f, acc[k + 4] * f);
  const float c0 = rs16(b[0], b[2]), c1 = rs16(b[1], b[3]);
  *(LAS v2f*)(rec + 16 + ((lane & 15) * 8 + (lane >> 4) * 2) * 4) = v2f{c0, c1};
  if (lane == 0) *(LAS v2f*)rec = v2f{M, Ls};
}
DI void pub_rec(LAS unsigned char* rec, __amdgpu_buffer_rsrc_t ws, int tgt, int j, int lane, unsigned ta, unsigned tb) {
  v2f ml[4], av[4];
#pragma unroll
  for (int w = 0; w < 4; ++w) { ml[w] = *(LAS v2f*)(rec + w * REC_B); av[w] = *(LAS v2f*)(rec + w * REC_B + 16 + lane * 8); }
  const float M = fmaxf(fmaxf(ml[0].x, ml[1].x), fmaxf(ml[2].x, ml[3].x));
  float Ls = 0.f, a0 = 0.f, a1 = 0.f;
#pragma unroll
  for (int w = 0; w < 4; ++w) { const float f = ex2(ml[w].x - M); Ls = fmaf(f, ml[w].y, Ls); a0 = fmaf(f, av[w].x, a0); a1 = fmaf(f, av[w].y, a1); }
  const int base = W_P + tgt * P_STR;
  gst(ws, base + (lane >> 3) * 1024 + j * 128 + (lane & 7) * 16, ubits(a0), ubits(a1), ta, tb);
  if (lane < 8) gst(ws, base + 8192 + j * 128 + lane * 16, ubits(M), ubits(Ls), ta, tb);
}

__global__ void __launch_bounds__(NTHR) fused_decode(Args a) {
  __shared__ __attribute__((aligned(16))) unsigned char smem[L_END];
  LAS unsigned char* L = (LAS unsigned char*)smem;
  ctl_t ctl = (ctl_t)(L + L_CTL);
  const int wg = blockIdx.x, g = wg >> 3, j = wg & 7;
  const int lane = threadIdx.x & 63, w = rfl(threadIdx.x >> 6);
  const __amdgpu_buffer_rsrc_t ws = mkrs(a.ws, W_ENDB);
  const int cb = (wg & ~15) + ((wg & 7) << 1) + ((wg >> 3) & 1);
  ST(w == 8 ? 0 : 31);
  const bool qwave = w < 4 || w == 6 || w == 7;
  const int qw = w < 4 ? w : w - 2;
  v4u Wq[6]; v3u X0, X1; v2u ep;
  if (qwave) {
    const int r0 = qw * 96 + (lane >> 2) * 6;
    const unsigned char* xp = (cb < 128 ? a.xA : a.xB) + r0 * 2;
    const unsigned char* wp = (cb < 128 ? a.wqA : a.wqB) + (size_t)r0 * 8192 + (cb & 127) * 64 + (lane & 3) * 16;
#pragma unroll
    for (int i = 0; i < 6; ++i) Wq[i] = __builtin_nontemporal_load((const GAS v4u*)(wp + i * 8192));
    X0 = *(const GAS v3u*)xp; X1 = *(const GAS v3u*)(xp + 1152);
  } else if (w == 8) {
    ep = __builtin_amdgcn_raw_buffer_load_b64(ws, W_X, 0, SC1);
    if (cb >= 128 && (cb & 3) == 0 && lane == 0) __builtin_amdgcn_raw_buffer_store_b32(0u, ws, W_CTR + ((cb - 128) >> 2) * 256, 0, SC1);
    for (int i = lane; i < C_WORDS; i += 64) ctl[i] = i == C_END ? 0x7fffffffu : i >= C_TICK ? 0xffffffffu : 0u;
  }
  asm volatile("s_waitcnt lgkmcnt(0)\n\ts_barrier" ::: "memory");

  int c_begin = 0, c_end = 0;
  if (qwave) {
    float ac[2][8];
#pragma unroll
    for (int k = 0; k < 8; ++k) { ac[0][k] = 0.f; ac[1][k] = 0.f; }
#pragma unroll
    for (int s = 0; s < 3; ++s)
#pragma unroll
      for (int d = 0; d < 4; ++d) {
        const unsigned lo = __builtin_amdgcn_perm(Wq[2 * s + 1][d], Wq[2 * s][d], 0x05040100u), hi = __builtin_amdgcn_perm(Wq[2 * s + 1][d], Wq[2 * s][d], 0x07060302u);
        ac[0][2 * d] = dot2(lo, X0[s], ac[0][2 * d]); ac[0][2 * d + 1] = dot2(hi, X0[s], ac[0][2 * d + 1]);
        ac[1][2 * d] = dot2(lo, X1[s], ac[1][2 * d]); ac[1][2 * d + 1] = dot2(hi, X1[s], ac[1][2 * d + 1]);
      }
    float b8[8], c4[4];
#pragma unroll
    for (int k = 0; k < 8; ++k) b8[k] = rs32(ac[0][k], ac[1][k]);
#pragma unroll
    for (int k = 0; k < 4; ++k) { float t = rs16(b8[k], b8[k + 4]); t += dpp<0x128>(t); t += dpp<0x124>(t); c4[k] = t; }
    if ((lane & 12) == 0) *(LAS v4f*)(L + L_QP + qw * 256 + (lane >> 5) * 128 + (lane & 3) * 32 + ((lane >> 4) & 1) * 16) = v4f{c4[0], c4[1], c4[2], c4[3]};
    lgkm0();
    if (lane == 0) __hip_atomic_fetch_add((LAS unsigned*)(L + L_CTL) + C_QCNT, 1u, __ATOMIC_RELAXED, __HIP_MEMORY_SCOPE_WORKGROUP);
  }

  if (w < 4) {
    if (!wait_ge(ctl, C_QRDY, 1, 1)) return;
    if (w == 0) ST(3);
    const unsigned ta = rfl(ctl[C_TAGA]), tb = rfl(ctl[C_TAGB]);
    const LAS v4u* qp = (const LAS v4u*)(L + L_Q) + (lane & 15);
    {
      const v4u q[2] = {qp[0], qp[16]};
      St<2> s; st_init(s);
      for (int n = 0; n < 4; ++n) if (!unit<2>(L, ctl, n, w, lane, q, s)) return;
      put_rec(L + L_REC + (0 * 4 + w) * REC_B, lane, s.m[0], s.l[0], s.acc[0]);
      put_rec(L + L_REC + (1 * 4 + w) * REC_B, lane, s.m[1], s.l[1], s.acc[1]);
      lgkm0();
      unsigned old = 0;
      if (lane == 0) old = __hip_atomic_fetch_add((LAS unsigned*)(L + L_CTL) + C_CNT, 1u, __ATOMIC_RELAXED, __HIP_MEMORY_SCOPE_WORKGROUP);
      if (rfl(old) == 3) {
        asm volatile("" ::: "memory");
        pub_rec(L + L_REC, ws, g * 2, j, lane, ta, tb);
        pub_rec(L + L_REC + 4 * REC_B, ws, g * 2 + 1, j, lane, ta, tb);
        ST(4);
      }
    }
    int n = 4;
#pragma unroll 1
    for (int b = 0; b < 2; ++b) {
      const v4u q[1] = {qp[32 + 16 * b]};
      St<1> s; st_init(s);
      const int nend = b == 0 ? 20 : 0x7fffffff;
      for (; n < nend; ++n) if (!unit<1>(L, ctl, n, w, lane, q, s)) break;
      if (w == 0 && b == 1) ST(6);
      put_rec(L + L_REC + ((2 + b) * 4 + w) * REC_B, lane, s.m[0], s.l[0], s.acc[0]);
      lgkm0();
      unsigned old = 0;
      if (lane == 0) old = __hip_atomic_fetch_add((LAS unsigned*)(L + L_CTL) + C_CNT + 1 + b, 1u, __ATOMIC_RELAXED, __HIP_MEMORY_SCOPE_WORKGROUP);
      if (rfl(old) == 3) {
        asm volatile("" ::: "memory");
        pub_rec(L + L_REC + (2 + b) * 4 * REC_B, ws, 64 + b * 32 + g, j, lane, ta, tb);
        ST(5 + 2 * b);
      }
    }
    return;
  } else if (w < 6) {
    const int v = w - 4;
    const __amdgpu_buffer_rsrc_t rA = mkrs(v ? a.vA : a.kA, 64u << 20), rB = mkrs(v ? a.vB : a.kB, 128u << 20);
    const unsigned lbase = (unsigned)(uintptr_t)L + v * (RU * 4096), voff = lane * 16;
    int u = 0, pub = 0, n = 0;
    for (;; ++n) {
      unsigned soff;
      if (n < 4) soff = g * (2u << 20) + (3u << 19) + (n * 8 + j) * 16384;
      else if (n < 20) soff = g * (2u << 20) + ((n - 4) * 8 + j) * 16384;
      else if (n < NS) soff = (32 + g) * (2u << 20) + ((n - 20) * 8 + j) * 16384;
      else {
        unsigned t, sp = 0;
        while ((t = rfl(ctl[C_TICK + n - NS])) == 0xffffffffu && ++sp < SPIN_CAP) __builtin_amdgcn_s_sleep(1);
        if (t >= (unsigned)POOL) break;
        soff = (32 + g) * (2u << 20) + (NSTAT * 8 + t) * 16384;
      }
      if (v == 0 && n + AHEAD >= NS) ctl[C_WANT] = n + AHEAD + 1 - NS;
#pragma unroll
      for (int q = 0; q < 4; ++q, ++u) {
        if (n >= RU / 4) {
          const unsigned need = n - RU / 4 + 1;
          if (rfl(ctl[C_DONE + q]) < need) {
            waitvm<0>(); pub = u; ctl[v] = pub;
            if (!wait_ge(ctl, C_DONE + q, need, 1)) return;
          }
        }
        if (n < 4) dma4(rA, voff, soff + q * 4096, lbase + (u % RU) * 4096);
        else dma4(rB, voff, soff + q * 4096, lbase + (u % RU) * 4096);
        waitvm<4 * WINU>();
        if (u + 1 - WINU > pub) { pub = u + 1 - WINU; ctl[v] = pub; }
      }
    }
    if (v == 0) { ctl[C_END] = n; ST(8); }
#define DRAIN(k) if (WINU > k) { waitvm<4 * k>(); if (u - k > pub) { pub = u - k; ctl[v] = pub; } }
    DRAIN(11) DRAIN(10) DRAIN(9) DRAIN(8) DRAIN(7) DRAIN(6) DRAIN(5) DRAIN(4) DRAIN(3) DRAIN(2) DRAIN(1) DRAIN(0)
#undef DRAIN
    ST(9 + v);
    return;
  } else if (w < 8) {
    const int hb = w - 6;
    const int head = hb ? g : 2 * (g & 15) + (g >> 4);
    const unsigned char* wo = (hb ? a.woB : a.woA) + (size_t)(head * 128 + j * 16) * 1152;
    unsigned Wo[8][10];
#pragma unroll
    for (int s = 0; s < 8; ++s) {
#pragma unroll
      for (int k = 0; k < 5; ++k) {
        const int off = k < 4 ? k * 256 + lane * 4 : 1024 + (lane & 31) * 4;
        const unsigned r0 = __builtin_nontemporal_load((const GAS unsigned*)(wo + (2 * s) * 1152 + off));
        const unsigned r1 = __builtin_nontemporal_load((const GAS unsigned*)(wo + (2 * s + 1) * 1152 + off));
        Wo[s][2 * k] = __builtin_amdgcn_perm(r1, r0, 0x05040100u); Wo[s][2 * k + 1] = __builtin_amdgcn_perm(r1, r0, 0x07060302u);
      }
    }
    if (!wait_ge(ctl, C_QRDY, 1, 8)) return;
    const unsigned ta = rfl(ctl[C_TAGA]), tb = rfl(ctl[C_TAGB]);
#pragma unroll 1
    for (int b = 0; b < 2; ++b) {
      if (hb == 0) { if (!wait_ge(ctl, C_DONE, 8, 8)) return; }
      else if (b == 0) { if (!wait_ge(ctl, C_DONE, NS < 26 ? NS : 26, 8)) return; }
      else { if (!wait_ge(ctl, C_CNT + 2, 4, 1)) return; }
      const int tgt = hb ? 64 + b * 32 + g : (b * 16 + (g & 15)) * 2 + (g >> 4);
      const int base = W_P + tgt * P_STR;
      v4u d, ml;
      for (unsigned sp = 0;; ++sp) {
        d = gld(ws, base + j * 1024 + lane * 16);
        ml = gld(ws, base + 8192 + lane * 16);
        if (__all(gok(d, ta, tb) && gok(ml, ta, tb))) break;
        if (sp > SPIN_CAP) return;
        if (!(hb && b)) __builtin_amdgcn_s_sleep(32);
      }
      if (hb && b) ST(11);
      float M = fbits(ml.x); M = fmaxf(M, dpp<0x128>(M)); M = xmax(M);
      const float f = ex2(fbits(ml.x) - M);
      float Ls = f * fbits(ml.z), o0 = f * fbits(d.x), o1 = f * fbits(d.z);
      Ls += dpp<0x128>(Ls); o0 += dpp<0x128>(o0); o1 += dpp<0x128>(o1);
      Ls = xsum(Ls); o0 = xsum(o0); o1 = xsum(o1);
      const float inv = 1.0f / Ls;
      const unsigned op = pk_bf16(o0 * inv, o1 * inv);
      float y[10];
#pragma unroll
      for (int k = 0; k < 10; ++k) y[k] = 0.f;
#pragma unroll
      for (int s = 0; s < 8; ++s) {
        const unsigned os = __builtin_amdgcn_readlane(op, s);
#pragma unroll
        for (int k = 0; k < 10; ++k) y[k] = dot2(Wo[s][k], os, y[k]);
      }
      const int yb = W_Y + ((hb * 2 + b) * 256 + wg) * Y_STR + lane * 16;
#pragma unroll
      for (int k = 0; k < 4; ++k) gst(ws, yb + k * 1024, ubits(y[2 * k]), ubits(y[2 * k + 1]), ta, tb);
      if (lane < 32) gst(ws, yb + 4096, ubits(y[8]), ubits(y[9]), ta, tb);
      if (hb && b) { ctl[C_YPUB] = 1; ST(12); }
    }
    if (hb) return;
    c_begin = 0; c_end = 3;
  } else {
    const unsigned ta = rfl(ep.x) + 1u, tb = rfl(ep.y) + 1u;
    waitvm<0>();
    if (!wait_ge(ctl, C_QCNT, 6, 1)) return;
    ST(1);
    if (lane < 16) {
      v4f s = {0.f, 0.f, 0.f, 0.f};
#pragma unroll
      for (int i = 0; i < 6; ++i) s += *(LAS v4f*)(L + L_QP + i * 256 + lane * 16);
      const int b = lane >> 3, quad = lane & 7;
      const int off = cb < 128 ? W_QA + ((b * 16 + (cb >> 3)) * 64 + (cb & 7) * 8 + quad) * 16 : W_QB + (((cb - 128) >> 2) * 64 + b * 32 + (cb & 3) * 8 + quad) * 16;
      gst(ws, off, pk_bf16(s.x, s.y), pk_bf16(s.z, s.w), ta, tb);
    }
    v4u qa, qb;
    for (unsigned sp = 0;; ++sp) {
      qa = gld(ws, W_QA + g * 1024 + lane * 16);
      qb = gld(ws, W_QB + g * 1024 + lane * 16);
      if (__all(gok(qa, ta, tb) && gok(qb, ta, tb))) break;
      if (sp > SPIN_CAP) return;
      __builtin_amdgcn_s_sleep(2);
    }
    *(LAS v2u*)(L + L_Q + lane * 8) = v2u{qa.x, qa.z};
    *(LAS v2u*)(L + L_Q + 512 + lane * 8) = v2u{qb.x, qb.z};
    ctl[C_TAGA] = ta; ctl[C_TAGB] = tb;
    lgkm0();
    ctl[C_QRDY] = 1;
    ST(2);
    for (int k = 0; k <= POOL; ++k) {
      if (!wait_ge(ctl, C_WANT, k + 1, 1)) return;
      unsigned t = 0;
      if (lane == 0) t = __hip_atomic_fetch_add((GAS unsigned*)(a.ws + W_CTR + g * 256), 1u, __ATOMIC_RELAXED, __HIP_MEMORY_SCOPE_AGENT);
      t = rfl(t);
      ctl[C_TICK + k] = t;
      if (t >= (unsigned)POOL) break;
    }
    c_begin = 3; c_end = 4;
  }
  if (wg >= 144) return;
  const unsigned ta = rfl(ctl[C_TAGA]), tb = rfl(ctl[C_TAGB]);
#pragma unroll 1
  for (int c = c_begin; c < c_end; ++c) {
    if (c < 3) { if (!wait_ge(ctl, C_DONE, NS, 8)) return; }
    else { if (!wait_ge(ctl, C_YPUB, 1, 1)) return; }
    const int base = W_Y + (c * 256 + (lane >> 1)) * Y_STR + wg * 32 + (lane & 1) * 16;
    float s0, s1;
    for (unsigned sp = 0;; ++sp) {
      v4u y[8];
#pragma unroll
      for (int i = 0; i < 8; ++i) y[i] = gld(ws, base + i * 32 * Y_STR);
      bool ok = true; s0 = 0.f; s1 = 0.f;
#pragma unroll
      for (int i = 0; i < 8; ++i) { ok &= gok(y[i], ta, tb); s0 += fbits(y[i].x); s1 += fbits(y[i].z); }
      if (__all(ok)) break;
      if (sp > SPIN_CAP) return;
      if (c < 3) __builtin_amdgcn_s_sleep(32);
    }
    s0 += dpp<0x4E>(s0); s1 += dpp<0x4E>(s1);
    s0 += dpp<0x124>(s0); s1 += dpp<0x124>(s1);
    s0 += dpp<0x128>(s0); s1 += dpp<0x128>(s1);
    s0 = xsum(s0); s1 = xsum(s1);
    if (lane < 2) *(GAS unsigned*)(a.out + ((c & 1) * 1152 + (c >> 1) * 576 + wg * 4 + lane * 2) * 2) = pk_bf16(s0, s1);
    if (c == 3) {
      if (wg == 0 && lane == 0) __builtin_amdgcn_raw_buffer_store_b64(v2u{ta, tb}, ws, W_X, 0, SC1);
      ST(13);
    }
  }
}

extern "C" void kernel_launch(void* const* d_in, const int* in_sizes, int n_in, void* d_out, int out_size, void* d_ws, size_t ws_size,
                              hipStream_t stream) {
  Args a;
  a.xA = (const unsigned char*)d_in[0]; a.kA = (const unsigned char*)d_in[1]; a.vA = (const unsigned char*)d_in[2];
  a.wqA = (const unsigned char*)d_in[3]; a.woA = (const unsigned char*)d_in[4];
  a.xB = (const unsigned char*)d_in[5]; a.kB = (const unsigned char*)d_in[6]; a.vB = (const unsigned char*)d_in[7];
  a.wqB = (const unsigned char*)d_in[8]; a.woB = (const unsigned char*)d_in[9];
  a.out = (unsigned char*)d_out; a.ws = (unsigned char*)d_ws;
  fused_decode<<<256, NTHR, 0, stream>>>(a);
}
```

```cpp
#include <hip/hip_runtime.h>
#include <stdint.h>
#include <stdio.h>

typedef unsigned v4u __attribute__((ext_vector_type(4)));
typedef unsigned v3u __attribute__((ext_vector_type(3)));
typedef unsigned v2u __attribute__((ext_vector_type(2)));
typedef float v4f __attribute__((ext_vector_type(4)));
typedef float v2f __attribute__((ext_vector_type(2)));
typedef __bf16 bf16x2 __attribute__((ext_vector_type(2)));
#define LAS __attribute__((address_space(3)))
#define GAS __attribute__((address_space(1)))
#define DI __device__ __forceinline__

#define NSTAT 8
#define WINU 4
#define AHEAD 2
constexpr int NWAVE = 9, NTHR = NWAVE * 64;
constexpr int RU = 16;
constexpr int NS = 4 + 16 + NSTAT;
constexpr int POOL = (16 - NSTAT) * 8;
constexpr unsigned SPIN_CAP = 1u << 20;
constexpr int L_RK = 0, L_RV = RU * 4096, L_CTL = 2 * RU * 4096, L_Q = L_CTL + 1024, L_QP = L_Q + 1024, L_REC = L_QP + 1536, REC_B = 528,
              L_END = L_REC + 16 * REC_B;
enum { C_FULLK = 0, C_FULLV = 1, C_END = 2, C_QRDY = 3, C_DONE = 4, C_WANT = 8, C_QCNT = 9, C_TAGA = 10, C_TAGB = 11, C_CNT = 12, C_YPUB = 15, C_TICK = 16, C_WORDS = 16 + POOL + 8 };
static_assert(C_WORDS * 4 <= 1024, "");
constexpr int W_X = 0, W_CTR = 4096, W_QA = 16384, W_QB = W_QA + 32768, W_P = W_QB + 32768, P_STR = 9216, W_Y = W_P + 128 * P_STR, Y_STR = 4608,
              W_ENDB = W_Y + 4 * 256 * Y_STR, W_STAMP = (W_ENDB + 4095) / 4096 * 4096;
constexpr float SCALE = 0.088388347648318440f, L2E = 1.4426950408889634f;

struct Args {
  const unsigned char *xA, *kA, *vA, *wqA, *woA, *xB, *kB, *vB, *wqB, *woB;
  unsigned char *out, *ws;
};

DI float dot2(unsigned a, unsigned b, float c) { return __builtin_amdgcn_fdot2_f32_bf16(__builtin_bit_cast(bf16x2, a), __builtin_bit_cast(bf16x2, b), c, false); }
template <int CTRL> DI float dpp(float x) { return __builtin_bit_cast(float, __builtin_amdgcn_mov_dpp(__builtin_bit_cast(int, x), CTRL, 0xf, 0xf, true)); }
DI float rowsum16(float x) { x += dpp<0x128>(x); x += dpp<0x124>(x); x += dpp<0x122>(x); x += dpp<0x121>(x); return x; }
DI unsigned pk_bf16(float lo, float hi) { return __builtin_bit_cast(unsigned, bf16x2{(__bf16)lo, (__bf16)hi}); }
DI float bf_lo(unsigned u) { return __builtin_bit_cast(float, u << 16); }
DI float bf_hi(unsigned u) { return __builtin_bit_cast(float, u & 0xffff0000u); }
DI float fbits(unsigned u) { return __builtin_bit_cast(float, u); }
DI unsigned ubits(float f) { return __builtin_bit_cast(unsigned, f); }
DI unsigned rfl(unsigned x) { return __builtin_amdgcn_readfirstlane(x); }
DI float ex2(float x) { return __builtin_amdgcn_exp2f(x); }
DI float rs32(float x, float y) { auto s = __builtin_amdgcn_permlane32_swap(ubits(x), ubits(y), false, false); return fbits(s[0]) + fbits(s[1]); }
DI float rs16(float x, float y) { auto s = __builtin_amdgcn_permlane16_swap(ubits(x), ubits(y), false, false); return fbits(s[0]) + fbits(s[1]); }
DI float xsum(float x) { x = rs16(x, x); return rs32(x, x); }
DI float xmax(float x) {
  auto s = __builtin_amdgcn_permlane16_swap(ubits(x), ubits(x), false, false); x = fmaxf(fbits(s[0]), fbits(s[1]));
  auto t = __builtin_amdgcn_permlane32_swap(ubits(x), ubits(x), false, false); return fmaxf(fbits(t[0]), fbits(t[1]));
}
DI void lgkm0() { asm volatile("s_waitcnt lgkmcnt(0)" ::: "memory"); }
template <int N> DI void waitvm() { asm volatile("s_waitcnt vmcnt(%0)" ::"n"(N) : "memory"); }
DI __amdgpu_buffer_rsrc_t mkrs(const void* p, unsigned bytes) { return __builtin_amdgcn_make_buffer_rsrc((void*)p, 0, bytes, 0x00020000); }
DI void dma4(__amdgpu_buffer_rsrc_t rs, unsigned voff, unsigned soff, unsigned lds) {
  unsigned keep;
  asm volatile("s_mov_b32 %0, m0\n\ts_mov_b32 m0, %4\n\ts_nop 3\n\t"
               "buffer_load_dwordx4 %1, %2, %3 offen nt lds\n\t"
               "buffer_load_dwordx4 %1, %2, %3 offen offset:1024 nt lds\n\t"
               "buffer_load_dwordx4 %1, %2, %3 offen offset:2048 nt lds\n\t"
               "buffer_load_dwordx4 %1, %2, %3 offen offset:3072 nt lds\n\t"
               "s_mov_b32 m0, %0"
               : "=&s"(keep) : "v"(voff), "s"(rs), "s"(soff), "s"(lds) : "memory");
}
constexpr int SC1 = 16, NT = 2;
DI v4u gld(__amdgpu_buffer_rsrc_t rs, int off) { return __builtin_amdgcn_raw_buffer_load_b128(rs, off, 0, SC1); }
DI void gst(__amdgpu_buffer_rsrc_t rs, int off, unsigned a, unsigned b, unsigned ta, unsigned tb) { __builtin_amdgcn_raw_buffer_store_b128(v4u{a, ta, b, tb}, rs, off, 0, SC1); }
DI bool gok(v4u g, unsigned ta, unsigned tb) { return g.y == ta && g.w == tb; }

#define ST(i) do { } while (0)

typedef volatile LAS unsigned* ctl_t;
DI bool wait_ge(ctl_t ctl, int idx, unsigned v, int slp) {
  for (unsigned s = 0; rfl(ctl[idx]) < v; ++s) {
    if (s > SPIN_CAP) return false;
    if (slp == 1) __builtin_amdgcn_s_sleep(1); else __builtin_amdgcn_s_sleep(8);
  }
  asm volatile("" ::: "memory");
  return true;
}

template <int G> struct St { float m[G], l[G], acc[G][8]; };
template <int G> DI void st_init(St<G>& s) {
#pragma unroll
  for (int g = 0; g < G; ++g) { s.m[g] = -INFINITY; s.l[g] = 0.f;
#pragma unroll
    for (int k = 0; k < 8; ++k) s.acc[g][k] = 0.f; }
}
template <int G> DI bool unit(LAS unsigned char* L, ctl_t ctl, int n, int cw, int lane, const v4u (&q)[G], St<G>& s) {
  const unsigned u = n * 4 + cw;
  for (unsigned sp = 0; rfl(ctl[C_FULLK]) <= u; ++sp) {
    if ((unsigned)n >= rfl(ctl[C_END]) || sp > SPIN_CAP) return false;
    __builtin_amdgcn_s_sleep(1);
  }
  asm volatile("" ::: "memory");
  const LAS v4u* kp = (const LAS v4u*)(L + L_RK + (u % RU) * 4096) + lane;
  v4u K[4], V[4];
#pragma unroll
  for (int t = 0; t < 4; ++t) K[t] = kp[t * 64];
  float p[G][4], al[G];
#pragma unroll
  for (int g = 0; g < G; ++g) {
    float d[4];
#pragma unroll
    for (int t = 0; t < 4; ++t) {
      float x = dot2(K[t].x, q[g].x, 0.f); x = dot2(K[t].y, q[g].y, x); x = dot2(K[t].z, q[g].z, x); x = dot2(K[t].w, q[g].w, x);
      d[t] = rowsum16(x);
    }
    const unsigned a01 = pk_bf16(d[0], d[1]), a23 = pk_bf16(d[2], d[3]);
    const unsigned b01 = pk_bf16(bf_lo(a01) * SCALE, bf_hi(a01) * SCALE), b23 = pk_bf16(bf_lo(a23) * SCALE, bf_hi(a23) * SCALE);
    d[0] = bf_lo(b01) * L2E; d[1] = bf_hi(b01) * L2E; d[2] = bf_lo(b23) * L2E; d[3] = bf_hi(b23) * L2E;
    const float mx = fmaxf(fmaxf(fmaxf(d[0], d[1]), fmaxf(d[2], d[3])), s.m[g]);
    al[g] = ex2(s.m[g] - mx);
    s.m[g] = mx;
#pragma unroll
    for (int t = 0; t < 4; ++t) p[g][t] = ex2(d[t] - mx);
    s.l[g] = s.l[g] * al[g] + ((p[g][0] + p[g][1]) + (p[g][2] + p[g][3]));
#pragma unroll
    for (int k = 0; k < 8; ++k) s.acc[g][k] *= al[g];
  }
  if (!wait_ge(ctl, C_FULLV, u + 1, 1)) return false;
  const LAS v4u* vp = (const LAS v4u*)(L + L_RV + (u % RU) * 4096) + lane;
#pragma unroll
  for (int t = 0; t < 4; ++t) V[t] = vp[t * 64];
  lgkm0();
  ctl[C_DONE + cw] = n + 1;
  asm volatile("" : "+v"(V[0]), "+v"(V[1]), "+v"(V[2]), "+v"(V[3])::"memory");
#pragma unroll
  for (int t = 0; t < 4; ++t) {
#pragma unroll
    for (int k = 0; k < 4; ++k) {
      const float lo = bf_lo(V[t][k]), hi = bf_hi(V[t][k]);
#pragma unroll
      for (int g = 0; g < G; ++g) { s.acc[g][2 * k] = fmaf(p[g][t], lo, s.acc[g][2 * k]); s.acc[g][2 * k + 1] = fmaf(p[g][t], hi, s.acc[g][2 * k + 1]); }
    }
  }
  return true;
}
DI void put_rec(LAS unsigned char* rec, int lane, float m, float l, const float (&acc)[8]) {
  const float M = xmax(m), f = ex2(m - M);
  const float Ls = xsum(l * f);
  float b[4];
#pragma unroll
  for (int k = 0; k < 4; ++k) b[k] = rs32(acc[k] * f, acc[k + 4] * f);
  const float c0 = rs16(b[0], b[2]), c1 = rs16(b[1], b[3]);
  *(LAS v2f*)(rec + 16 + ((lane & 15) * 8 + (lane >> 4) * 2) * 4) = v2f{c0, c1};
  if (lane == 0) *(LAS v2f*)rec = v2f{M, Ls};
}
DI void pub_rec(LAS unsigned char* rec, __amdgpu_buffer_rsrc_t ws, int tgt, int j, int lane, unsigned ta, unsigned tb) {
  v2f ml[4], av[4];
#pragma unroll
  for (int w = 0; w < 4; ++w) { ml[w] = *(LAS v2f*)(rec + w * REC_B); av[w] = *(LAS v2f*)(rec + w * REC_B + 16 + lane * 8); }
  const float M = fmaxf(fmaxf(ml[0].x, ml[1].x), fmaxf(ml[2].x, ml[3].x));
  float Ls = 0.f, a0 = 0.f, a1 = 0.f;
#pragma unroll
  for (int w = 0; w < 4; ++w) { const float f = ex2(ml[w].x - M); Ls = fmaf(f, ml[w].y, Ls); a0 = fmaf(f, av[w].x, a0); a1 = fmaf(f, av[w].y, a1); }
  const int base = W_P + tgt * P_STR;
  gst(ws, base + (lane >> 3) * 1024 + j * 128 + (lane & 7) * 16, ubits(a0), ubits(a1), ta, tb);
  if (lane < 8) gst(ws, base + 8192 + j * 128 + lane * 16, ubits(M), ubits(Ls), ta, tb);
}

__global__ void __launch_bounds__(NTHR) fused_decode(Args a) {
  __shared__ __attribute__((aligned(16))) unsigned char smem[L_END];
  LAS unsigned char* L = (LAS unsigned char*)smem;
  ctl_t ctl = (ctl_t)(L + L_CTL);
  const int wg = blockIdx.x, g = wg >> 3, j = wg & 7;
  const int lane = threadIdx.x & 63, w = rfl(threadIdx.x >> 6);
  const __amdgpu_buffer_rsrc_t ws = mkrs(a.ws, W_ENDB);
  const int cb = (wg & ~15) + ((wg & 7) << 1) + ((wg >> 3) & 1);
  ST(w == 8 ? 0 : 31);
  const bool qwave = w < 4 || w == 6 || w == 7;
  const int qw = w < 4 ? w : w - 2;
  v4u Wq[6]; v3u X0, X1; v2u ep;
  if (qwave) {
    const int r0 = qw * 96 + (lane >> 2) * 6;
    const unsigned char* xp = (cb < 128 ? a.xA : a.xB) + r0 * 2;
    const unsigned char* wp = (cb < 128 ? a.wqA : a.wqB) + (size_t)r0 * 8192 + (cb & 127) * 64 + (lane & 3) * 16;
#pragma unroll
    for (int i = 0; i < 6; ++i) Wq[i] = __builtin_nontemporal_load((const GAS v4u*)(wp + i * 8192));
    X0 = *(const GAS v3u*)xp; X1 = *(const GAS v3u*)(xp + 1152);
  } else if (w == 8) {
    ep = __builtin_amdgcn_raw_buffer_load_b64(ws, W_X, 0, SC1);
    if (cb >= 128 && (cb & 3) == 0 && lane == 0) __builtin_amdgcn_raw_buffer_store_b32(0u, ws, W_CTR + ((cb - 128) >> 2) * 256, 0, SC1);
    for (int i = lane; i < C_WORDS; i += 64) ctl[i] = i == C_END ? 0x7fffffffu : i >= C_TICK ? 0xffffffffu : 0u;
  }
  asm volatile("s_waitcnt lgkmcnt(0)\n\ts_barrier" ::: "memory");

  int c_begin = 0, c_end = 0;
  if (qwave) {
    float ac[2][8];
#pragma unroll
    for (int k = 0; k < 8; ++k) { ac[0][k] = 0.f; ac[1][k] = 0.f; }
#pragma unroll
    for (int s = 0; s < 3; ++s)
#pragma unroll
      for (int d = 0; d < 4; ++d) {
        const unsigned lo = __builtin_amdgcn_perm(Wq[2 * s + 1][d], Wq[2 * s][d], 0x05040100u), hi = __builtin_amdgcn_perm(Wq[2 * s + 1][d], Wq[2 * s][d], 0x07060302u);
        ac[0][2 * d] = dot2(lo, X0[s], ac[0][2 * d]); ac[0][2 * d + 1] = dot2(hi, X0[s], ac[0][2 * d + 1]);
        ac[1][2 * d] = dot2(lo, X1[s], ac[1][2 * d]); ac[1][2 * d + 1] = dot2(hi, X1[s], ac[1][2 * d + 1]);
      }
    float b8[8], c4[4];
#pragma unroll
    for (int k = 0; k < 8; ++k) b8[k] = rs32(ac[0][k], ac[1][k]);
#pragma unroll
    for (int k = 0; k < 4; ++k) { float t = rs16(b8[k], b8[k + 4]); t += dpp<0x128>(t); t += dpp<0x124>(t); c4[k] = t; }
    if ((lane & 12) == 0) *(LAS v4f*)(L + L_QP + qw * 256 + (lane >> 5) * 128 + (lane & 3) * 32 + ((lane >> 4) & 1) * 16) = v4f{c4[0], c4[1], c4[2], c4[3]};
    lgkm0();
    if (lane == 0) __hip_atomic_fetch_add((LAS unsigned*)(L + L_CTL) + C_QCNT, 1u, __ATOMIC_RELAXED, __HIP_MEMORY_SCOPE_WORKGROUP);
  }

  if (w < 4) {
    if (!wait_ge(ctl, C_QRDY, 1, 1)) return;
    if (w == 0) ST(3);
    const unsigned ta = rfl(ctl[C_TAGA]), tb = rfl(ctl[C_TAGB]);
    const LAS v4u* qp = (const LAS v4u*)(L + L_Q) + (lane & 15);
    {
      const v4u q[2] = {qp[0], qp[16]};
      St<2> s; st_init(s);
      for (int n = 0; n < 4; ++n) if (!unit<2>(L, ctl, n, w, lane, q, s)) return;
      put_rec(L + L_REC + (0 * 4 + w) * REC_B, lane, s.m[0], s.l[0], s.acc[0]);
      put_rec(L + L_REC + (1 * 4 + w) * REC_B, lane, s.m[1], s.l[1], s.acc[1]);
      lgkm0();
      unsigned old = 0;
      if (lane == 0) old = __hip_atomic_fetch_add((LAS unsigned*)(L + L_CTL) + C_CNT, 1u, __ATOMIC_RELAXED, __HIP_MEMORY_SCOPE_WORKGROUP);
      if (rfl(old) == 3) {
        asm volatile("" ::: "memory");
        pub_rec(L + L_REC, ws, g * 2, j, lane, ta, tb);
        pub_rec(L + L_REC + 4 * REC_B, ws, g * 2 + 1, j, lane, ta, tb);
        ST(4);
      }
    }
    int n = 4;
#pragma unroll 1
    for (int b = 0; b < 2; ++b) {
      const v4u q[1] = {qp[32 + 16 * b]};
      St<1> s; st_init(s);
      const int nend = b == 0 ? 20 : 0x7fffffff;
      for (; n < nend; ++n) if (!unit<1>(L, ctl, n, w, lane, q, s)) break;
      if (w == 0 && b == 1) ST(6);
      put_rec(L + L_REC + ((2 + b) * 4 + w) * REC_B, lane, s.m[0], s.l[0], s.acc[0]);
      lgkm0();
      unsigned old = 0;
      if (lane == 0) old = __hip_atomic_fetch_add((LAS unsigned*)(L + L_CTL) + C_CNT + 1 + b, 1u, __ATOMIC_RELAXED, __HIP_MEMORY_SCOPE_WORKGROUP);
      if (rfl(old) == 3) {
        asm volatile("" ::: "memory");
        pub_rec(L + L_REC + (2 + b) * 4 * REC_B, ws, 64 + b * 32 + g, j, lane, ta, tb);
        ST(5 + 2 * b);
      }
    }
    return;
  } else if (w < 6) {
    const int v = w - 4;
    const __amdgpu_buffer_rsrc_t rA = mkrs(v ? a.vA : a.kA, 64u << 20), rB = mkrs(v ? a.vB : a.kB, 128u << 20);
    const unsigned lbase = (unsigned)(uintptr_t)L + v * (RU * 4096), voff = lane * 16;
    int u = 0, pub = 0, n = 0;
    for (;; ++n) {
      unsigned soff;
      if (n < 4) soff = g * (2u << 20) + (3u << 19) + (n * 8 + j) * 16384;
      else if (n < 20) soff = g * (2u << 20) + ((n - 4) * 8 + j) * 16384;
      else if (n < NS) soff = (32 + g) * (2u << 20) + ((n - 20) * 8 + j) * 16384;
      else {
        unsigned t, sp = 0;
        while ((t = rfl(ctl[C_TICK + n - NS])) == 0xffffffffu && ++sp < SPIN_CAP) __builtin_amdgcn_s_sleep(1);
        if (t >= (unsigned)POOL) break;
        soff = (32 + g) * (2u << 20) + (NSTAT * 8 + t) * 16384;
      }
      if (v == 0 && n + AHEAD >= NS) ctl[C_WANT] = n + AHEAD + 1 - NS;
#pragma unroll
      for (int q = 0; q < 4; ++q, ++u) {
        if (n >= RU / 4) {
          const unsigned need = n - RU / 4 + 1;
          if (rfl(ctl[C_DONE + q]) < need) {
            waitvm<0>(); pub = u; ctl[v] = pub;
            if (!wait_ge(ctl, C_DONE + q, need, 1)) return;
          }
        }
        if (n < 4) dma4(rA, voff, soff + q * 4096, lbase + (u % RU) * 4096);
        else dma4(rB, voff, soff + q * 4096, lbase + (u % RU) * 4096);
        waitvm<4 * WINU>();
        if (u + 1 - WINU > pub) { pub = u + 1 - WINU; ctl[v] = pub; }
      }
    }
    if (v == 0) { ctl[C_END] = n; ST(8); }
#define DRAIN(k) if (WINU > k) { waitvm<4 * k>(); if (u - k > pub) { pub = u - k; ctl[v] = pub; } }
    DRAIN(11) DRAIN(10) DRAIN(9) DRAIN(8) DRAIN(7) DRAIN(6) DRAIN(5) DRAIN(4) DRAIN(3) DRAIN(2) DRAIN(1) DRAIN(0)
#undef DRAIN
    ST(9 + v);
    return;
  } else if (w < 8) {
    const int hb = w - 6;
    const int head = hb ? g : 2 * (g & 15) + (g >> 4);
    const unsigned char* wo = (hb ? a.woB : a.woA) + (size_t)(head * 128 + j * 16) * 1152;
    unsigned Wo[8][10];
#pragma unroll
    for (int s = 0; s < 8; ++s) {
#pragma unroll
      for (int k = 0; k < 5; ++k) {
        const int off = k < 4 ? k * 256 + lane * 4 : 1024 + (lane & 31) * 4;
        const unsigned r0 = __builtin_nontemporal_load((const GAS unsigned*)(wo + (2 * s) * 1152 + off));
        const unsigned r1 = __builtin_nontemporal_load((const GAS unsigned*)(wo + (2 * s + 1) * 1152 + off));
        Wo[s][2 * k] = __builtin_amdgcn_perm(r1, r0, 0x05040100u); Wo[s][2 * k + 1] = __builtin_amdgcn_perm(r1, r0, 0x07060302u);
      }
    }
    if (!wait_ge(ctl, C_QRDY, 1, 8)) return;
    const unsigned ta = rfl(ctl[C_TAGA]), tb = rfl(ctl[C_TAGB]);
#pragma unroll 1
    for (int b = 0; b < 2; ++b) {
      if (hb == 0) { if (!wait_ge(ctl, C_DONE, 8, 8)) return; }
      else if (b == 0) { if (!wait_ge(ctl, C_DONE, NS < 26 ? NS : 26, 8)) return; }
      else { if (!wait_ge(ctl, C_CNT + 2, 4, 1)) return; }
      const int tgt = hb ? 64 + b * 32 + g : (b * 16 + (g & 15)) * 2 + (g >> 4);
      const int base = W_P + tgt * P_STR;
      v4u d, ml;
      for (unsigned sp = 0;; ++sp) {
        d = gld(ws, base + j * 1024 + lane * 16);
        ml = gld(ws, base + 8192 + lane * 16);
        if (__all(gok(d, ta, tb) && gok(ml, ta, tb))) break;
        if (sp > SPIN_CAP) return;
        if (!(hb && b)) __builtin_amdgcn_s_sleep(32);
      }
      if (hb && b) ST(11);
      float M = fbits(ml.x); M = fmaxf(M, dpp<0x128>(M)); M = xmax(M);
      const float f = ex2(fbits(ml.x) - M);
      float Ls = f * fbits(ml.z), o0 = f * fbits(d.x), o1 = f * fbits(d.z);
      Ls += dpp<0x128>(Ls); o0 += dpp<0x128>(o0); o1 += dpp<0x128>(o1);
      Ls = xsum(Ls); o0 = xsum(o0); o1 = xsum(o1);
      const float inv = 1.0f / Ls;
      const unsigned op = pk_bf16(o0 * inv, o1 * inv);
      float y[10];
#pragma unroll
      for (int k = 0; k < 10; ++k) y[k] = 0.f;
#pragma unroll
      for (int s = 0; s < 8; ++s) {
        const unsigned os = __builtin_amdgcn_readlane(op, s);
#pragma unroll
        for (int k = 0; k < 10; ++k) y[k] = dot2(Wo[s][k], os, y[k]);
      }
      const int yb = W_Y + ((hb * 2 + b) * 256 + wg) * Y_STR + lane * 16;
#pragma unroll
      for (int k = 0; k < 4; ++k) gst(ws, yb + k * 1024, ubits(y[2 * k]), ubits(y[2 * k + 1]), ta, tb);
      if (lane < 32) gst(ws, yb + 4096, ubits(y[8]), ubits(y[9]), ta, tb);
      if (hb && b) { ctl[C_YPUB] = 1; ST(12); }
    }
    if (hb) return;
    c_begin = 0; c_end = 3;
  } else {
    const unsigned ta = rfl(ep.x) + 1u, tb = rfl(ep.y) + 1u;
    waitvm<0>();
    if (!wait_ge(ctl, C_QCNT, 6, 1)) return;
    ST(1);
    if (lane < 16) {
      v4f s = {0.f, 0.f, 0.f, 0.f};
#pragma unroll
      for (int i = 0; i < 6; ++i) s += *(LAS v4f*)(L + L_QP + i * 256 + lane * 16);
      const int b = lane >> 3, quad = lane & 7;
      const int off = cb < 128 ? W_QA + ((b * 16 + (cb >> 3)) * 64 + (cb & 7) * 8 + quad) * 16 : W_QB + (((cb - 128) >> 2) * 64 + b * 32 + (cb & 3) * 8 + quad) * 16;
      gst(ws, off, pk_bf16(s.x, s.y), pk_bf16(s.z, s.w), ta, tb);
    }
    v4u qa, qb;
    for (unsigned sp = 0;; ++sp) {
      qa = gld(ws, W_QA + g * 1024 + lane * 16);
      qb = gld(ws, W_QB + g * 1024 + lane * 16);
      if (__all(gok(qa, ta, tb) && gok(qb, ta, tb))) break;
      if (sp > SPIN_CAP) return;
      __builtin_amdgcn_s_sleep(2);
    }
    *(LAS v2u*)(L + L_Q + lane * 8) = v2u{qa.x, qa.z};
    *(LAS v2u*)(L + L_Q + 512 + lane * 8) = v2u{qb.x, qb.z};
    ctl[C_TAGA] = ta; ctl[C_TAGB] = tb;
    lgkm0();
    ctl[C_QRDY] = 1;
    ST(2);
    for (int k = 0; k <= POOL; ++k) {
      if (!wait_ge(ctl, C_WANT, k + 1, 1)) return;
      unsigned t = 0;
      if (lane == 0) t = __hip_atomic_fetch_add((GAS unsigned*)(a.ws + W_CTR + g * 256), 1u, __ATOMIC_RELAXED, __HIP_MEMORY_SCOPE_AGENT);
      t = rfl(t);
      ctl[C_TICK + k] = t;
      if (t >= (unsigned)POOL) break;
    }
    c_begin = 3; c_end = 4;
  }
  if (wg >= 144) return;
  const unsigned ta = rfl(ctl[C_TAGA]), tb = rfl(ctl[C_TAGB]);
#pragma unroll 1
  for (int c = c_begin; c < c_end; ++c) {
    if (c < 3) { if (!wait_ge(ctl, C_DONE, NS, 8)) return; }
    else { if (!wait_ge(ctl, C_YPUB, 1, 1)) return; }
    const int base = W_Y + (c * 256 + (lane >> 1)) * Y_STR + wg * 32 + (lane & 1) * 16;
    float s0, s1;
    for (unsigned sp = 0;; ++sp) {
      v4u y[8];
#pragma unroll
      for (int i = 0; i < 8; ++i) y[i] = gld(ws, base + i * 32 * Y_STR);
      bool ok = true; s0 = 0.f; s1 = 0.f;
#pragma unroll
      for (int i = 0; i < 8; ++i) { ok &= gok(y[i], ta, tb); s0 += fbits(y[i].x); s1 += fbits(y[i].z); }
      if (__all(ok)) break;
      if (sp > SPIN_CAP) return;
      if (c < 3) __builtin_amdgcn_s_sleep(32);
    }
    s0 += dpp<0x4E>(s0); s1 += dpp<0x4E>(s1);
    s0 += dpp<0x124>(s0); s1 += dpp<0x124>(s1);
    s0 += dpp<0x128>(s0); s1 += dpp<0x128>(s1);
    s0 = xsum(s0); s1 = xsum(s1);
    if (lane < 2) *(GAS unsigned*)(a.out + ((c & 1) * 1152 + (c >> 1) * 576 + wg * 4 + lane * 2) * 2) = pk_bf16(s0, s1);
    if (c == 3) {
      if (wg == 0 && lane == 0) __builtin_amdgcn_raw_buffer_store_b64(v2u{ta, tb}, ws, W_X, 0, SC1);
      ST(13);
    }
  }
}

extern "C" void kernel_launch(void* const* d_in, const int* in_sizes, int n_in, void* d_out, int out_size, void* d_ws, size_t ws_size,
                              hipStream_t stream) {
  Args a;
  a.xA = (const unsigned char*)d_in[0]; a.kA = (const unsigned char*)d_in[1]; a.vA = (const unsigned char*)d_in[2];
  a.wqA = (const unsigned char*)d_in[3]; a.woA = (const unsigned char*)d_in[4];
  a.xB = (const unsigned char*)d_in[5]; a.kB = (const unsigned char*)d_in[6]; a.vB = (const unsigned char*)d_in[7];
  a.wqB = (const unsigned char*)d_in[8]; a.woB = (const unsigned char*)d_in[9];
  a.out = (unsigned char*)d_out; a.ws = (unsigned char*)d_ws;
  for (int i = 0; i < 10; ++i) fprintf(stderr, "in%d %p ", i, d_in[i]);
  fprintf(stderr, "out %p ws %p\n", d_out, d_ws);
  fused_decode<<<256, NTHR, 0, stream>>>(a);
}
```

```cpp
#include <hip/hip_runtime.h>
#include <stdint.h>

typedef unsigned v4u __attribute__((ext_vector_type(4)));
typedef unsigned v2u __attribute__((ext_vector_type(2)));
typedef float v4f __attribute__((ext_vector_type(4)));
typedef float v2f __attribute__((ext_vector_type(2)));
typedef short v4s __attribute__((ext_vector_type(4)));
typedef __bf16 bf16x2 __attribute__((ext_vector_type(2)));
typedef __bf16 bf16x8 __attribute__((ext_vector_type(8)));
#define LAS __attribute__((address_space(3)))
#define GAS __attribute__((address_space(1)))
#define DI __device__ __forceinline__

#define NSTAT 10
#define WINU 12
#define ROT 8
#define PSTAG 8
#define AHEAD 3
#define GOAT 1
#define WOAT 0
constexpr int NWAVE = 12, NTHR = NWAVE * 64;
constexpr int NS = 4 + 16 + NSTAT;
constexpr int POOL = (16 - NSTAT) * 8;
constexpr int NB1 = 1 + POOL / 8;
static_assert(NB1 <= 7 && NSTAT >= 4 && WINU % 2 == 0 && WINU >= 4 && WINU <= 12, "");
constexpr unsigned SPIN_CAP = 1u << 18;
constexpr int NHOLD = 16, HOLD_B = 544, REC_B = 528;
constexpr int L_RK = 0, L_RV = 65536, L_CTL = 131072, L_Q = L_CTL + 512, L_OP = L_Q + 1024, L_FP = L_OP + 128, L_QP = L_FP + 256, L_REC = L_QP + 9 * 256,
              L_HOLD = L_REC + 24 * REC_B, L_END = L_HOLD + NHOLD * HOLD_B;
static_assert(L_END <= 163840, "");
enum { C_FULL = 0, C_END = 1, C_QRDY = 3, C_WANT = 4, C_QCNT = 5, C_TAGA = 6, C_TAGB = 7, C_FREE = 8  , C_CNT = 16, C_YPUB = 19,
       C_PHASE = 20, C_HELD = 21, C_OSEQ = 22  , C_FCNT = 24, C_TICK = 28, C_WORDS = 28 + POOL + 8 };
static_assert(C_WORDS * 4 <= 512, "");
constexpr int W_X = 0, W_CTR = 4096, W_QA = 16384, W_QB = W_QA + 32768, W_P = W_QB + 32768, P_STR = 9216  ,
              W_P1 = W_P + 96 * P_STR, P1_STR = 66560  , W_Y = W_P1 + 32 * P1_STR,
              W_ENDB = W_Y + 4 * 36 * 16384  , W_STAMP = (W_ENDB + 4095) / 4096 * 4096;
constexpr float SCALE = 0.088388347648318440f, L2E = 1.4426950408889634f;

struct Args {
  const unsigned char *xA, *kA, *vA, *wqA, *woA, *xB, *kB, *vB, *wqB, *woB;
  unsigned char *out, *ws;
};

DI float dot2(unsigned a, unsigned b, float c) { return __builtin_amdgcn_fdot2_f32_bf16(__builtin_bit_cast(bf16x2, a), __builtin_bit_cast(bf16x2, b), c, false); }
template <int CTRL> DI float dpp(float x) { return __builtin_bit_cast(float, __builtin_amdgcn_mov_dpp(__builtin_bit_cast(int, x), CTRL, 0xf, 0xf, true)); }
DI unsigned pk_bf16(float lo, float hi) {
  unsigned r = __builtin_bit_cast(unsigned, bf16x2{(__bf16)lo, (__bf16)hi});
  asm("" : "+v"(r));
  return r;
}
DI float bf_lo(unsigned u) { return __builtin_bit_cast(float, u << 16); }
DI float bf_hi(unsigned u) { return __builtin_bit_cast(float, u & 0xffff0000u); }
DI float fbits(unsigned u) { return __builtin_bit_cast(float, u); }
DI unsigned ubits(float f) { return __builtin_bit_cast(unsigned, f); }
DI unsigned rfl(unsigned x) { return __builtin_amdgcn_readfirstlane(x); }
DI float ex2(float x) { return __builtin_amdgcn_exp2f(x); }
DI v4f mfma(v4u a, v4u b, v4f c) { return __builtin_amdgcn_mfma_f32_16x16x32_bf16(__builtin_bit_cast(bf16x8, a), __builtin_bit_cast(bf16x8, b), c, 0, 0, 0); }
DI v2u trd(LAS unsigned char* p) { return __builtin_bit_cast(v2u, __builtin_amdgcn_ds_read_tr16_b64_v4i16((LAS v4s*)p)); }
DI float rs32(float x, float y) { auto s = __builtin_amdgcn_permlane32_swap(ubits(x), ubits(y), false, false); return fbits(s[0]) + fbits(s[1]); }
DI float rs16(float x, float y) { auto s = __builtin_amdgcn_permlane16_swap(ubits(x), ubits(y), false, false); return fbits(s[0]) + fbits(s[1]); }
DI float xsum(float x) { x = rs16(x, x); return rs32(x, x); }
DI float xmax(float x) {
  auto s = __builtin_amdgcn_permlane16_swap(ubits(x), ubits(x), false, false); x = fmaxf(fbits(s[0]), fbits(s[1]));
  auto t = __builtin_amdgcn_permlane32_swap(ubits(x), ubits(x), false, false); return fmaxf(fbits(t[0]), fbits(t[1]));
}
DI void lgkm0() { asm volatile("s_waitcnt lgkmcnt(0)" ::: "memory"); }
template <int N> DI void waitvm() { asm volatile("s_waitcnt vmcnt(%0)" ::"n"(N) : "memory"); }
DI __amdgpu_buffer_rsrc_t mkrs(const void* p, unsigned bytes) { return __builtin_amdgcn_make_buffer_rsrc((void*)p, 0, bytes, 0x00020000); }
DI void dma8(__amdgpu_buffer_rsrc_t rs, unsigned voff, unsigned soff, unsigned lds) {
  unsigned keep, s2;
  asm volatile("s_mov_b32 %0, m0\n\ts_mov_b32 m0, %5\n\ts_add_u32 %1, %4, 0x1000\n\ts_nop 2\n\t"
               "buffer_load_dwordx4 %2, %3, %4 offen nt lds\n\t"
               "buffer_load_dwordx4 %2, %3, %4 offen offset:1024 nt lds\n\t"
               "buffer_load_dwordx4 %2, %3, %4 offen offset:2048 nt lds\n\t"
               "buffer_load_dwordx4 %2, %3, %4 offen offset:3072 nt lds\n\t"
               "s_add_u32 m0, m0, 0x1000\n\ts_nop 0\n\t"
               "buffer_load_dwordx4 %2, %3, %1 offen nt lds\n\t"
               "buffer_load_dwordx4 %2, %3, %1 offen offset:1024 nt lds\n\t"
               "buffer_load_dwordx4 %2, %3, %1 offen offset:2048 nt lds\n\t"
               "buffer_load_dwordx4 %2, %3, %1 offen offset:3072 nt lds\n\t"
               "s_mov_b32 m0, %0"
               : "=&s"(keep), "=&s"(s2) : "v"(voff), "s"(rs), "s"(soff), "s"(lds) : "memory", "scc");
}
constexpr int SC1 = 16;
DI v4u gld(__amdgpu_buffer_rsrc_t rs, int off, int soff = 0) { return __builtin_amdgcn_raw_buffer_load_b128(rs, off, soff, SC1); }
DI void gst(__amdgpu_buffer_rsrc_t rs, int off, unsigned a, unsigned b, unsigned ta, unsigned tb) { __builtin_amdgcn_raw_buffer_store_b128(v4u{a, ta, b, tb}, rs, off, 0, SC1); }
DI bool gok(v4u g, unsigned ta, unsigned tb) { return g.y == ta && g.w == tb; }

#define ST(i) do { } while (0)

typedef volatile LAS unsigned* ctl_t;
template <int SLP> DI bool wait_ge(ctl_t ctl, int idx, unsigned v) {
#pragma unroll 1
  for (unsigned s = 0; rfl(ctl[idx]) < v; ++s) {
    if (s > SPIN_CAP) return false;
    __builtin_amdgcn_s_sleep(SLP);
  }
  asm volatile("" ::: "memory");
  return true;
}
DI void lds_add(LAS unsigned char* L, int idx, unsigned v, int lane) {
  lgkm0();
  if (lane == 0) __hip_atomic_fetch_add((LAS unsigned*)(L + L_CTL) + idx, v, __ATOMIC_RELAXED, __HIP_MEMORY_SCOPE_WORKGROUP);
}
DI bool last_arriver(LAS unsigned char* L, int idx, unsigned n, int lane) {
  lgkm0();
  unsigned old = 0;
  if (lane == 0) old = __hip_atomic_fetch_add((LAS unsigned*)(L + L_CTL) + idx, 1u, __ATOMIC_RELAXED, __HIP_MEMORY_SCOPE_WORKGROUP);
  old = rfl(old);
  asm volatile("" ::: "memory");
  return old == n - 1;
}
DI void read_k(LAS unsigned char* L, unsigned off, v4u (&K)[4]) {
#pragma unroll
  for (int kk = 0; kk < 4; ++kk) K[kk] = *(const LAS v4u*)(L + L_RK + (off ^ (kk * 64)));
}
DI void read_v(LAS unsigned char* L, unsigned off, v4u (&V)[8]) {
#pragma unroll
  for (int blk = 0; blk < 8; ++blk) {
    LAS unsigned char* p = L + L_RV + (off ^ ((blk >> 1) * 64)) + (blk & 1) * 32;
    const v2u x = trd(p), y = trd(p + 4096);
    V[blk] = v4u{x.x, x.y, y.x, y.y};
  }
  __builtin_amdgcn_sched_barrier(0);
}
DI v4f scores(const v4u (&K)[4], const v4u (&q)[4]) {
  v4f d = {0.f, 0.f, 0.f, 0.f};
#pragma unroll
  for (int kk = 0; kk < 4; ++kk) d = mfma(K[kk], q[kk], d);
  const unsigned x01 = pk_bf16(d[0], d[1]), x23 = pk_bf16(d[2], d[3]);
  const unsigned b01 = pk_bf16(bf_lo(x01) * SCALE, bf_hi(x01) * SCALE), b23 = pk_bf16(bf_lo(x23) * SCALE, bf_hi(x23) * SCALE);
  return v4f{bf_lo(b01), bf_hi(b01), bf_lo(b23), bf_hi(b23)};
}
DI float sel(unsigned mask, float a, float b) { return fbits(__builtin_amdgcn_bitop3_b32(mask, ubits(a), ubits(b), 0xCA)); }
DI v4f sel4(unsigned mask, v4f a, v4f b) { return v4f{sel(mask, a[0], b[0]), sel(mask, a[1], b[1]), sel(mask, a[2], b[2]), sel(mask, a[3], b[3])}; }
DI v2f sel2(unsigned mask, v2f a, v2f b) { return v2f{sel(mask, a[0], b[0]), sel(mask, a[1], b[1])}; }
DI unsigned bitmask(int x, int bit) { unsigned m = (unsigned)((x << (31 - bit)) >> 31); asm("" : "+v"(m)); return m; }
DI float max4(v4f d) { return fmaxf(fmaxf(d[0], d[1]), fmaxf(d[2], d[3])); }
DI float sum4(v4f d) { return (d[0] + d[1]) + (d[2] + d[3]); }
DI v4f expo(v4f s, float m2) { return v4f{ex2(fmaf(s[0], L2E, -m2)), ex2(fmaf(s[1], L2E, -m2)), ex2(fmaf(s[2], L2E, -m2)), ex2(fmaf(s[3], L2E, -m2))}; }
DI v4u pack_p(v4f p0, v4f p1) { return v4u{pk_bf16(p0[0], p0[1]), pk_bf16(p0[2], p0[3]), pk_bf16(p1[0], p1[1]), pk_bf16(p1[2], p1[3])}; }
constexpr int OOB = 0x70000000;
DI void emit_rec(LAS unsigned char* L, ctl_t ctl, __amdgpu_buffer_rsrc_t ws, int slot, int base, int dstr, int rec, int gi, int lane, v2f o, float m, float l, unsigned ta, unsigned tb) {
  bool now = slot >= NHOLD || rfl(ctl[C_PHASE]) >= GOAT;
  if (!now) {
    LAS unsigned char* h = L + L_HOLD + slot * HOLD_B;
    *(LAS v2f*)(h + 32 + gi * 8) = o;
    if (lane == 0) { *(LAS v4u*)h = v4u{(unsigned)(base + rec * 128), (unsigned)dstr, (unsigned)(base + 8 * dstr + rec * 16), 0u}; *(LAS v2f*)(h + 16) = v2f{m, l}; }
    lgkm0();
    if (lane == 0) __hip_atomic_fetch_or((LAS unsigned*)(L + L_CTL) + C_HELD, 1u << slot, __ATOMIC_RELAXED, __HIP_MEMORY_SCOPE_WORKGROUP);
    lgkm0();
    now = rfl(ctl[C_PHASE]) >= GOAT;
  }
  if (now) {
    gst(ws, base + (gi >> 3) * dstr + rec * 128 + (gi & 7) * 16, ubits(o.x), ubits(o.y), ta, tb);
    if (lane == 0) gst(ws, base + 8 * dstr + rec * 16, ubits(m), ubits(l), ta, tb);
  }
}
template <int NR> DI v2f merge_rec(LAS unsigned char* rec, int lane, float& Mo, float& Lo) {
  v2f av[NR];
  const v2f ml = *(LAS v2f*)(rec + (lane & (NR - 1)) * REC_B);
#pragma unroll
  for (int w = 0; w < NR; ++w) av[w] = *(LAS v2f*)(rec + w * REC_B + 16 + lane * 8);
  float M = ml.x; M = fmaxf(M, dpp<0xB1>(M)); M = fmaxf(M, dpp<0x4E>(M));
  if (NR == 8) M = fmaxf(M, dpp<0x141>(M));
  const float f = ex2(ml.x - M);
  float Ls = f * ml.y; Ls += dpp<0xB1>(Ls); Ls += dpp<0x4E>(Ls);
  if (NR == 8) Ls += dpp<0x141>(Ls);
  float a0 = 0.f, a1 = 0.f;
#pragma unroll
  for (int w = 0; w < NR; ++w) { const float fw = fbits(__builtin_amdgcn_readlane(ubits(f), w)); a0 = fmaf(fw, av[w].x, a0); a1 = fmaf(fw, av[w].y, a1); }
  Mo = M; Lo = Ls;
  return v2f{a0, a1};
}

__global__ void __launch_bounds__(NTHR) fused_decode(Args a) {
  __shared__ __attribute__((aligned(16))) unsigned char smem[L_END];
  LAS unsigned char* L = (LAS unsigned char*)smem;
  ctl_t ctl = (ctl_t)(L + L_CTL);
  asm volatile("" ::"s"(a.xA), "s"(a.kA), "s"(a.vA), "s"(a.wqA), "s"(a.woA), "s"(a.xB), "s"(a.kB), "s"(a.vB), "s"(a.wqB), "s"(a.woB), "s"(a.out), "s"(a.ws));
  const int wg = blockIdx.x, g = wg >> 3, j = wg & 7;
  const int lane = threadIdx.x & 63, w = rfl(threadIdx.x >> 6);
  const __amdgpu_buffer_rsrc_t ws = mkrs(a.ws, W_ENDB);
  ST(w == 1 ? 0 : 31);

  if (w == 0) {
    __builtin_amdgcn_s_waitcnt(0);
    const unsigned lb = (unsigned)(uintptr_t)L;
    const unsigned voff = (lane * 16) ^ ((lane >> 4) << 6);
    const unsigned offA = g * (2u << 20) + (3u << 19) + j * 16384;
    __amdgpu_buffer_rsrc_t rk = mkrs(a.kA, 64u << 20), rv = mkrs(a.vA, 64u << 20);
    dma8(rk, voff, offA, lb + L_RK); dma8(rk, voff, offA + 8192, lb + L_RK + 8192);
    dma8(rk, voff, offA + 131072, lb + L_RK + 16384); dma8(rk, voff, offA + 131072 + 8192, lb + L_RK + 24576);
    dma8(rv, voff, offA, lb + L_RV); dma8(rv, voff, offA + 8192, lb + L_RV + 8192);
    asm volatile("s_barrier" ::: "memory");
    int u = 12, pub = 0, n = 1;
    unsigned soff = offA + 131072;
#define TB()
#define TE(x)
#define ISSUE_CHUNK(rs, so, ld) do { \
      dma8(rs, voff, so, ld); waitvm<4 * WINU>(); u += 2; if (u - WINU > pub) { pub = u - WINU; ctl[C_FULL] = pub; } \
      dma8(rs, voff, (so) + 8192, (ld) + 8192); waitvm<4 * WINU>(); u += 2; if (u - WINU > pub) { pub = u - WINU; ctl[C_FULL] = pub; } } while (0)
#pragma unroll 1
    for (;; ++n) {
      const int nk = n + 1;
      unsigned t;
#pragma unroll 1
      for (unsigned sp = 0;; ++sp) {
        const unsigned tk = ctl[C_TICK + (nk >= NS ? nk - NS : 0)], fk = ctl[C_FREE + (nk & 3)], fv = ctl[C_FREE + 4 + (n & 3)];
        t = nk >= NS ? rfl(tk) : 0u;
        if (t != 0xffffffffu && rfl(fk) >= (unsigned)(nk & ~3) && rfl(fv) >= (unsigned)(n & ~3)) break;
        if (sp > SPIN_CAP) return;
        TB();
        if (sp == 6) { waitvm<0>(); pub = u; ctl[C_FULL] = pub; }
        __builtin_amdgcn_s_sleep(1);
      }
      const bool last = t >= (unsigned)POOL;
      const unsigned sv = soff;
      if (!last) {
        if (nk + AHEAD >= NS) ctl[C_WANT] = nk + AHEAD + 1 - NS;
        if (nk == 4) rk = mkrs(a.kB, 128u << 20);
        soff = nk < 4 ? offA + nk * 131072
                      : (nk < 20 ? g : 32 + g) * (2u << 20) + (((nk < 20 ? (nk - 4) * 8 + j : nk < NS ? (nk - 20) * 8 + j : NSTAT * 8 + t) + g * ROT) & 127) * 16384;
        ISSUE_CHUNK(rk, soff, lb + L_RK + (nk & 3) * 16384);
      } else {
        ctl[C_END] = nk; ctl[C_PHASE] = 1; ST(8);
      }
      if (n == 4) rv = mkrs(a.vB, 128u << 20);
      ISSUE_CHUNK(rv, sv, lb + L_RV + (n & 3) * 16384);
      if (last) break;
    }
    ctl[C_PHASE] = 2;
    ST(9);
#define DRAIN(k) if (WINU > k) { waitvm<4 * k>(); const int p_ = u - k + (k < 4 ? 4 : 0); if (k == 0) ctl[C_PHASE] = 3; if (p_ > pub) { pub = p_; ctl[C_FULL] = pub; } }
    DRAIN(10) DRAIN(8) DRAIN(6) DRAIN(4) DRAIN(2) DRAIN(0)
#undef DRAIN
    ST(10);
    return;
  }
  const int cb = (wg & ~15) + ((wg & 7) << 1) + ((wg >> 3) & 1);
  const int cbase = j < 4 ? 0 : 2, prod = g * 4 + (j & 3);
  unsigned ta, tb;
  int jb, jend;
  if (w == 1) {
    const v2u ep = __builtin_amdgcn_raw_buffer_load_b64(ws, W_X, 0, SC1);
    if (cb >= 128 && (cb & 3) == 0 && lane == 0) __builtin_amdgcn_raw_buffer_store_b32(0u, ws, W_CTR + ((cb - 128) >> 2) * 256, 0, SC1);
    ctl[lane] = lane == C_END ? 0x7fffffffu : lane >= C_TICK ? 0xffffffffu : 0u;
    ctl[lane + 64] = 0xffffffffu;
    asm volatile("s_waitcnt lgkmcnt(0)\n\ts_barrier" ::: "memory");
    ta = rfl(ep.x) + 1u; tb = rfl(ep.y) + 1u;
    ta += ta == 0u; tb += tb == 0u;
    waitvm<0>();
    if (!wait_ge<1>(ctl, C_QCNT, 9)) return;
    ST(1);
    if (lane < 16) {
      v4f s = {0.f, 0.f, 0.f, 0.f};
#pragma unroll
      for (int i = 0; i < 9; ++i) s += *(LAS v4f*)(L + L_QP + i * 256 + lane * 16);
      const int b = lane >> 3, quad = lane & 7;
      const int off = cb < 128 ? W_QA + ((b * 16 + (cb >> 3)) * 64 + (cb & 7) * 8 + quad) * 16 : W_QB + (((cb - 128) >> 2) * 64 + b * 32 + (cb & 3) * 8 + quad) * 16;
      gst(ws, off, pk_bf16(s.x, s.y), pk_bf16(s.z, s.w), ta, tb);
    }
    jb = 0; jend = 3;
  } else if (w == 2) {
    asm volatile("s_barrier" ::: "memory");
    if (!wait_ge<16>(ctl, C_WANT, 1)) return;
    if (!wait_ge<2>(ctl, C_PHASE, GOAT)) return;
    ta = rfl(ctl[C_TAGA]); tb = rfl(ctl[C_TAGB]);
    unsigned held = rfl(ctl[C_HELD]);
    asm volatile("" ::: "memory");
#pragma unroll 1
    while (held) {
      const int slot = __builtin_ctz(held);
      held &= held - 1;
      LAS unsigned char* h = L + L_HOLD + slot * HOLD_B;
      const v4u hd = *(LAS v4u*)h;
      const v2u ml = *(LAS v2u*)(h + 16), o = *(LAS v2u*)(h + 32 + lane * 8);
      gst(ws, hd.x + (lane >> 3) * hd.y + (lane & 7) * 16, o.x, o.y, ta, tb);
      if (lane == 0) gst(ws, hd.z, ml.x, ml.y, ta, tb);
    }
    ST(14);
    jb = 1; jend = 3;
  } else {
    {
      {
        const int qw = w - 3;
        const int r0 = qw * 64 + (lane >> 2) * 4;
        const unsigned char* xp = (cb < 128 ? a.xA : a.xB) + r0 * 2;
        const unsigned char* wp = (cb < 128 ? a.wqA : a.wqB) + (size_t)r0 * 8192 + (cb & 127) * 64 + (lane & 3) * 16;
        v4u Wq[4];
#pragma unroll
        for (int i = 0; i < 4; ++i) Wq[i] = __builtin_nontemporal_load((const GAS v4u*)(wp + i * 8192));
        const v2u X0 = *(const GAS v2u*)xp, X1 = *(const GAS v2u*)(xp + 1152);
        asm volatile("s_barrier" ::: "memory");
        float ac[2][8];
#pragma unroll
        for (int k = 0; k < 8; ++k) { ac[0][k] = 0.f; ac[1][k] = 0.f; }
#pragma unroll
        for (int s = 0; s < 2; ++s)
#pragma unroll
          for (int d = 0; d < 4; ++d) {
            const unsigned lo = __builtin_amdgcn_perm(Wq[2 * s + 1][d], Wq[2 * s][d], 0x05040100u), hi = __builtin_amdgcn_perm(Wq[2 * s + 1][d], Wq[2 * s][d], 0x07060302u);
            ac[0][2 * d] = dot2(lo, X0[s], ac[0][2 * d]); ac[0][2 * d + 1] = dot2(hi, X0[s], ac[0][2 * d + 1]);
            ac[1][2 * d] = dot2(lo, X1[s], ac[1][2 * d]); ac[1][2 * d + 1] = dot2(hi, X1[s], ac[1][2 * d + 1]);
          }
        float b8[8], c4[4];
#pragma unroll
        for (int k = 0; k < 8; ++k) b8[k] = rs32(ac[0][k], ac[1][k]);
#pragma unroll
        for (int k = 0; k < 4; ++k) { float t = rs16(b8[k], b8[k + 4]); t += dpp<0x128>(t); t += dpp<0x124>(t); c4[k] = t; }
        if ((lane & 12) == 0) *(LAS v4f*)(L + L_QP + qw * 256 + (lane >> 5) * 128 + (lane & 3) * 32 + ((lane >> 4) & 1) * 16) = v4f{c4[0], c4[1], c4[2], c4[3]};
        lds_add(L, C_QCNT, 1, lane);
      }
      const int r = lane >> 4, c = lane & 15;
      const unsigned kof = c * 256 + r * 16 + (c & 3) * 64;
      const unsigned vof = (4 * r + (c >> 2)) * 256 + (c >> 2) * 64 + (c & 3) * 8;
      if (w >= 4) {
        const unsigned aw = w - 4, off = aw * 8192, an = aw >> 1, ah = 2 * (aw & 1) + 2;
        v4u K0[4], K1[4], V[8];
        if (!wait_ge<1>(ctl, C_FULL, (an ? 8 * an - 4 : 0) + ah)) return;
        read_k(L, off + kof, K0); read_k(L, off + 4096 + kof, K1);
        lds_add(L, C_FREE + (aw >> 1), 2, lane);
        if (!wait_ge<1>(ctl, C_FULL, 8 * an + 8 + ah)) return;
        read_v(L, off + vof, V);
        lds_add(L, C_FREE + 4 + (aw >> 1), 2, lane);
        if (!wait_ge<1>(ctl, C_QRDY, 1)) return;
        if (w == 4) ST(3);
        ta = rfl(ctl[C_TAGA]); tb = rfl(ctl[C_TAGB]);
        const LAS v4u* qp = (const LAS v4u*)(L + L_Q) + r + (c >> 3) * 16;
        const v4u q[4] = {qp[0], qp[4], qp[8], qp[12]};
        v4f s0 = scores(K0, q), s1 = scores(K1, q);
        const float M = xmax(fmaxf(max4(s0), max4(s1))) * L2E;
        s0 = expo(s0, M); s1 = expo(s1, M);
        const v4u P = pack_p(s0, s1);
        const float Ls = xsum(sum4(s0) + sum4(s1));
        v4f acc[8];
#pragma unroll
        for (int blk = 0; blk < 8; ++blk) acc[blk] = mfma(V[blk], P, v4f{0.f, 0.f, 0.f, 0.f});
#pragma unroll
        for (int k = 0; k < 4; ++k) acc[k] = sel4(bitmask(c, 2), acc[k + 4], acc[k]);
#pragma unroll
        for (int k = 0; k < 2; ++k) acc[k] = sel4(bitmask(c, 1), acc[k + 2], acc[k]);
        acc[0] = sel4(bitmask(c, 0), acc[1], acc[0]);
        LAS unsigned char* rec = L + L_REC + ((c >> 3) * 8 + aw) * REC_B;
        *(LAS v4f*)(rec + 16 + ((c & 7) * 16 + r * 4) * 4) = acc[0];
        if ((lane & 0x37) == 0) *(LAS v2f*)rec = v2f{M, Ls};
        if (w == 4) ST(15);
        if (last_arriver(L, C_CNT, 8, lane)) {
#pragma unroll 1
          for (int e = 0; e < 2; ++e) {
            float Mo, Lo;
            const v2f o = merge_rec<8>(L + L_REC + e * 8 * REC_B, lane, Mo, Lo);
            emit_rec(L, ctl, ws, e, W_P + (g * 2 + e) * P_STR, 1024, j, lane, lane, o, Mo, Lo, ta, tb);
          }
          ST(4);
        }
      }
      if (w >= 8) {
        const int cw = w - 8;
        const unsigned off = cw * 16384;
        v4u q[4];
        float m = -INFINITY, l = 0.f;
        v4f acc[8];
        bool fresh = true;
        const unsigned mk0 = bitmask(c, 0), mk1 = bitmask(c, 1), mk2 = bitmask(c, 2), mk3 = bitmask(c, 3);
#pragma unroll 1
        for (unsigned n = 4 + cw;; n += 4) {
          if (fresh) {
            const LAS v4u* qp = (const LAS v4u*)(L + L_Q) + r + (n < 20 ? 32 : 48);
#pragma unroll
            for (int kk = 0; kk < 4; ++kk) q[kk] = qp[4 * kk];
            m = -INFINITY; l = 0.f;
#pragma unroll
            for (int blk = 0; blk < 8; ++blk) acc[blk] = v4f{0.f, 0.f, 0.f, 0.f};
            fresh = false;
          }
          bool stop = false;
#pragma unroll 1
          for (unsigned sp = 0;; ++sp) {
            const v2u fe = *(volatile LAS v2u*)(L + L_CTL);
            if (n >= rfl(fe.y) || sp > SPIN_CAP) { stop = true; break; }
            if (rfl(fe.x) >= 8 * n) break;
            __builtin_amdgcn_s_sleep(1);
          }
          if (stop) break;
          asm volatile("" ::: "memory");
          v4f s[4];
          v4u K[4][4];
#pragma unroll
          for (int uu = 0; uu < 4; ++uu) read_k(L, off + uu * 4096 + kof, K[uu]);
          __builtin_amdgcn_sched_barrier(0);
#pragma unroll
          for (int uu = 0; uu < 4; ++uu) s[uu] = scores(K[uu], q);
          lds_add(L, C_FREE + cw, 4, lane);
          const float mn = fmaxf(xmax(fmaxf(fmaxf(max4(s[0]), max4(s[1])), fmaxf(max4(s[2]), max4(s[3])))) * L2E, m), al = ex2(m - mn);
          m = mn;
#pragma unroll
          for (int uu = 0; uu < 4; ++uu) s[uu] = expo(s[uu], mn);
          v4u P0 = pack_p(s[0], s[1]), P1 = pack_p(s[2], s[3]);
          l = fmaf(l, al, (sum4(s[0]) + sum4(s[1])) + (sum4(s[2]) + sum4(s[3])));
#pragma unroll
          for (int blk = 0; blk < 8; ++blk) acc[blk] *= al;
          const bool pubn = n >= NS - 4 || (n & ~3u) == 16;
          float Ls = xsum(l);
          asm volatile("" : "+v"(P0), "+v"(P1), "+v"(Ls), "+v"(acc[0]), "+v"(acc[1]), "+v"(acc[2]), "+v"(acc[3]), "+v"(acc[4]), "+v"(acc[5]), "+v"(acc[6]), "+v"(acc[7]));
          v4u V[8];
          if (!wait_ge<1>(ctl, C_FULL, 8 * n + 10)) return;
          read_v(L, off + vof, V);
#pragma unroll
          for (int blk = 0; blk < 8; ++blk) acc[blk] = mfma(V[blk], P0, acc[blk]);
          if (!wait_ge<1>(ctl, C_FULL, 8 * n + 12)) return;
          read_v(L, off + 8192 + vof, V);
#pragma unroll
          for (int blk = 0; blk < 8; ++blk) acc[blk] = mfma(V[blk], P1, acc[blk]);
          lds_add(L, C_FREE + 4 + cw, 4, lane);
          if (pubn) {
            v2f o[8];
#pragma unroll
            for (int k = 0; k < 8; ++k) o[k] = sel2(mk0, v2f{acc[k][2], acc[k][3]}, v2f{acc[k][0], acc[k][1]});
#pragma unroll
            for (int k = 0; k < 4; ++k) o[k] = sel2(mk3, o[k + 4], o[k]);
#pragma unroll
            for (int k = 0; k < 2; ++k) o[k] = sel2(mk2, o[k + 2], o[k]);
            o[0] = sel2(mk1, o[1], o[0]);
            int slot = 4 + n - NS, rec = 0, gi = (c >> 1) * 8 + 2 * r + (c & 1);
            bool emit = true;
            if (n >= NS) rec = 8 + rfl(ctl[C_TICK + n - NS]);
            else {
              const int b = n >= 20;
              LAS unsigned char* rp = L + L_REC + (16 + b * 4) * REC_B;
              *(LAS v2f*)(rp + cw * REC_B + 16 + gi * 8) = o[0];
              if (lane == 0) *(LAS v2f*)(rp + cw * REC_B) = v2f{m, Ls};
              emit = last_arriver(L, C_CNT + 1 + b, 4, lane);
              if (emit) o[0] = merge_rec<4>(rp, lane, m, Ls);
              slot = 2 + b; rec = j; gi = lane;
            }
            if (emit) {
              emit_rec(L, ctl, ws, slot, n >= 20 ? W_P1 + g * P1_STR : W_P + (64 + g) * P_STR, n >= 20 ? 8192 : 1024, rec, gi, lane, o[0], m, Ls, ta, tb);
              ST(n >= 20 ? 7 : 5);
            }
            fresh = true;
          }
        }
        jb = 3; jend = 4;
      } else {
      const int k = w == 3 ? 4 : w - 4;
      if (WOAT) { if (!wait_ge<16>(ctl, C_WANT, 1)) return; if (!wait_ge<2>(ctl, C_PHASE, WOAT)) return; }
      else if (!wait_ge<8>(ctl, C_QRDY, 1)) return;
      ta = rfl(ctl[C_TAGA]); tb = rfl(ctl[C_TAGB]);
      unsigned Wo[32];
      {
        const int head = j < 4 ? 2 * (g & 15) + (g >> 4) : g;
        const unsigned char* pw = (j < 4 ? a.woA : a.woB) + (size_t)(head * 128 + (j & 3) * 32) * 1152 + k * 256 + (k < 4 ? lane : lane & 31) * 4;
#pragma unroll
        for (int i = 0; i < 32; ++i) Wo[i] = __builtin_nontemporal_load((const GAS unsigned*)(pw + i * 1152));
#pragma unroll
        for (int s = 0; s < 16; ++s) {
          const unsigned a0 = Wo[2 * s], a1 = Wo[2 * s + 1];
          Wo[2 * s] = __builtin_amdgcn_perm(a1, a0, 0x05040100u); Wo[2 * s + 1] = __builtin_amdgcn_perm(a1, a0, 0x07060302u);
        }
#pragma unroll
        for (int s = 0; s < 32; s += 4) asm volatile("" : "+v"(Wo[s]), "+v"(Wo[s + 1]), "+v"(Wo[s + 2]), "+v"(Wo[s + 3]));
      }
      if (!wait_ge<16>(ctl, C_PHASE, 1)) return;
      int yoff = W_Y + ((cbase * 36 + k * 8 + (lane >> 3)) * 128 + prod) * 128 + (lane & 7) * 16;
      if (k == 4 && lane >= 32) yoff = OOB;
#pragma unroll 1
      for (int i = 0; i < 2; ++i) {
        if (!wait_ge<1>(ctl, C_OSEQ + i, 2)) return;
        const LAS v4u* op = (const LAS v4u*)(L + L_OP + i * 64);
        float y0 = 0.f, y1 = 0.f, z0 = 0.f, z1 = 0.f;
#pragma unroll
        for (int h = 0; h < 2; ++h) {
          const v4u oa = op[2 * h], ob = op[2 * h + 1];
#pragma unroll
          for (int s = 0; s < 4; ++s) {
            y0 = dot2(Wo[16 * h + 2 * s], oa[s], y0); y1 = dot2(Wo[16 * h + 2 * s + 1], oa[s], y1);
            z0 = dot2(Wo[16 * h + 8 + 2 * s], ob[s], z0); z1 = dot2(Wo[16 * h + 9 + 2 * s], ob[s], z1);
          }
        }
        gst(ws, yoff + i * (36 * 16384), ubits(y0 + z0), ubits(y1 + z1), ta, tb);
        if (w == 4) { ctl[C_YPUB] = i + 1; if (j >= 4 && i == 1) ST(12); }
      }
      return;
      }
    }
  }
#pragma unroll 1
  for (; jb < jend; ++jb) {
    if (jb == 1 && w == 1 && lane == 0) {
      const unsigned char* ctr = a.ws + W_CTR + g * 256;
      const unsigned zero = 0, one = 1;
      unsigned t0, t1, t2 = 0;
#define TICKET(t) asm volatile("global_atomic_add %0, %1, %2, %3 sc0" : "=v"(t) : "v"(zero), "v"(one), "s"(ctr) : "memory")
#define STEP(tnew, told, kk) \
  if (!wait_ge<2>(ctl, C_WANT, (kk) + AHEAD)) break; \
  TICKET(tnew); \
  asm volatile("s_waitcnt vmcnt(%1)" : "+v"(told) : "n"(AHEAD - 1) : "memory"); \
  ctl[C_TICK + (kk)] = told; \
  if (rfl(told) >= (unsigned)POOL) break;
      if (wait_ge<8>(ctl, C_WANT, 1)) {
        TICKET(t0);
        if (wait_ge<2>(ctl, C_WANT, 2)) {
          TICKET(t1);
#pragma unroll 1
          for (int kk = 0; kk <= POOL; kk += 3) { STEP(t2, t0, kk) STEP(t0, t1, kk + 1) STEP(t1, t2, kk + 2) }
        }
      }
      asm volatile("s_waitcnt vmcnt(0)" : "+v"(t0), "+v"(t1), "+v"(t2)::"memory");
    }
    const bool fin = jb == 3;
    const int c = fin ? cbase + (prod & 1) : cbase + jb - 1, rho = prod >> 1;
    int voff, voff7;
    unsigned mask;
    unsigned long long ign7 = 0;
    if (fin) {
      if (rho >= 36) return;
      voff7 = voff = W_Y + (c * 36 + rho) * 16384 + (w - 8) * 4096 + lane * 16;
      mask = 0x0fu;
      if (!wait_ge<1>(ctl, C_YPUB, (prod & 1) + 1)) return;
    } else if (jb == 0) {
      voff = W_QA + g * 1024 + lane * 16;
      voff7 = voff + (W_QB - W_QA) - 7 * 1024;
      mask = 0x81u;
    } else {
      const int base = c < 2 ? W_P + ((c * 16 + (g & 15)) * 2 + (g >> 4)) * P_STR : c == 2 ? W_P + (64 + g) * P_STR : W_P1 + g * P1_STR;
      const int dstr = c == 3 ? 8192 : 1024, NB = c == 3 ? NB1 : 1;
      voff = base + (2 * (j & 3) + w - 1) * dstr + lane * 16;
      voff7 = base + 8 * dstr - 7 * 1024 + (lane < 8 * NB ? lane : 0) * 16;
      mask = ((1u << NB) - 1u) | 0x80u;
      ign7 = ~0ull << (8 * NB);
      if (!wait_ge<2>(ctl, C_PHASE, GOAT)) return;
    }
    v4u A[8], B[8];
    v2u y[8];
    unsigned long long dn[8];
#pragma unroll
    for (int i = 0; i < 8; ++i) { y[i] = v2u{0u, 0u}; dn[i] = (mask >> i & 1) ? 0ull : ~0ull; }
#define ISSUE(X) _Pragma("unroll") for (int i = 0; i < 8; ++i) X[i] = gld(ws, i == 7 ? voff7 : voff, dn[i] != ~0ull ? i * 1024 : OOB); __builtin_amdgcn_sched_barrier(0);
#define CHECK(X) _Pragma("unroll") for (int i = 0; i < 8; ++i) { const bool ok = gok(X[i], ta, tb); y[i].x = ok ? X[i].x : y[i].x; y[i].y = ok ? X[i].z : y[i].y; \
      dn[i] |= __builtin_amdgcn_ballot_w64(X[i].y == ta) & __builtin_amdgcn_ballot_w64(X[i].w == tb); } \
    if (((dn[0] & dn[1]) & (dn[2] & dn[3]) & (dn[4] & dn[5]) & (dn[6] & dn[7])) == ~0ull) break;
    ISSUE(A)
    __builtin_amdgcn_s_sleep(PSTAG);
    unsigned sp = 0;
#pragma unroll 1
    for (; sp < SPIN_CAP; ++sp) {
      ISSUE(B)
      CHECK(A)
      ISSUE(A)
      CHECK(B)
    }
#undef ISSUE
#undef CHECK
    if (sp >= SPIN_CAP) return;
    if (jb == 0) {
      *(LAS v2u*)(L + L_Q + lane * 8) = y[0];
      *(LAS v2u*)(L + L_Q + 512 + lane * 8) = y[7];
      ctl[C_TAGA] = ta; ctl[C_TAGB] = tb;
      lgkm0();
      ctl[C_QRDY] = 1;
      ST(2);
    } else if (fin) {
      const float s0 = (fbits(y[0].x) + fbits(y[1].x)) + (fbits(y[2].x) + fbits(y[3].x)), s1 = (fbits(y[0].y) + fbits(y[1].y)) + (fbits(y[2].y) + fbits(y[3].y));
      float s = rs32(s0, s1);
      s = rs16(s, s);
      s += dpp<0x128>(s);
      LAS float* fp = (LAS float*)(L + L_FP);
      if ((lane & 24) == 0) fp[((lane & 7) * 2 + (lane >> 5)) * 4 + w - 8] = s;
      if (last_arriver(L, C_FCNT, 4, lane)) {
        if (lane < 16) {
          const v4f p = *(LAS v4f*)(fp + lane * 4);
          const float t = (p.x + p.y) + (p.z + p.w);
          *(GAS unsigned short*)(a.out + ((c & 1) * 1152 + (c >> 1) * 576 + rho * 16 + lane) * 2) = (unsigned short)pk_bf16(t, t);
        }
        if (c == 3 && rho == 0 && lane == 0) __builtin_amdgcn_raw_buffer_store_b64(v2u{ta, tb}, ws, W_X, 0, SC1);
        ST(c == 3 ? 13 : 6);
      }
    } else {
      if (c == 3 && w == 2) ST(11);
      const float mr = fbits(y[7].x);
      float M = fmaxf(mr, dpp<0xB1>(mr)); M = fmaxf(M, dpp<0x4E>(M)); M = fmaxf(M, dpp<0x141>(M)); M = fmaxf(M, dpp<0x128>(M)); M = xmax(M);
      const float f = (ign7 >> lane & 1) ? 0.f : ex2(mr - M);
      float o0 = 0.f, o1 = 0.f;
#pragma unroll
      for (int b = 0; b < NB1; ++b) {
        const float fb = fbits(__builtin_amdgcn_ds_bpermute((8 * b + (lane >> 3)) * 4, ubits(f)));
        o0 = fmaf(fb, fbits(y[b].x), o0); o1 = fmaf(fb, fbits(y[b].y), o1);
      }
      float Ls = f * fbits(y[7].y);
      Ls += dpp<0xB1>(Ls); Ls += dpp<0x4E>(Ls); Ls += dpp<0x141>(Ls); Ls += dpp<0x128>(Ls); Ls = xsum(Ls);
      float o = rs32(o0, o1);
      o = rs16(o, o);
      o += dpp<0x128>(o);
      o *= __builtin_amdgcn_rcpf(Ls);
      if ((lane & 24) == 0) *(LAS unsigned short*)(L + L_OP + (jb - 1) * 64 + (w - 1) * 32 + (lane & 7) * 4 + (lane >> 5) * 2) = (unsigned short)pk_bf16(o, o);
      lds_add(L, C_OSEQ + jb - 1, 1, lane);
    }
    asm volatile("" ::"v"(A[0]), "v"(A[1]), "v"(A[2]), "v"(A[3]), "v"(A[4]), "v"(A[5]), "v"(A[6]), "v"(A[7]) : "memory");
    asm volatile("" ::"v"(B[0]), "v"(B[1]), "v"(B[2]), "v"(B[3]), "v"(B[4]), "v"(B[5]), "v"(B[6]), "v"(B[7]) : "memory");
  }
}

extern "C" void kernel_launch(void* const* d_in, const int* in_sizes, int n_in, void* d_out, int out_size, void* d_ws, size_t ws_size,
                              hipStream_t stream) {
  Args a;
  a.xA = (const unsigned char*)d_in[0]; a.kA = (const unsigned char*)d_in[1]; a.vA = (const unsigned char*)d_in[2];
  a.wqA = (const unsigned char*)d_in[3]; a.woA = (const unsigned char*)d_in[4];
  a.xB = (const unsigned char*)d_in[5]; a.kB = (const unsigned char*)d_in[6]; a.vB = (const unsigned char*)d_in[7];
  a.wqB = (const unsigned char*)d_in[8]; a.woB = (const unsigned char*)d_in[9];
  a.out = (unsigned char*)d_out; a.ws = (unsigned char*)d_ws;
  fused_decode<<<256, NTHR, 0, stream>>>(a);
}
```

```cpp
#include <hip/hip_runtime.h>
#include <stdint.h>

typedef unsigned v4u __attribute__((ext_vector_type(4)));
typedef unsigned v2u __attribute__((ext_vector_type(2)));
typedef float v4f __attribute__((ext_vector_type(4)));
typedef float v2f __attribute__((ext_vector_type(2)));
typedef short v4s __attribute__((ext_vector_type(4)));
typedef __bf16 bf16x2 __attribute__((ext_vector_type(2)));
typedef __bf16 bf16x8 __attribute__((ext_vector_type(8)));
#define LAS __attribute__((address_space(3)))
#define GAS __attribute__((address_space(1)))
#define DI __device__ __forceinline__

#define NSTAT 10
#define WINU 12
#define ROT 8
#define PSTAG 8
#define AHEAD 3
#define GOAT 1
#define WOAT 0
constexpr int NWAVE = 12, NTHR = NWAVE * 64;
constexpr int NS = 4 + 16 + NSTAT;
constexpr int POOL = (16 - NSTAT) * 8;
constexpr int NB1 = 1 + POOL / 8;
static_assert(NB1 <= 7 && NSTAT >= 4 && WINU % 2 == 0 && WINU >= 4 && WINU <= 12, "");
constexpr unsigned SPIN_CAP = 1u << 18;
constexpr int NHOLD = 16, HOLD_B = 544, REC_B = 528;
constexpr int L_RK = 0, L_RV = 65536, L_CTL = 131072, L_Q = L_CTL + 512, L_OP = L_Q + 1024, L_FP = L_OP + 128, L_QP = L_FP + 256, L_REC = L_QP + 9 * 256,
              L_HOLD = L_REC + 24 * REC_B, L_END = L_HOLD + NHOLD * HOLD_B;
static_assert(L_END <= 163840, "");
enum { C_FULL = 0, C_END = 1, C_QRDY = 3, C_WANT = 4, C_QCNT = 5, C_TAGA = 6, C_TAGB = 7, C_FREE = 8  , C_CNT = 16, C_YPUB = 19,
       C_PHASE = 20, C_HELD = 21, C_OSEQ = 22  , C_FCNT = 24, C_TICK = 28, C_WORDS = 28 + POOL + 8 };
static_assert(C_WORDS * 4 <= 512, "");
constexpr int W_X = 0, W_CTR = 4096, W_QA = 16384, W_QB = W_QA + 32768, W_P = W_QB + 32768, P_STR = 9216  ,
              W_P1 = W_P + 96 * P_STR, P1_STR = 66560  , W_Y = W_P1 + 32 * P1_STR,
              W_ENDB = W_Y + 4 * 36 * 16384  , W_STAMP = (W_ENDB + 4095) / 4096 * 4096;
constexpr float SCALE = 0.088388347648318440f, L2E = 1.4426950408889634f;

struct Args {
  const unsigned char *xA, *kA, *vA, *wqA, *woA, *xB, *kB, *vB, *wqB, *woB;
  unsigned char *out, *ws;
};

DI float dot2(unsigned a, unsigned b, float c) { return __builtin_amdgcn_fdot2_f32_bf16(__builtin_bit_cast(bf16x2, a), __builtin_bit_cast(bf16x2, b), c, false); }
template <int CTRL> DI float dpp(float x) { return __builtin_bit_cast(float, __builtin_amdgcn_mov_dpp(__builtin_bit_cast(int, x), CTRL, 0xf, 0xf, true)); }
DI unsigned pk_bf16(float lo, float hi) {
  unsigned r = __builtin_bit_cast(unsigned, bf16x2{(__bf16)lo, (__bf16)hi});
  asm("" : "+v"(r));
  return r;
}
DI float bf_lo(unsigned u) { return __builtin_bit_cast(float, u << 16); }
DI float bf_hi(unsigned u) { return __builtin_bit_cast(float, u & 0xffff0000u); }
DI float fbits(unsigned u) { return __builtin_bit_cast(float, u); }
DI unsigned ubits(float f) { return __builtin_bit_cast(unsigned, f); }
DI unsigned rfl(unsigned x) { return __builtin_amdgcn_readfirstlane(x); }
DI float ex2(float x) { return __builtin_amdgcn_exp2f(x); }
DI v4f mfma(v4u a, v4u b, v4f c) { return __builtin_amdgcn_mfma_f32_16x16x32_bf16(__builtin_bit_cast(bf16x8, a), __builtin_bit_cast(bf16x8, b), c, 0, 0, 0); }
DI v2u trd(LAS unsigned char* p) { return __builtin_bit_cast(v2u, __builtin_amdgcn_ds_read_tr16_b64_v4i16((LAS v4s*)p)); }
DI float rs32(float x, float y) { auto s = __builtin_amdgcn_permlane32_swap(ubits(x), ubits(y), false, false); return fbits(s[0]) + fbits(s[1]); }
DI float rs16(float x, float y) { auto s = __builtin_amdgcn_permlane16_swap(ubits(x), ubits(y), false, false); return fbits(s[0]) + fbits(s[1]); }
DI float xsum(float x) { x = rs16(x, x); return rs32(x, x); }
DI float xmax(float x) {
  auto s = __builtin_amdgcn_permlane16_swap(ubits(x), ubits(x), false, false); x = fmaxf(fbits(s[0]), fbits(s[1]));
  auto t = __builtin_amdgcn_permlane32_swap(ubits(x), ubits(x), false, false); return fmaxf(fbits(t[0]), fbits(t[1]));
}
DI void lgkm0() { asm volatile("s_waitcnt lgkmcnt(0)" ::: "memory"); }
template <int N> DI void waitvm() { asm volatile("s_waitcnt vmcnt(%0)" ::"n"(N) : "memory"); }
DI __amdgpu_buffer_rsrc_t mkrs(const void* p, unsigned bytes) { return __builtin_amdgcn_make_buffer_rsrc((void*)p, 0, bytes, 0x00020000); }
#define DMA8(NAME, POL) DI void NAME(__amdgpu_buffer_rsrc_t rs, unsigned voff, unsigned soff, unsigned lds) { \
  unsigned keep, s2; \
  asm volatile("s_mov_b32 %0, m0\n\ts_mov_b32 m0, %5\n\ts_add_u32 %1, %4, 0x1000\n\ts_nop 2\n\t" \
               "buffer_load_dwordx4 %2, %3, %4 offen " POL " lds\n\t" \
               "buffer_load_dwordx4 %2, %3, %4 offen offset:1024 " POL " lds\n\t" \
               "buffer_load_dwordx4 %2, %3, %4 offen offset:2048 " POL " lds\n\t" \
               "buffer_load_dwordx4 %2, %3, %4 offen offset:3072 " POL " lds\n\t" \
               "s_add_u32 m0, m0, 0x1000\n\ts_nop 0\n\t" \
               "buffer_load_dwordx4 %2, %3, %1 offen " POL " lds\n\t" \
               "buffer_load_dwordx4 %2, %3, %1 offen offset:1024 " POL " lds\n\t" \
               "buffer_load_dwordx4 %2, %3, %1 offen offset:2048 " POL " lds\n\t" \
               "buffer_load_dwordx4 %2, %3, %1 offen offset:3072 " POL " lds\n\t" \
               "s_mov_b32 m0, %0" \
               : "=&s"(keep), "=&s"(s2) : "v"(voff), "s"(rs), "s"(soff), "s"(lds) : "memory", "scc"); }
DMA8(dma8, "nt") DMA8(dma8d, "")
#define POLMASK 32
DI void dma8p(bool def, __amdgpu_buffer_rsrc_t rs, unsigned voff, unsigned soff, unsigned lds) { if (def) dma8d(rs, voff, soff, lds); else dma8(rs, voff, soff, lds); }
constexpr int SC1 = 16;
DI v4u gld(__amdgpu_buffer_rsrc_t rs, int off, int soff = 0) { return __builtin_amdgcn_raw_buffer_load_b128(rs, off, soff, SC1); }
DI void gst(__amdgpu_buffer_rsrc_t rs, int off, unsigned a, unsigned b, unsigned ta, unsigned tb) { __builtin_amdgcn_raw_buffer_store_b128(v4u{a, ta, b, tb}, rs, off, 0, SC1); }
DI bool gok(v4u g, unsigned ta, unsigned tb) { return g.y == ta && g.w == tb; }

#define ST(i) do { } while (0)

typedef volatile LAS unsigned* ctl_t;
template <int SLP> DI bool wait_ge(ctl_t ctl, int idx, unsigned v) {
#pragma unroll 1
  for (unsigned s = 0; rfl(ctl[idx]) < v; ++s) {
    if (s > SPIN_CAP) return false;
    __builtin_amdgcn_s_sleep(SLP);
  }
  asm volatile("" ::: "memory");
  return true;
}
DI void lds_add(LAS unsigned char* L, int idx, unsigned v, int lane) {
  lgkm0();
  if (lane == 0) __hip_atomic_fetch_add((LAS unsigned*)(L + L_CTL) + idx, v, __ATOMIC_RELAXED, __HIP_MEMORY_SCOPE_WORKGROUP);
}
DI bool last_arriver(LAS unsigned char* L, int idx, unsigned n, int lane) {
  lgkm0();
  unsigned old = 0;
  if (lane == 0) old = __hip_atomic_fetch_add((LAS unsigned*)(L + L_CTL) + idx, 1u, __ATOMIC_RELAXED, __HIP_MEMORY_SCOPE_WORKGROUP);
  old = rfl(old);
  asm volatile("" ::: "memory");
  return old == n - 1;
}
DI void read_k(LAS unsigned char* L, unsigned off, v4u (&K)[4]) {
#pragma unroll
  for (int kk = 0; kk < 4; ++kk) K[kk] = *(const LAS v4u*)(L + L_RK + (off ^ (kk * 64)));
}
DI void read_v(LAS unsigned char* L, unsigned off, v4u (&V)[8]) {
#pragma unroll
  for (int blk = 0; blk < 8; ++blk) {
    LAS unsigned char* p = L + L_RV + (off ^ ((blk >> 1) * 64)) + (blk & 1) * 32;
    const v2u x = trd(p), y = trd(p + 4096);
    V[blk] = v4u{x.x, x.y, y.x, y.y};
  }
  __builtin_amdgcn_sched_barrier(0);
}
DI v4f scores(const v4u (&K)[4], const v4u (&q)[4]) {
  v4f d = {0.f, 0.f, 0.f, 0.f};
#pragma unroll
  for (int kk = 0; kk < 4; ++kk) d = mfma(K[kk], q[kk], d);
  const unsigned x01 = pk_bf16(d[0], d[1]), x23 = pk_bf16(d[2], d[3]);
  const unsigned b01 = pk_bf16(bf_lo(x01) * SCALE, bf_hi(x01) * SCALE), b23 = pk_bf16(bf_lo(x23) * SCALE, bf_hi(x23) * SCALE);
  return v4f{bf_lo(b01), bf_hi(b01), bf_lo(b23), bf_hi(b23)};
}
DI float sel(unsigned mask, float a, float b) { return fbits(__builtin_amdgcn_bitop3_b32(mask, ubits(a), ubits(b), 0xCA)); }
DI v4f sel4(unsigned mask, v4f a, v4f b) { return v4f{sel(mask, a[0], b[0]), sel(mask, a[1], b[1]), sel(mask, a[2], b[2]), sel(mask, a[3], b[3])}; }
DI v2f sel2(unsigned mask, v2f a, v2f b) { return v2f{sel(mask, a[0], b[0]), sel(mask, a[1], b[1])}; }
DI unsigned bitmask(int x, int bit) { unsigned m = (unsigned)((x << (31 - bit)) >> 31); asm("" : "+v"(m)); return m; }
DI float max4(v4f d) { return fmaxf(fmaxf(d[0], d[1]), fmaxf(d[2], d[3])); }
DI float sum4(v4f d) { return (d[0] + d[1]) + (d[2] + d[3]); }
DI v4f expo(v4f s, float m2) { return v4f{ex2(fmaf(s[0], L2E, -m2)), ex2(fmaf(s[1], L2E, -m2)), ex2(fmaf(s[2], L2E, -m2)), ex2(fmaf(s[3], L2E, -m2))}; }
DI v4u pack_p(v4f p0, v4f p1) { return v4u{pk_bf16(p0[0], p0[1]), pk_bf16(p0[2], p0[3]), pk_bf16(p1[0], p1[1]), pk_bf16(p1[2], p1[3])}; }
constexpr int OOB = 0x70000000;
DI void emit_rec(LAS unsigned char* L, ctl_t ctl, __amdgpu_buffer_rsrc_t ws, int slot, int base, int dstr, int rec, int gi, int lane, v2f o, float m, float l, unsigned ta, unsigned tb) {
  bool now = slot >= NHOLD || rfl(ctl[C_PHASE]) >= GOAT;
  if (!now) {
    LAS unsigned char* h = L + L_HOLD + slot * HOLD_B;
    *(LAS v2f*)(h + 32 + gi * 8) = o;
    if (lane == 0) { *(LAS v4u*)h = v4u{(unsigned)(base + rec * 128), (unsigned)dstr, (unsigned)(base + 8 * dstr + rec * 16), 0u}; *(LAS v2f*)(h + 16) = v2f{m, l}; }
    lgkm0();
    if (lane == 0) __hip_atomic_fetch_or((LAS unsigned*)(L + L_CTL) + C_HELD, 1u << slot, __ATOMIC_RELAXED, __HIP_MEMORY_SCOPE_WORKGROUP);
    lgkm0();
    now = rfl(ctl[C_PHASE]) >= GOAT;
  }
  if (now) {
    gst(ws, base + (gi >> 3) * dstr + rec * 128 + (gi & 7) * 16, ubits(o.x), ubits(o.y), ta, tb);
    if (lane == 0) gst(ws, base + 8 * dstr + rec * 16, ubits(m), ubits(l), ta, tb);
  }
}
template <int NR> DI v2f merge_rec(LAS unsigned char* rec, int lane, float& Mo, float& Lo) {
  v2f av[NR];
  const v2f ml = *(LAS v2f*)(rec + (lane & (NR - 1)) * REC_B);
#pragma unroll
  for (int w = 0; w < NR; ++w) av[w] = *(LAS v2f*)(rec + w * REC_B + 16 + lane * 8);
  float M = ml.x; M = fmaxf(M, dpp<0xB1>(M)); M = fmaxf(M, dpp<0x4E>(M));
  if (NR == 8) M = fmaxf(M, dpp<0x141>(M));
  const float f = ex2(ml.x - M);
  float Ls = f * ml.y; Ls += dpp<0xB1>(Ls); Ls += dpp<0x4E>(Ls);
  if (NR == 8) Ls += dpp<0x141>(Ls);
  float a0 = 0.f, a1 = 0.f;
#pragma unroll
  for (int w = 0; w < NR; ++w) { const float fw = fbits(__builtin_amdgcn_readlane(ubits(f), w)); a0 = fmaf(fw, av[w].x, a0); a1 = fmaf(fw, av[w].y, a1); }
  Mo = M; Lo = Ls;
  return v2f{a0, a1};
}

__global__ void __launch_bounds__(NTHR) fused_decode(Args a) {
  __shared__ __attribute__((aligned(16))) unsigned char smem[L_END];
  LAS unsigned char* L = (LAS unsigned char*)smem;
  ctl_t ctl = (ctl_t)(L + L_CTL);
  asm volatile("" ::"s"(a.xA), "s"(a.kA), "s"(a.vA), "s"(a.wqA), "s"(a.woA), "s"(a.xB), "s"(a.kB), "s"(a.vB), "s"(a.wqB), "s"(a.woB), "s"(a.out), "s"(a.ws));
  const int wg = blockIdx.x, g = wg >> 3, j = wg & 7;
  const int lane = threadIdx.x & 63, w = rfl(threadIdx.x >> 6);
  const __amdgpu_buffer_rsrc_t ws = mkrs(a.ws, W_ENDB);
  ST(w == 1 ? 0 : 31);

  if (w == 0) {
    __builtin_amdgcn_s_waitcnt(0);
    const unsigned lb = (unsigned)(uintptr_t)L;
    const unsigned voff = (lane * 16) ^ ((lane >> 4) << 6);
    const unsigned offA = g * (2u << 20) + (3u << 19) + j * 16384;
    __amdgpu_buffer_rsrc_t rk = mkrs(a.kA, 64u << 20), rv = mkrs(a.vA, 64u << 20);
    dma8p(POLMASK & 1, rk, voff, offA, lb + L_RK); dma8p(POLMASK & 1, rk, voff, offA + 8192, lb + L_RK + 8192);
    dma8p(POLMASK & 1, rk, voff, offA + 131072, lb + L_RK + 16384); dma8p(POLMASK & 1, rk, voff, offA + 131072 + 8192, lb + L_RK + 24576);
    dma8p(POLMASK & 2, rv, voff, offA, lb + L_RV); dma8p(POLMASK & 2, rv, voff, offA + 8192, lb + L_RV + 8192);
    asm volatile("s_barrier" ::: "memory");
    int u = 12, pub = 0, n = 1;
    unsigned soff = offA + 131072;
#define TB()
#define TE(x)
#define ISSUE_CHUNK(df, rs, so, ld) do { \
      dma8p(df, rs, voff, so, ld); waitvm<4 * WINU>(); u += 2; if (u - WINU > pub) { pub = u - WINU; ctl[C_FULL] = pub; } \
      dma8p(df, rs, voff, (so) + 8192, (ld) + 8192); waitvm<4 * WINU>(); u += 2; if (u - WINU > pub) { pub = u - WINU; ctl[C_FULL] = pub; } } while (0)
#pragma unroll 1
    for (;; ++n) {
      const int nk = n + 1;
      unsigned t;
#pragma unroll 1
      for (unsigned sp = 0;; ++sp) {
        const unsigned tk = ctl[C_TICK + (nk >= NS ? nk - NS : 0)], fk = ctl[C_FREE + (nk & 3)], fv = ctl[C_FREE + 4 + (n & 3)];
        t = nk >= NS ? rfl(tk) : 0u;
        if (t != 0xffffffffu && rfl(fk) >= (unsigned)(nk & ~3) && rfl(fv) >= (unsigned)(n & ~3)) break;
        if (sp > SPIN_CAP) return;
        TB();
        if (sp == 6) { waitvm<0>(); pub = u; ctl[C_FULL] = pub; }
        __builtin_amdgcn_s_sleep(1);
      }
      const bool last = t >= (unsigned)POOL;
      const unsigned sv = soff;
      if (!last) {
        if (nk + AHEAD >= NS) ctl[C_WANT] = nk + AHEAD + 1 - NS;
        if (nk == 4) rk = mkrs(a.kB, 128u << 20);
        soff = nk < 4 ? offA + nk * 131072
                      : (nk < 20 ? g : 32 + g) * (2u << 20) + (((nk < 20 ? (nk - 4) * 8 + j : nk < NS ? (nk - 20) * 8 + j : NSTAT * 8 + t) + g * ROT) & 127) * 16384;
        ISSUE_CHUNK(POLMASK & (nk < 4 ? 1 : nk < 20 ? 4 : 16), rk, soff, lb + L_RK + (nk & 3) * 16384);
      } else {
        ctl[C_END] = nk; ctl[C_PHASE] = 1; ST(8);
      }
      if (n == 4) rv = mkrs(a.vB, 128u << 20);
      ISSUE_CHUNK(POLMASK & (n < 4 ? 2 : n < 20 ? 8 : 32), rv, sv, lb + L_RV + (n & 3) * 16384);
      if (last) break;
    }
    ctl[C_PHASE] = 2;
    ST(9);
#define DRAIN(k) if (WINU > k) { waitvm<4 * k>(); const int p_ = u - k + (k < 4 ? 4 : 0); if (k == 0) ctl[C_PHASE] = 3; if (p_ > pub) { pub = p_; ctl[C_FULL] = pub; } }
    DRAIN(10) DRAIN(8) DRAIN(6) DRAIN(4) DRAIN(2) DRAIN(0)
#undef DRAIN
    ST(10);
    return;
  }
  const int cb = (wg & ~15) + ((wg & 7) << 1) + ((wg >> 3) & 1);
  const int cbase = j < 4 ? 0 : 2, prod = g * 4 + (j & 3);
  unsigned ta, tb;
  int jb, jend;
  if (w == 1) {
    const v2u ep = __builtin_amdgcn_raw_buffer_load_b64(ws, W_X, 0, SC1);
    if (cb >= 128 && (cb & 3) == 0 && lane == 0) __builtin_amdgcn_raw_buffer_store_b32(0u, ws, W_CTR + ((cb - 128) >> 2) * 256, 0, SC1);
    ctl[lane] = lane == C_END ? 0x7fffffffu : lane >= C_TICK ? 0xffffffffu : 0u;
    ctl[lane + 64] = 0xffffffffu;
    asm volatile("s_waitcnt lgkmcnt(0)\n\ts_barrier" ::: "memory");
    ta = rfl(ep.x) + 1u; tb = rfl(ep.y) + 1u;
    ta += ta == 0u; tb += tb == 0u;
    waitvm<0>();
    if (!wait_ge<1>(ctl, C_QCNT, 9)) return;
    ST(1);
    if (lane < 16) {
      v4f s = {0.f, 0.f, 0.f, 0.f};
#pragma unroll
      for (int i = 0; i < 9; ++i) s += *(LAS v4f*)(L + L_QP + i * 256 + lane * 16);
      const int b = lane >> 3, quad = lane & 7;
      const int off = cb < 128 ? W_QA + ((b * 16 + (cb >> 3)) * 64 + (cb & 7) * 8 + quad) * 16 : W_QB + (((cb - 128) >> 2) * 64 + b * 32 + (cb & 3) * 8 + quad) * 16;
      gst(ws, off, pk_bf16(s.x, s.y), pk_bf16(s.z, s.w), ta, tb);
    }
    jb = 0; jend = 3;
  } else if (w == 2) {
    asm volatile("s_barrier" ::: "memory");
    if (!wait_ge<16>(ctl, C_WANT, 1)) return;
    if (!wait_ge<2>(ctl, C_PHASE, GOAT)) return;
    ta = rfl(ctl[C_TAGA]); tb = rfl(ctl[C_TAGB]);
    unsigned held = rfl(ctl[C_HELD]);
    asm volatile("" ::: "memory");
#pragma unroll 1
    while (held) {
      const int slot = __builtin_ctz(held);
      held &= held - 1;
      LAS unsigned char* h = L + L_HOLD + slot * HOLD_B;
      const v4u hd = *(LAS v4u*)h;
      const v2u ml = *(LAS v2u*)(h + 16), o = *(LAS v2u*)(h + 32 + lane * 8);
      gst(ws, hd.x + (lane >> 3) * hd.y + (lane & 7) * 16, o.x, o.y, ta, tb);
      if (lane == 0) gst(ws, hd.z, ml.x, ml.y, ta, tb);
    }
    ST(14);
    jb = 1; jend = 3;
  } else {
    {
      {
        const int qw = w - 3;
        const int r0 = qw * 64 + (lane >> 2) * 4;
        const unsigned char* xp = (cb < 128 ? a.xA : a.xB) + r0 * 2;
        const unsigned char* wp = (cb < 128 ? a.wqA : a.wqB) + (size_t)r0 * 8192 + (cb & 127) * 64 + (lane & 3) * 16;
        v4u Wq[4];
#pragma unroll
        for (int i = 0; i < 4; ++i) Wq[i] = __builtin_nontemporal_load((const GAS v4u*)(wp + i * 8192));
        const v2u X0 = *(const GAS v2u*)xp, X1 = *(const GAS v2u*)(xp + 1152);
        asm volatile("s_barrier" ::: "memory");
        float ac[2][8];
#pragma unroll
        for (int k = 0; k < 8; ++k) { ac[0][k] = 0.f; ac[1][k] = 0.f; }
#pragma unroll
        for (int s = 0; s < 2; ++s)
#pragma unroll
          for (int d = 0; d < 4; ++d) {
            const unsigned lo = __builtin_amdgcn_perm(Wq[2 * s + 1][d], Wq[2 * s][d], 0x05040100u), hi = __builtin_amdgcn_perm(Wq[2 * s + 1][d], Wq[2 * s][d], 0x07060302u);
            ac[0][2 * d] = dot2(lo, X0[s], ac[0][2 * d]); ac[0][2 * d + 1] = dot2(hi, X0[s], ac[0][2 * d + 1]);
            ac[1][2 * d] = dot2(lo, X1[s], ac[1][2 * d]); ac[1][2 * d + 1] = dot2(hi, X1[s], ac[1][2 * d + 1]);
          }
        float b8[8], c4[4];
#pragma unroll
        for (int k = 0; k < 8; ++k) b8[k] = rs32(ac[0][k], ac[1][k]);
#pragma unroll
        for (int k = 0; k < 4; ++k) { float t = rs16(b8[k], b8[k + 4]); t += dpp<0x128>(t); t += dpp<0x124>(t); c4[k] = t; }
        if ((lane & 12) == 0) *(LAS v4f*)(L + L_QP + qw * 256 + (lane >> 5) * 128 + (lane & 3) * 32 + ((lane >> 4) & 1) * 16) = v4f{c4[0], c4[1], c4[2], c4[3]};
        lds_add(L, C_QCNT, 1, lane);
      }
      const int r = lane >> 4, c = lane & 15;
      const unsigned kof = c * 256 + r * 16 + (c & 3) * 64;
      const unsigned vof = (4 * r + (c >> 2)) * 256 + (c >> 2) * 64 + (c & 3) * 8;
      if (w >= 4) {
        const unsigned aw = w - 4, off = aw * 8192, an = aw >> 1, ah = 2 * (aw & 1) + 2;
        v4u K0[4], K1[4], V[8];
        if (!wait_ge<1>(ctl, C_FULL, (an ? 8 * an - 4 : 0) + ah)) return;
        read_k(L, off + kof, K0); read_k(L, off + 4096 + kof, K1);
        lds_add(L, C_FREE + (aw >> 1), 2, lane);
        if (!wait_ge<1>(ctl, C_FULL, 8 * an + 8 + ah)) return;
        read_v(L, off + vof, V);
        lds_add(L, C_FREE + 4 + (aw >> 1), 2, lane);
        if (!wait_ge<1>(ctl, C_QRDY, 1)) return;
        if (w == 4) ST(3);
        ta = rfl(ctl[C_TAGA]); tb = rfl(ctl[C_TAGB]);
        const LAS v4u* qp = (const LAS v4u*)(L + L_Q) + r + (c >> 3) * 16;
        const v4u q[4] = {qp[0], qp[4], qp[8], qp[12]};
        v4f s0 = scores(K0, q), s1 = scores(K1, q);
        const float M = xmax(fmaxf(max4(s0), max4(s1))) * L2E;
        s0 = expo(s0, M); s1 = expo(s1, M);
        const v4u P = pack_p(s0, s1);
        const float Ls = xsum(sum4(s0) + sum4(s1));
        v4f acc[8];
#pragma unroll
        for (int blk = 0; blk < 8; ++blk) acc[blk] = mfma(V[blk], P, v4f{0.f, 0.f, 0.f, 0.f});
#pragma unroll
        for (int k = 0; k < 4; ++k) acc[k] = sel4(bitmask(c, 2), acc[k + 4], acc[k]);
#pragma unroll
        for (int k = 0; k < 2; ++k) acc[k] = sel4(bitmask(c, 1), acc[k + 2], acc[k]);
        acc[0] = sel4(bitmask(c, 0), acc[1], acc[0]);
        LAS unsigned char* rec = L + L_REC + ((c >> 3) * 8 + aw) * REC_B;
        *(LAS v4f*)(rec + 16 + ((c & 7) * 16 + r * 4) * 4) = acc[0];
        if ((lane & 0x37) == 0) *(LAS v2f*)rec = v2f{M, Ls};
        if (w == 4) ST(15);
        if (last_arriver(L, C_CNT, 8, lane)) {
#pragma unroll 1
          for (int e = 0; e < 2; ++e) {
            float Mo, Lo;
            const v2f o = merge_rec<8>(L + L_REC + e * 8 * REC_B, lane, Mo, Lo);
            emit_rec(L, ctl, ws, e, W_P + (g * 2 + e) * P_STR, 1024, j, lane, lane, o, Mo, Lo, ta, tb);
          }
          ST(4);
        }
      }
      if (w >= 8) {
        const int cw = w - 8;
        const unsigned off = cw * 16384;
        v4u q[4];
        float m = -INFINITY, l = 0.f;
        v4f acc[8];
        bool fresh = true;
        const unsigned mk0 = bitmask(c, 0), mk1 = bitmask(c, 1), mk2 = bitmask(c, 2), mk3 = bitmask(c, 3);
#pragma unroll 1
        for (unsigned n = 4 + cw;; n += 4) {
          if (fresh) {
            const LAS v4u* qp = (const LAS v4u*)(L + L_Q) + r + (n < 20 ? 32 : 48);
#pragma unroll
            for (int kk = 0; kk < 4; ++kk) q[kk] = qp[4 * kk];
            m = -INFINITY; l = 0.f;
#pragma unroll
            for (int blk = 0; blk < 8; ++blk) acc[blk] = v4f{0.f, 0.f, 0.f, 0.f};
            fresh = false;
          }
          bool stop = false;
#pragma unroll 1
          for (unsigned sp = 0;; ++sp) {
            const v2u fe = *(volatile LAS v2u*)(L + L_CTL);
            if (n >= rfl(fe.y) || sp > SPIN_CAP) { stop = true; break; }
            if (rfl(fe.x) >= 8 * n) break;
            __builtin_amdgcn_s_sleep(1);
          }
          if (stop) break;
          asm volatile("" ::: "memory");
          v4f s[4];
          v4u K[4][4];
#pragma unroll
          for (int uu = 0; uu < 4; ++uu) read_k(L, off + uu * 4096 + kof, K[uu]);
          __builtin_amdgcn_sched_barrier(0);
#pragma unroll
          for (int uu = 0; uu < 4; ++uu) s[uu] = scores(K[uu], q);
          lds_add(L, C_FREE + cw, 4, lane);
          const float mn = fmaxf(xmax(fmaxf(fmaxf(max4(s[0]), max4(s[1])), fmaxf(max4(s[2]), max4(s[3])))) * L2E, m), al = ex2(m - mn);
          m = mn;
#pragma unroll
          for (int uu = 0; uu < 4; ++uu) s[uu] = expo(s[uu], mn);
          v4u P0 = pack_p(s[0], s[1]), P1 = pack_p(s[2], s[3]);
          l = fmaf(l, al, (sum4(s[0]) + sum4(s[1])) + (sum4(s[2]) + sum4(s[3])));
#pragma unroll
          for (int blk = 0; blk < 8; ++blk) acc[blk] *= al;
          const bool pubn = n >= NS - 4 || (n & ~3u) == 16;
          float Ls = xsum(l);
          asm volatile("" : "+v"(P0), "+v"(P1), "+v"(Ls), "+v"(acc[0]), "+v"(acc[1]), "+v"(acc[2]), "+v"(acc[3]), "+v"(acc[4]), "+v"(acc[5]), "+v"(acc[6]), "+v"(acc[7]));
          v4u V[8];
          if (!wait_ge<1>(ctl, C_FULL, 8 * n + 10)) return;
          read_v(L, off + vof, V);
#pragma unroll
          for (int blk = 0; blk < 8; ++blk) acc[blk] = mfma(V[blk], P0, acc[blk]);
          if (!wait_ge<1>(ctl, C_FULL, 8 * n + 12)) return;
          read_v(L, off + 8192 + vof, V);
#pragma unroll
          for (int blk = 0; blk < 8; ++blk) acc[blk] = mfma(V[blk], P1, acc[blk]);
          lds_add(L, C_FREE + 4 + cw, 4, lane);
          if (pubn) {
            v2f o[8];
#pragma unroll
            for (int k = 0; k < 8; ++k) o[k] = sel2(mk0, v2f{acc[k][2], acc[k][3]}, v2f{acc[k][0], acc[k][1]});
#pragma unroll
            for (int k = 0; k < 4; ++k) o[k] = sel2(mk3, o[k + 4], o[k]);
#pragma unroll
            for (int k = 0; k < 2; ++k) o[k] = sel2(mk2, o[k + 2], o[k]);
            o[0] = sel2(mk1, o[1], o[0]);
            int slot = 4 + n - NS, rec = 0, gi = (c >> 1) * 8 + 2 * r + (c & 1);
            bool emit = true;
            if (n >= NS) rec = 8 + rfl(ctl[C_TICK + n - NS]);
            else {
              const int b = n >= 20;
              LAS unsigned char* rp = L + L_REC + (16 + b * 4) * REC_B;
              *(LAS v2f*)(rp + cw * REC_B + 16 + gi * 8) = o[0];
              if (lane == 0) *(LAS v2f*)(rp + cw * REC_B) = v2f{m, Ls};
              emit = last_arriver(L, C_CNT + 1 + b, 4, lane);
              if (emit) o[0] = merge_rec<4>(rp, lane, m, Ls);
              slot = 2 + b; rec = j; gi = lane;
            }
            if (emit) {
              emit_rec(L, ctl, ws, slot, n >= 20 ? W_P1 + g * P1_STR : W_P + (64 + g) * P_STR, n >= 20 ? 8192 : 1024, rec, gi, lane, o[0], m, Ls, ta, tb);
              ST(n >= 20 ? 7 : 5);
            }
            fresh = true;
          }
        }
        jb = 3; jend = 4;
      } else {
      const int k = w == 3 ? 4 : w - 4;
      if (WOAT) { if (!wait_ge<16>(ctl, C_WANT, 1)) return; if (!wait_ge<2>(ctl, C_PHASE, WOAT)) return; }
      else if (!wait_ge<8>(ctl, C_QRDY, 1)) return;
      ta = rfl(ctl[C_TAGA]); tb = rfl(ctl[C_TAGB]);
      unsigned Wo[32];
      {
        const int head = j < 4 ? 2 * (g & 15) + (g >> 4) : g;
        const unsigned char* pw = (j < 4 ? a.woA : a.woB) + (size_t)(head * 128 + (j & 3) * 32) * 1152 + k * 256 + (k < 4 ? lane : lane & 31) * 4;
#pragma unroll
        for (int i = 0; i < 32; ++i) Wo[i] = __builtin_nontemporal_load((const GAS unsigned*)(pw + i * 1152));
#pragma unroll
        for (int s = 0; s < 16; ++s) {
          const unsigned a0 = Wo[2 * s], a1 = Wo[2 * s + 1];
          Wo[2 * s] = __builtin_amdgcn_perm(a1, a0, 0x05040100u); Wo[2 * s + 1] = __builtin_amdgcn_perm(a1, a0, 0x07060302u);
        }
#pragma unroll
        for (int s = 0; s < 32; s += 4) asm volatile("" : "+v"(Wo[s]), "+v"(Wo[s + 1]), "+v"(Wo[s + 2]), "+v"(Wo[s + 3]));
      }
      if (!wait_ge<16>(ctl, C_PHASE, 1)) return;
      int yoff = W_Y + ((cbase * 36 + k * 8 + (lane >> 3)) * 128 + prod) * 128 + (lane & 7) * 16;
      if (k == 4 && lane >= 32) yoff = OOB;
#pragma unroll 1
      for (int i = 0; i < 2; ++i) {
        if (!wait_ge<1>(ctl, C_OSEQ + i, 2)) return;
        const LAS v4u* op = (const LAS v4u*)(L + L_OP + i * 64);
        float y0 = 0.f, y1 = 0.f, z0 = 0.f, z1 = 0.f;
#pragma unroll
        for (int h = 0; h < 2; ++h) {
          const v4u oa = op[2 * h], ob = op[2 * h + 1];
#pragma unroll
          for (int s = 0; s < 4; ++s) {
            y0 = dot2(Wo[16 * h + 2 * s], oa[s], y0); y1 = dot2(Wo[16 * h + 2 * s + 1], oa[s], y1);
            z0 = dot2(Wo[16 * h + 8 + 2 * s], ob[s], z0); z1 = dot2(Wo[16 * h + 9 + 2 * s], ob[s], z1);
          }
        }
        gst(ws, yoff + i * (36 * 16384), ubits(y0 + z0), ubits(y1 + z1), ta, tb);
        if (w == 4) { ctl[C_YPUB] = i + 1; if (j >= 4 && i == 1) ST(12); }
      }
      return;
      }
    }
  }
#pragma unroll 1
  for (; jb < jend; ++jb) {
    if (jb == 1 && w == 1 && lane == 0) {
      const unsigned char* ctr = a.ws + W_CTR + g * 256;
      const unsigned zero = 0, one = 1;
      unsigned t0, t1, t2 = 0;
#define TICKET(t) asm volatile("global_atomic_add %0, %1, %2, %3 sc0" : "=v"(t) : "v"(zero), "v"(one), "s"(ctr) : "memory")
#define STEP(tnew, told, kk) \
  if (!wait_ge<2>(ctl, C_WANT, (kk) + AHEAD)) break; \
  TICKET(tnew); \
  asm volatile("s_waitcnt vmcnt(%1)" : "+v"(told) : "n"(AHEAD - 1) : "memory"); \
  ctl[C_TICK + (kk)] = told; \
  if (rfl(told) >= (unsigned)POOL) break;
      if (wait_ge<8>(ctl, C_WANT, 1)) {
        TICKET(t0);
        if (wait_ge<2>(ctl, C_WANT, 2)) {
          TICKET(t1);
#pragma unroll 1
          for (int kk = 0; kk <= POOL; kk += 3) { STEP(t2, t0, kk) STEP(t0, t1, kk + 1) STEP(t1, t2, kk + 2) }
        }
      }
      asm volatile("s_waitcnt vmcnt(0)" : "+v"(t0), "+v"(t1), "+v"(t2)::"memory");
    }
    const bool fin = jb == 3;
    const int c = fin ? cbase + (prod & 1) : cbase + jb - 1, rho = prod >> 1;
    int voff, voff7;
    unsigned mask;
    unsigned long long ign7 = 0;
    if (fin) {
      if (rho >= 36) return;
      voff7 = voff = W_Y + (c * 36 + rho) * 16384 + (w - 8) * 4096 + lane * 16;
      mask = 0x0fu;
      if (!wait_ge<1>(ctl, C_YPUB, (prod & 1) + 1)) return;
    } else if (jb == 0) {
      voff = W_QA + g * 1024 + lane * 16;
      voff7 = voff + (W_QB - W_QA) - 7 * 1024;
      mask = 0x81u;
    } else {
      const int base = c < 2 ? W_P + ((c * 16 + (g & 15)) * 2 + (g >> 4)) * P_STR : c == 2 ? W_P + (64 + g) * P_STR : W_P1 + g * P1_STR;
      const int dstr = c == 3 ? 8192 : 1024, NB = c == 3 ? NB1 : 1;
      voff = base + (2 * (j & 3) + w - 1) * dstr + lane * 16;
      voff7 = base + 8 * dstr - 7 * 1024 + (lane < 8 * NB ? lane : 0) * 16;
      mask = ((1u << NB) - 1u) | 0x80u;
      ign7 = ~0ull << (8 * NB);
      if (!wait_ge<2>(ctl, C_PHASE, GOAT)) return;
    }
    v4u A[8], B[8];
    v2u y[8];
    unsigned long long dn[8];
#pragma unroll
    for (int i = 0; i < 8; ++i) { y[i] = v2u{0u, 0u}; dn[i] = (mask >> i & 1) ? 0ull : ~0ull; }
#define ISSUE(X) _Pragma("unroll") for (int i = 0; i < 8; ++i) X[i] = gld(ws, i == 7 ? voff7 : voff, dn[i] != ~0ull ? i * 1024 : OOB); __builtin_amdgcn_sched_barrier(0);
#define CHECK(X) _Pragma("unroll") for (int i = 0; i < 8; ++i) { const bool ok = gok(X[i], ta, tb); y[i].x = ok ? X[i].x : y[i].x; y[i].y = ok ? X[i].z : y[i].y; \
      dn[i] |= __builtin_amdgcn_ballot_w64(X[i].y == ta) & __builtin_amdgcn_ballot_w64(X[i].w == tb); } \
    if (((dn[0] & dn[1]) & (dn[2] & dn[3]) & (dn[4] & dn[5]) & (dn[6] & dn[7])) == ~0ull) break;
    ISSUE(A)
    __builtin_amdgcn_s_sleep(PSTAG);
    unsigned sp = 0;
#pragma unroll 1
    for (; sp < SPIN_CAP; ++sp) {
      ISSUE(B)
      CHECK(A)
      ISSUE(A)
      CHECK(B)
    }
#undef ISSUE
#undef CHECK
    if (sp >= SPIN_CAP) return;
    if (jb == 0) {
      *(LAS v2u*)(L + L_Q + lane * 8) = y[0];
      *(LAS v2u*)(L + L_Q + 512 + lane * 8) = y[7];
      ctl[C_TAGA] = ta; ctl[C_TAGB] = tb;
      lgkm0();
      ctl[C_QRDY] = 1;
      ST(2);
    } else if (fin) {
      const float s0 = (fbits(y[0].x) + fbits(y[1].x)) + (fbits(y[2].x) + fbits(y[3].x)), s1 = (fbits(y[0].y) + fbits(y[1].y)) + (fbits(y[2].y) + fbits(y[3].y));
      float s = rs32(s0, s1);
      s = rs16(s, s);
      s += dpp<0x128>(s);
      LAS float* fp = (LAS float*)(L + L_FP);
      if ((lane & 24) == 0) fp[((lane & 7) * 2 + (lane >> 5)) * 4 + w - 8] = s;
      if (last_arriver(L, C_FCNT, 4, lane)) {
        if (lane < 16) {
          const v4f p = *(LAS v4f*)(fp + lane * 4);
          const float t = (p.x + p.y) + (p.z + p.w);
          *(GAS unsigned short*)(a.out + ((c & 1) * 1152 + (c >> 1) * 576 + rho * 16 + lane) * 2) = (unsigned short)pk_bf16(t, t);
        }
        if (c == 3 && rho == 0 && lane == 0) __builtin_amdgcn_raw_buffer_store_b64(v2u{ta, tb}, ws, W_X, 0, SC1);
        ST(c == 3 ? 13 : 6);
      }
    } else {
      if (c == 3 && w == 2) ST(11);
      const float mr = fbits(y[7].x);
      float M = fmaxf(mr, dpp<0xB1>(mr)); M = fmaxf(M, dpp<0x4E>(M)); M = fmaxf(M, dpp<0x141>(M)); M = fmaxf(M, dpp<0x128>(M)); M = xmax(M);
      const float f = (ign7 >> lane & 1) ? 0.f : ex2(mr - M);
      float o0 = 0.f, o1 = 0.f;
#pragma unroll
      for (int b = 0; b < NB1; ++b) {
        const float fb = fbits(__builtin_amdgcn_ds_bpermute((8 * b + (lane >> 3)) * 4, ubits(f)));
        o0 = fmaf(fb, fbits(y[b].x), o0); o1 = fmaf(fb, fbits(y[b].y), o1);
      }
      float Ls = f * fbits(y[7].y);
      Ls += dpp<0xB1>(Ls); Ls += dpp<0x4E>(Ls); Ls += dpp<0x141>(Ls); Ls += dpp<0x128>(Ls); Ls = xsum(Ls);
      float o = rs32(o0, o1);
      o = rs16(o, o);
      o += dpp<0x128>(o);
      o *= __builtin_amdgcn_rcpf(Ls);
      if ((lane & 24) == 0) *(LAS unsigned short*)(L + L_OP + (jb - 1) * 64 + (w - 1) * 32 + (lane & 7) * 4 + (lane >> 5) * 2) = (unsigned short)pk_bf16(o, o);
      lds_add(L, C_OSEQ + jb - 1, 1, lane);
    }
    asm volatile("" ::"v"(A[0]), "v"(A[1]), "v"(A[2]), "v"(A[3]), "v"(A[4]), "v"(A[5]), "v"(A[6]), "v"(A[7]) : "memory");
    asm volatile("" ::"v"(B[0]), "v"(B[1]), "v"(B[2]), "v"(B[3]), "v"(B[4]), "v"(B[5]), "v"(B[6]), "v"(B[7]) : "memory");
  }
}

extern "C" void kernel_launch(void* const* d_in, const int* in_sizes, int n_in, void* d_out, int out_size, void* d_ws, size_t ws_size,
                              hipStream_t stream) {
  Args a;
  a.xA = (const unsigned char*)d_in[0]; a.kA = (const unsigned char*)d_in[1]; a.vA = (const unsigned char*)d_in[2];
  a.wqA = (const unsigned char*)d_in[3]; a.woA = (const unsigned char*)d_in[4];
  a.xB = (const unsigned char*)d_in[5]; a.kB = (const unsigned char*)d_in[6]; a.vB = (const unsigned char*)d_in[7];
  a.wqB = (const unsigned char*)d_in[8]; a.woB = (const unsigned char*)d_in[9];
  a.out = (unsigned char*)d_out; a.ws = (unsigned char*)d_ws;
  fused_decode<<<256, NTHR, 0, stream>>>(a);
}
```

```cpp
#include <hip/hip_runtime.h>
#include <stdint.h>

typedef unsigned v4u __attribute__((ext_vector_type(4)));
typedef unsigned v2u __attribute__((ext_vector_type(2)));
typedef float v4f __attribute__((ext_vector_type(4)));
typedef float v2f __attribute__((ext_vector_type(2)));
typedef short v4s __attribute__((ext_vector_type(4)));
typedef __bf16 bf16x2 __attribute__((ext_vector_type(2)));
typedef __bf16 bf16x8 __attribute__((ext_vector_type(8)));
#define LAS __attribute__((address_space(3)))
#define GAS __attribute__((address_space(1)))
#define DI __device__ __forceinline__

#define NSTAT 10
#define WINU 8
#define ROT 8
#define PSTAG 8
#define AHEAD 2
#define GOAT 1
#define NEAR 10
#define POLLAT 2
#define DRY 0
#define YPAD 0
#define DPAD 0
#define TOUCH 1
#define DT1 90
#define DT2 80
#define FLAGK 0
#define NREP 16
#define STOUCH 1
#define LDLAST 0
#define WOAT 0
constexpr int NWAVE = 12, NTHR = NWAVE * 64;
constexpr int NS = 4 + 16 + NSTAT;
constexpr int POOL = (16 - NSTAT) * 8;
constexpr int NB1 = 1 + POOL / 8;
static_assert(NB1 == 7 && NSTAT >= 4 && WINU % 2 == 0 && WINU >= 4 && WINU <= 12, "");
constexpr unsigned SPIN_CAP = 1u << 18;
constexpr int NHOLD = 16, HOLD_B = 544, REC_B = 528;
constexpr int L_RK = 0, L_RV = 65536, L_CTL = 131072, L_Q = L_CTL + 512, L_OP = L_Q + 1024, L_FP = L_OP + 128, L_QP = L_FP + 256, L_REC = L_QP + 9 * 256,
              L_HOLD = L_REC + 24 * REC_B, L_END = L_HOLD + NHOLD * HOLD_B;
static_assert(L_END <= 163840, "");
enum { C_FULL = 0, C_END = 1, C_QRDY = 3, C_WANT = 4, C_QCNT = 5, C_TAGA = 6, C_TAGB = 7, C_FREE = 8  , C_CNT = 16, C_YPUB = 19,
       C_PHASE = 20, C_HELD = 21, C_OSEQ = 22  , C_FCNT = 24, C_DUMMY = 25, C_TICK = 28, C_WORDS = 28 + POOL + 8 };
static_assert(C_WORDS * 4 <= 512, "");
constexpr int W_X = 0, W_CTR = 4096, W_F1 = 16384  , W_F2 = W_F1 + 32768  , W_QA = W_F2 + 2 * NREP * 1024, W_QB = W_QA + 32768, W_P = W_QB + 32768, P_STR = 9216  ,
              W_P1 = W_P + 96 * P_STR, D1 = 8192 + DPAD, P1_STR = 8 * D1 + 1024 + 256  , YS = 16384 + YPAD, W_Y = W_P1 + 32 * P1_STR,
              W_ENDB = 256 + W_Y + 4 * 36 * YS  , W_STAMP = (W_ENDB + 4095) / 4096 * 4096;
constexpr int W_SAFE1 = W_P1 + 24 * P1_STR + 7424, W_SAFE2 = W_ENDB - 128;
static_assert(W_SAFE1 >> 21 == 1 && W_SAFE2 >> 21 == 2, "");
constexpr float SCALE = 0.088388347648318440f, L2E = 1.4426950408889634f;

struct Args {
  const unsigned char *xA, *kA, *vA, *wqA, *woA, *xB, *kB, *vB, *wqB, *woB;
  unsigned char *out, *ws;
};

DI float dot2(unsigned a, unsigned b, float c) { return __builtin_amdgcn_fdot2_f32_bf16(__builtin_bit_cast(bf16x2, a), __builtin_bit_cast(bf16x2, b), c, false); }
template <int CTRL> DI float dpp(float x) { return __builtin_bit_cast(float, __builtin_amdgcn_mov_dpp(__builtin_bit_cast(int, x), CTRL, 0xf, 0xf, true)); }
DI unsigned pk_bf16(float lo, float hi) {
  unsigned r = __builtin_bit_cast(unsigned, bf16x2{(__bf16)lo, (__bf16)hi});
  asm("" : "+v"(r));
  return r;
}
DI float bf_lo(unsigned u) { return __builtin_bit_cast(float, u << 16); }
DI float bf_hi(unsigned u) { return __builtin_bit_cast(float, u & 0xffff0000u); }
DI float fbits(unsigned u) { return __builtin_bit_cast(float, u); }
DI unsigned ubits(float f) { return __builtin_bit_cast(unsigned, f); }
DI unsigned rfl(unsigned x) { return __builtin_amdgcn_readfirstlane(x); }
DI float ex2(float x) { return __builtin_amdgcn_exp2f(x); }
DI v4f mfma(v4u a, v4u b, v4f c) { return __builtin_amdgcn_mfma_f32_16x16x32_bf16(__builtin_bit_cast(bf16x8, a), __builtin_bit_cast(bf16x8, b), c, 0, 0, 0); }
DI v2u trd(LAS unsigned char* p) { return __builtin_bit_cast(v2u, __builtin_amdgcn_ds_read_tr16_b64_v4i16((LAS v4s*)p)); }
DI float rs32(float x, float y) { auto s = __builtin_amdgcn_permlane32_swap(ubits(x), ubits(y), false, false); return fbits(s[0]) + fbits(s[1]); }
DI float rs16(float x, float y) { auto s = __builtin_amdgcn_permlane16_swap(ubits(x), ubits(y), false, false); return fbits(s[0]) + fbits(s[1]); }
DI float xsum(float x) { x = rs16(x, x); return rs32(x, x); }
DI float xmax(float x) {
  auto s = __builtin_amdgcn_permlane16_swap(ubits(x), ubits(x), false, false); x = fmaxf(fbits(s[0]), fbits(s[1]));
  auto t = __builtin_amdgcn_permlane32_swap(ubits(x), ubits(x), false, false); return fmaxf(fbits(t[0]), fbits(t[1]));
}
DI void lgkm0() { asm volatile("s_waitcnt lgkmcnt(0)" ::: "memory"); }
template <int N> DI void waitvm() { asm volatile("s_waitcnt vmcnt(%0)" ::"n"(N) : "memory"); }
DI __amdgpu_buffer_rsrc_t mkrs(const void* p, unsigned bytes) { return __builtin_amdgcn_make_buffer_rsrc((void*)p, 0, bytes, 0x00020000); }
#define DMA8(NAME, POL) DI void NAME(__amdgpu_buffer_rsrc_t rs, unsigned voff, unsigned soff, unsigned lds) { \
  unsigned keep, s2; \
  asm volatile("s_mov_b32 %0, m0\n\ts_mov_b32 m0, %5\n\ts_add_u32 %1, %4, 0x1000\n\ts_nop 2\n\t" \
               "buffer_load_dwordx4 %2, %3, %4 offen " POL " lds\n\t" \
               "buffer_load_dwordx4 %2, %3, %4 offen offset:1024 " POL " lds\n\t" \
               "buffer_load_dwordx4 %2, %3, %4 offen offset:2048 " POL " lds\n\t" \
               "buffer_load_dwordx4 %2, %3, %4 offen offset:3072 " POL " lds\n\t" \
               "s_add_u32 m0, m0, 0x1000\n\ts_nop 0\n\t" \
               "buffer_load_dwordx4 %2, %3, %1 offen " POL " lds\n\t" \
               "buffer_load_dwordx4 %2, %3, %1 offen offset:1024 " POL " lds\n\t" \
               "buffer_load_dwordx4 %2, %3, %1 offen offset:2048 " POL " lds\n\t" \
               "buffer_load_dwordx4 %2, %3, %1 offen offset:3072 " POL " lds\n\t" \
               "s_mov_b32 m0, %0" \
               : "=&s"(keep), "=&s"(s2) : "v"(voff), "s"(rs), "s"(soff), "s"(lds) : "memory", "scc"); }
DMA8(dma8, "nt") DMA8(dma8d, "")
#define POLMASK 0
DI void dma8p(bool def, __amdgpu_buffer_rsrc_t rs, unsigned voff, unsigned soff, unsigned lds) { if (def) dma8d(rs, voff, soff, lds); else dma8(rs, voff, soff, lds); }
constexpr int SC1 = 16;
DI v4u gld(__amdgpu_buffer_rsrc_t rs, int off, int soff = 0) { return __builtin_amdgcn_raw_buffer_load_b128(rs, off, soff, SC1); }
DI void gst(__amdgpu_buffer_rsrc_t rs, int off, unsigned a, unsigned b, unsigned ta, unsigned tb) { __builtin_amdgcn_raw_buffer_store_b128(v4u{a, ta, b, tb}, rs, off, 0, SC1); }
DI bool gok(v4u g, unsigned ta, unsigned tb) { return g.y == ta && g.w == tb; }

#define ST(i) do { } while (0)

typedef volatile LAS unsigned* ctl_t;
template <int SLP> DI bool wait_ge(ctl_t ctl, int idx, unsigned v) {
#pragma unroll 1
  for (unsigned s = 0; rfl(ctl[idx]) < v; ++s) {
    if (s > SPIN_CAP) return false;
    __builtin_amdgcn_s_sleep(SLP);
  }
  asm volatile("" ::: "memory");
  return true;
}
DI void lds_add(LAS unsigned char* L, int idx, unsigned v, int lane) {
  lgkm0();
  if (lane == 0) __hip_atomic_fetch_add((LAS unsigned*)(L + L_CTL) + idx, v, __ATOMIC_RELAXED, __HIP_MEMORY_SCOPE_WORKGROUP);
}
DI bool last_arriver(LAS unsigned char* L, int idx, unsigned n, int lane) {
  lgkm0();
  unsigned old = 0;
  if (lane == 0) old = __hip_atomic_fetch_add((LAS unsigned*)(L + L_CTL) + idx, 1u, __ATOMIC_RELAXED, __HIP_MEMORY_SCOPE_WORKGROUP);
  old = rfl(old);
  asm volatile("" ::: "memory");
  return old == n - 1;
}
DI void read_k(LAS unsigned char* L, unsigned off, v4u (&K)[4]) {
#pragma unroll
  for (int kk = 0; kk < 4; ++kk) K[kk] = *(const LAS v4u*)(L + L_RK + (off ^ (kk * 64)));
}
DI void read_v(LAS unsigned char* L, unsigned off, v4u (&V)[8]) {
#pragma unroll
  for (int blk = 0; blk < 8; ++blk) {
    LAS unsigned char* p = L + L_RV + (off ^ ((blk >> 1) * 64)) + (blk & 1) * 32;
    const v2u x = trd(p), y = trd(p + 4096);
    V[blk] = v4u{x.x, x.y, y.x, y.y};
  }
  __builtin_amdgcn_sched_barrier(0);
}
DI v4f scores(const v4u (&K)[4], const v4u (&q)[4]) {
  v4f d = {0.f, 0.f, 0.f, 0.f};
#pragma unroll
  for (int kk = 0; kk < 4; ++kk) d = mfma(K[kk], q[kk], d);
  const unsigned x01 = pk_bf16(d[0], d[1]), x23 = pk_bf16(d[2], d[3]);
  const unsigned b01 = pk_bf16(bf_lo(x01) * SCALE, bf_hi(x01) * SCALE), b23 = pk_bf16(bf_lo(x23) * SCALE, bf_hi(x23) * SCALE);
  return v4f{bf_lo(b01), bf_hi(b01), bf_lo(b23), bf_hi(b23)};
}
DI float sel(unsigned mask, float a, float b) { return fbits(__builtin_amdgcn_bitop3_b32(mask, ubits(a), ubits(b), 0xCA)); }
DI v4f sel4(unsigned mask, v4f a, v4f b) { return v4f{sel(mask, a[0], b[0]), sel(mask, a[1], b[1]), sel(mask, a[2], b[2]), sel(mask, a[3], b[3])}; }
DI v2f sel2(unsigned mask, v2f a, v2f b) { return v2f{sel(mask, a[0], b[0]), sel(mask, a[1], b[1])}; }
DI unsigned bitmask(int x, int bit) { unsigned m = (unsigned)((x << (31 - bit)) >> 31); asm("" : "+v"(m)); return m; }
DI float max4(v4f d) { return fmaxf(fmaxf(d[0], d[1]), fmaxf(d[2], d[3])); }
DI float sum4(v4f d) { return (d[0] + d[1]) + (d[2] + d[3]); }
DI v4f expo(v4f s, float m2) { return v4f{ex2(fmaf(s[0], L2E, -m2)), ex2(fmaf(s[1], L2E, -m2)), ex2(fmaf(s[2], L2E, -m2)), ex2(fmaf(s[3], L2E, -m2))}; }
DI v4u pack_p(v4f p0, v4f p1) { return v4u{pk_bf16(p0[0], p0[1]), pk_bf16(p0[2], p0[3]), pk_bf16(p1[0], p1[1]), pk_bf16(p1[2], p1[3])}; }
constexpr int OOB = 0x70000000;
DI unsigned now32() { return (unsigned)__builtin_amdgcn_s_memrealtime(); }
DI void hint_set(__amdgpu_buffer_rsrc_t ws, int off) {
  const unsigned t = now32();
  asm volatile("buffer_atomic_swap %0, %1, %2, 0 offen" ::"v"(t), "v"(off), "s"(ws) : "memory");
}
DI void hint_wait(__amdgpu_buffer_rsrc_t ws, int off, unsigned t0, unsigned dt, unsigned& x, unsigned& y) {
  bool dn = off == OOB;
  unsigned rel = 0;
#define HPOLL() __builtin_amdgcn_raw_ptr_buffer_atomic_add_i32(0, ws, dn ? OOB : off, 0, 0)
#define HCHECK(x) { const unsigned r_ = (x) - t0; const bool ok_ = !dn && r_ > 500u && r_ < 200000u; rel = ok_ ? r_ : rel; dn |= ok_; if (__all(dn)) break; }
  x = HPOLL();
  __builtin_amdgcn_s_sleep(PSTAG);
#pragma unroll 1
  for (unsigned sp = 0; sp < 4096; ++sp) {
    y = HPOLL(); __builtin_amdgcn_sched_barrier(0);
    HCHECK(x)
    x = HPOLL(); __builtin_amdgcn_sched_barrier(0);
    HCHECK(y)
  }
#undef HPOLL
#undef HCHECK
  float r = (float)rel;
  r = fmaxf(r, dpp<0xB1>(r)); r = fmaxf(r, dpp<0x4E>(r)); r = fmaxf(r, dpp<0x141>(r)); r = fmaxf(r, dpp<0x128>(r)); r = xmax(r);
  const unsigned until = rfl((unsigned)r) + dt;
#pragma unroll 1
  for (unsigned sp = 0; sp < 4096 && now32() - t0 < until; ++sp) { }
}
DI void emit_rec(LAS unsigned char* L, ctl_t ctl, __amdgpu_buffer_rsrc_t ws, int slot, int base, int dstr, int rec, int gi, int lane, v2f o, float m, float l, unsigned ta, unsigned tb, bool now) {
  if (!now) {
    LAS unsigned char* h = L + L_HOLD + slot * HOLD_B;
    *(LAS v2f*)(h + 32 + gi * 8) = o;
    if (lane == 0) { *(LAS v4u*)h = v4u{(unsigned)(base + rec * 128), (unsigned)dstr, (unsigned)(base + 8 * dstr + rec * 16), 0u}; *(LAS v2f*)(h + 16) = v2f{m, l}; }
    lgkm0();
    if (lane == 0) __hip_atomic_fetch_or((LAS unsigned*)(L + L_CTL) + C_HELD, 1u << slot, __ATOMIC_RELAXED, __HIP_MEMORY_SCOPE_WORKGROUP);
    lgkm0();
    now = rfl(ctl[C_PHASE]) >= GOAT;
  }
  if (now || DRY) {
    if (!now) base = OOB;
    gst(ws, base + (gi >> 3) * dstr + rec * 128 + (gi & 7) * 16, ubits(o.x), ubits(o.y), ta, tb);
    gst(ws, lane == 0 ? base + 8 * dstr + rec * 16 : OOB, ubits(m), ubits(l), ta, tb);
  }
}
template <int NR> DI v2f merge_rec(LAS unsigned char* rec, int lane, float& Mo, float& Lo) {
  v2f av[NR];
  const v2f ml = *(LAS v2f*)(rec + (lane & (NR - 1)) * REC_B);
#pragma unroll
  for (int w = 0; w < NR; ++w) av[w] = *(LAS v2f*)(rec + w * REC_B + 16 + lane * 8);
  float M = ml.x; M = fmaxf(M, dpp<0xB1>(M)); M = fmaxf(M, dpp<0x4E>(M));
  if (NR == 8) M = fmaxf(M, dpp<0x141>(M));
  const float f = ex2(ml.x - M);
  float Ls = f * ml.y; Ls += dpp<0xB1>(Ls); Ls += dpp<0x4E>(Ls);
  if (NR == 8) Ls += dpp<0x141>(Ls);
  float a0 = 0.f, a1 = 0.f;
#pragma unroll
  for (int w = 0; w < NR; ++w) { const float fw = fbits(__builtin_amdgcn_readlane(ubits(f), w)); a0 = fmaf(fw, av[w].x, a0); a1 = fmaf(fw, av[w].y, a1); }
  Mo = M; Lo = Ls;
  return v2f{a0, a1};
}

__global__ void __launch_bounds__(NTHR) fused_decode(Args a) {
  __shared__ __attribute__((aligned(16))) unsigned char smem[L_END];
  LAS unsigned char* L = (LAS unsigned char*)smem;
  ctl_t ctl = (ctl_t)(L + L_CTL);
  asm volatile("" ::"s"(a.xA), "s"(a.kA), "s"(a.vA), "s"(a.wqA), "s"(a.woA), "s"(a.xB), "s"(a.kB), "s"(a.vB), "s"(a.wqB), "s"(a.woB), "s"(a.out), "s"(a.ws));
  const int wg = blockIdx.x, g = wg >> 3, j = wg & 7;
  const int lane = threadIdx.x & 63, w = rfl(threadIdx.x >> 6);
  const __amdgpu_buffer_rsrc_t ws = mkrs(a.ws, W_ENDB);
  ST(w == 1 ? 0 : 31);
  const unsigned tstart = now32();

  if (w == 0) {
    __builtin_amdgcn_s_waitcnt(0);
    const unsigned lb = (unsigned)(uintptr_t)L;
    const unsigned voff = (lane * 16) ^ ((lane >> 4) << 6);
    const unsigned offA = g * (2u << 20) + (3u << 19) + j * 16384;
    __amdgpu_buffer_rsrc_t rk = mkrs(a.kA, 64u << 20), rv = mkrs(a.vA, 64u << 20);
    if (LDLAST) asm volatile("s_barrier" ::: "memory");
    dma8p(POLMASK & 1, rk, voff, offA, lb + L_RK);
    dma8p(POLMASK & 1, rk, voff, offA + 8192, lb + L_RK + 8192);
    dma8p(POLMASK & 1, rk, voff, offA + 131072, lb + L_RK + 16384); dma8p(POLMASK & 1, rk, voff, offA + 131072 + 8192, lb + L_RK + 24576);
    dma8p(POLMASK & 2, rv, voff, offA, lb + L_RV); dma8p(POLMASK & 2, rv, voff, offA + 8192, lb + L_RV + 8192);
    if (!LDLAST) asm volatile("s_barrier" ::: "memory");
    int u = 12, pub = 0, n = 1;
    unsigned soff = offA + 131072;
#define TB()
#define TE(x)
#define ISSUE_CHUNK(df, rs, so, ld) do { \
      dma8p(df, rs, voff, so, ld); waitvm<4 * WINU>(); u += 2; if (u - WINU > pub) { pub = u - WINU; ctl[C_FULL] = pub; } \
      dma8p(df, rs, voff, (so) + 8192, (ld) + 8192); waitvm<4 * WINU>(); u += 2; if (u - WINU > pub) { pub = u - WINU; ctl[C_FULL] = pub; } } while (0)
#pragma unroll 1
    for (;; ++n) {
      const int nk = n + 1;
      unsigned t;
#pragma unroll 1
      for (unsigned sp = 0;; ++sp) {
        const unsigned tk = ctl[C_TICK + (nk >= NS ? nk - NS : 0)], fk = ctl[C_FREE + (nk & 3)], fv = ctl[C_FREE + 4 + (n & 3)];
        t = nk >= NS ? rfl(tk) : 0u;
        if (t != 0xffffffffu && rfl(fk) >= (unsigned)(nk & ~3) && rfl(fv) >= (unsigned)(n & ~3)) break;
        if (sp > SPIN_CAP) return;
        TB();
        if (sp == 6) { waitvm<0>(); pub = u; ctl[C_FULL] = pub; }
        __builtin_amdgcn_s_sleep(1);
      }
      const bool last = t >= (unsigned)POOL;
      const unsigned sv = soff;
      if (!last) {
        if (nk + AHEAD >= NS) ctl[C_WANT] = nk + AHEAD + 1 - NS;
        if (nk == 4) rk = mkrs(a.kB, 128u << 20);
        soff = nk < 4 ? offA + nk * 131072
                      : (nk < 20 ? g : 32 + g) * (2u << 20) + (((nk < 20 ? (nk - 4) * 8 + j : nk < NS ? (nk - 20) * 8 + j : NSTAT * 8 + t) + g * ROT) & 127) * 16384;
        ISSUE_CHUNK(POLMASK & (nk < 4 ? 1 : nk < 20 ? 4 : 16), rk, soff, lb + L_RK + (nk & 3) * 16384);
      } else {
        ctl[C_END] = nk; ctl[C_PHASE] = 1; ST(8);
      }
      if (n == 4) rv = mkrs(a.vB, 128u << 20);
      ISSUE_CHUNK(POLMASK & (n < 4 ? 2 : n < 20 ? 8 : 32), rv, sv, lb + L_RV + (n & 3) * 16384);
      if (last) break;
    }
    ctl[C_PHASE] = 2;
    ST(9);
#define DRAIN(k) if (WINU > k) { waitvm<4 * k>(); const int p_ = u - k + (k < 4 ? 4 : 0); if (p_ > pub) { pub = p_; ctl[C_FULL] = pub; } if (k == FLAGK) hint_set(ws, lane == 0 ? W_F1 + wg * 128 : OOB); if (k == 0) ctl[C_PHASE] = 3; }
    DRAIN(10) DRAIN(8) DRAIN(6) DRAIN(4) DRAIN(2) DRAIN(0)
#undef DRAIN
    ST(10);
    return;
  }
  const int cb = (wg & ~15) + ((wg & 7) << 1) + ((wg >> 3) & 1);
  const int cbase = j < 4 ? 0 : 2, prod = g * 4 + (j & 3);
  unsigned ta, tb;
  int jb, jend;
  if (w == 1) {
    const v2u ep = __builtin_amdgcn_raw_buffer_load_b64(ws, W_X, 0, SC1);
    if (cb >= 128 && (cb & 3) == 0 && lane == 0) __builtin_amdgcn_raw_buffer_store_b32(0u, ws, W_CTR + ((cb - 128) >> 2) * 256, 0, SC1);
    ctl[lane] = lane == C_END ? 0x7fffffffu : lane >= C_TICK ? 0xffffffffu : 0u;
    ctl[lane + 64] = 0xffffffffu;
    asm volatile("s_waitcnt lgkmcnt(0)\n\ts_barrier" ::: "memory");
    ta = rfl(ep.x) + 1u; tb = rfl(ep.y) + 1u;
    ta += ta == 0u; tb += tb == 0u;
    waitvm<0>();
    if (!wait_ge<1>(ctl, C_QCNT, 9)) return;
    ST(1);
    if (lane < 16) {
      v4f s = {0.f, 0.f, 0.f, 0.f};
#pragma unroll
      for (int i = 0; i < 9; ++i) s += *(LAS v4f*)(L + L_QP + i * 256 + lane * 16);
      const int b = lane >> 3, quad = lane & 7;
      const int off = cb < 128 ? W_QA + ((b * 16 + (cb >> 3)) * 64 + (cb & 7) * 8 + quad) * 16 : W_QB + (((cb - 128) >> 2) * 64 + b * 32 + (cb & 3) * 8 + quad) * 16;
      gst(ws, off, pk_bf16(s.x, s.y), pk_bf16(s.z, s.w), ta, tb);
    }
    jb = 0; jend = 2;
  } else if (w == 2) {
    if (STOUCH) {
      const size_t pa = (size_t)g * (2u << 20) + (3u << 19), pb = (size_t)g * (2u << 20), pc = pb + (64u << 20);
      const unsigned char* wq = cb < 128 ? a.wqA : a.wqB;
      const unsigned char* wo = (j < 4 ? a.woA : a.woB) + (size_t)((j < 4 ? 2 * (g & 15) + (g >> 4) : g) * 128 + (j & 3) * 32) * 1152;
      unsigned d0, d1, d2, d3, d4, d5, d6, d7, d8, d9, d10, d11, d12, d13;
      asm volatile("s_load_dword %0, %14, 0\n\ts_load_dword %1, %15, 0\n\ts_load_dword %2, %16, 0\n\ts_load_dword %3, %17, 0\n\ts_load_dword %4, %18, 0\n\t"
                   "s_load_dword %5, %19, 0\n\ts_load_dword %6, %20, 0\n\ts_load_dword %7, %21, 0\n\ts_load_dword %8, %22, 0\n\ts_load_dword %9, %23, 0\n\t"
                   "s_load_dword %10, %24, 0\n\ts_load_dword %11, %25, 0\n\ts_load_dword %12, %26, 0\n\ts_load_dword %13, %27, 0\n\ts_barrier\n\ts_waitcnt lgkmcnt(0)"
                   : "=&s"(d0), "=&s"(d1), "=&s"(d2), "=&s"(d3), "=&s"(d4), "=&s"(d5), "=&s"(d6), "=&s"(d7), "=&s"(d8), "=&s"(d9), "=&s"(d10), "=&s"(d11), "=&s"(d12), "=&s"(d13)
                   : "s"(a.kA + pa), "s"(a.vA + pa), "s"(wq), "s"(wq + (2u << 20)), "s"(wq + (4u << 20)), "s"(cb < 128 ? a.xA : a.xB), "s"(a.ws + 2048), "s"(wo),
                     "s"(a.kB + pb), "s"(a.vB + pb), "s"(a.kB + pc), "s"(a.vB + pc), "s"(a.ws + W_SAFE1), "s"(a.ws + W_SAFE2)
                   : "memory");
    } else
    asm volatile("s_barrier" ::: "memory");
    if (!wait_ge<16>(ctl, C_QRDY, 1)) return;
    ta = rfl(ctl[C_TAGA]); tb = rfl(ctl[C_TAGB]);
    jb = DRY ? -2 : 0; jend = 2;
  } else {
    {
      {
        const int qw = w - 3;
        const int r0 = qw * 64 + (lane >> 2) * 4;
        const unsigned char* xp = (cb < 128 ? a.xA : a.xB) + r0 * 2;
        const unsigned char* wp = (cb < 128 ? a.wqA : a.wqB) + (size_t)r0 * 8192 + (cb & 127) * 64 + (lane & 3) * 16;
        v4u Wq[4];
#pragma unroll
        for (int i = 0; i < 4; ++i) Wq[i] = __builtin_nontemporal_load((const GAS v4u*)(wp + i * 8192));
        const v2u X0 = *(const GAS v2u*)xp, X1 = *(const GAS v2u*)(xp + 1152);
        asm volatile("s_barrier" ::: "memory");
        float ac[2][8];
#pragma unroll
        for (int k = 0; k < 8; ++k) { ac[0][k] = 0.f; ac[1][k] = 0.f; }
#pragma unroll
        for (int s = 0; s < 2; ++s)
#pragma unroll
          for (int d = 0; d < 4; ++d) {
            const unsigned lo = __builtin_amdgcn_perm(Wq[2 * s + 1][d], Wq[2 * s][d], 0x05040100u), hi = __builtin_amdgcn_perm(Wq[2 * s + 1][d], Wq[2 * s][d], 0x07060302u);
            ac[0][2 * d] = dot2(lo, X0[s], ac[0][2 * d]); ac[0][2 * d + 1] = dot2(hi, X0[s], ac[0][2 * d + 1]);
            ac[1][2 * d] = dot2(lo, X1[s], ac[1][2 * d]); ac[1][2 * d + 1] = dot2(hi, X1[s], ac[1][2 * d + 1]);
          }
        float b8[8], c4[4];
#pragma unroll
        for (int k = 0; k < 8; ++k) b8[k] = rs32(ac[0][k], ac[1][k]);
#pragma unroll
        for (int k = 0; k < 4; ++k) { float t = rs16(b8[k], b8[k + 4]); t += dpp<0x128>(t); t += dpp<0x124>(t); c4[k] = t; }
        if ((lane & 12) == 0) *(LAS v4f*)(L + L_QP + qw * 256 + (lane >> 5) * 128 + (lane & 3) * 32 + ((lane >> 4) & 1) * 16) = v4f{c4[0], c4[1], c4[2], c4[3]};
        lds_add(L, C_QCNT, 1, lane);
      }
      const int r = lane >> 4, c = lane & 15;
      const unsigned kof = c * 256 + r * 16 + (c & 3) * 64;
      const unsigned vof = (4 * r + (c >> 2)) * 256 + (c >> 2) * 64 + (c & 3) * 8;
      if (w >= 4) {
        const unsigned aw = w - 4, off = aw * 8192, an = aw >> 1, ah = 2 * (aw & 1) + 2;
        v4u K0[4], K1[4], V[8];
        if (!wait_ge<1>(ctl, C_FULL, (an ? 8 * an - 4 : 0) + ah)) return;
        read_k(L, off + kof, K0); read_k(L, off + 4096 + kof, K1);
        lds_add(L, C_FREE + (aw >> 1), 2, lane);
        if (!wait_ge<1>(ctl, C_FULL, 8 * an + 8 + ah)) return;
        read_v(L, off + vof, V);
        lds_add(L, C_FREE + 4 + (aw >> 1), 2, lane);
        if (!wait_ge<1>(ctl, C_QRDY, 1)) return;
        if (w == 4) ST(3);
        ta = rfl(ctl[C_TAGA]); tb = rfl(ctl[C_TAGB]);
        const LAS v4u* qp = (const LAS v4u*)(L + L_Q) + r + (c >> 3) * 16;
        const v4u q[4] = {qp[0], qp[4], qp[8], qp[12]};
        v4f s0 = scores(K0, q), s1 = scores(K1, q);
        const float M = xmax(fmaxf(max4(s0), max4(s1))) * L2E;
        s0 = expo(s0, M); s1 = expo(s1, M);
        const v4u P = pack_p(s0, s1);
        const float Ls = xsum(sum4(s0) + sum4(s1));
        v4f acc[8];
#pragma unroll
        for (int blk = 0; blk < 8; ++blk) acc[blk] = mfma(V[blk], P, v4f{0.f, 0.f, 0.f, 0.f});
#pragma unroll
        for (int k = 0; k < 4; ++k) acc[k] = sel4(bitmask(c, 2), acc[k + 4], acc[k]);
#pragma unroll
        for (int k = 0; k < 2; ++k) acc[k] = sel4(bitmask(c, 1), acc[k + 2], acc[k]);
        acc[0] = sel4(bitmask(c, 0), acc[1], acc[0]);
        LAS unsigned char* rec = L + L_REC + ((c >> 3) * 8 + aw) * REC_B;
        *(LAS v4f*)(rec + 16 + ((c & 7) * 16 + r * 4) * 4) = acc[0];
        if ((lane & 0x37) == 0) *(LAS v2f*)rec = v2f{M, Ls};
        if (w == 4) ST(15);
        if (last_arriver(L, C_CNT, 8, lane)) {
#pragma unroll 1
          for (int e = 0; e < 2; ++e) {
            float Mo, Lo;
            const v2f o = merge_rec<8>(L + L_REC + e * 8 * REC_B, lane, Mo, Lo);
            emit_rec(L, ctl, ws, e, W_P + (g * 2 + e) * P_STR, 1024, j, lane, lane, o, Mo, Lo, ta, tb, false);
          }
          ST(4);
        }
      }
      if (w >= 8) {
        const int cw = w - 8;
        const unsigned off = cw * 16384;
        v4u q[4];
        float m = -INFINITY, l = 0.f;
        v4f acc[8];
        bool fresh = true;
        const unsigned mk0 = bitmask(c, 0), mk1 = bitmask(c, 1), mk2 = bitmask(c, 2), mk3 = bitmask(c, 3);
#pragma unroll 1
        for (unsigned n = 4 + cw;; n += 4) {
          if (fresh) {
            const LAS v4u* qp = (const LAS v4u*)(L + L_Q) + r + (n < 20 ? 32 : 48);
#pragma unroll
            for (int kk = 0; kk < 4; ++kk) q[kk] = qp[4 * kk];
            m = -INFINITY; l = 0.f;
#pragma unroll
            for (int blk = 0; blk < 8; ++blk) acc[blk] = v4f{0.f, 0.f, 0.f, 0.f};
            fresh = false;
          }
          bool stop = false;
#pragma unroll 1
          for (unsigned sp = 0;; ++sp) {
            const v2u fe = *(volatile LAS v2u*)(L + L_CTL);
            if (n >= rfl(fe.y) || sp > SPIN_CAP) { stop = true; break; }
            if (rfl(fe.x) >= 8 * n) break;
            __builtin_amdgcn_s_sleep(1);
          }
          if (stop) break;
          asm volatile("" ::: "memory");
          v4f s[4];
          v4u K[4][4];
#pragma unroll
          for (int uu = 0; uu < 4; ++uu) read_k(L, off + uu * 4096 + kof, K[uu]);
          __builtin_amdgcn_sched_barrier(0);
#pragma unroll
          for (int uu = 0; uu < 4; ++uu) s[uu] = scores(K[uu], q);
          lds_add(L, C_FREE + cw, 4, lane);
          const float mn = fmaxf(xmax(fmaxf(fmaxf(max4(s[0]), max4(s[1])), fmaxf(max4(s[2]), max4(s[3])))) * L2E, m), al = ex2(m - mn);
          m = mn;
#pragma unroll
          for (int uu = 0; uu < 4; ++uu) s[uu] = expo(s[uu], mn);
          v4u P0 = pack_p(s[0], s[1]), P1 = pack_p(s[2], s[3]);
          l = fmaf(l, al, (sum4(s[0]) + sum4(s[1])) + (sum4(s[2]) + sum4(s[3])));
#pragma unroll
          for (int blk = 0; blk < 8; ++blk) acc[blk] *= al;
          const bool pubn = n >= NS - 4 || (n & ~3u) == 16;
          float Ls = xsum(l);
          asm volatile("" : "+v"(P0), "+v"(P1), "+v"(Ls), "+v"(acc[0]), "+v"(acc[1]), "+v"(acc[2]), "+v"(acc[3]), "+v"(acc[4]), "+v"(acc[5]), "+v"(acc[6]), "+v"(acc[7]));
          v4u V[8];
          if (!wait_ge<1>(ctl, C_FULL, 8 * n + 10)) return;
          read_v(L, off + vof, V);
#pragma unroll
          for (int blk = 0; blk < 8; ++blk) acc[blk] = mfma(V[blk], P0, acc[blk]);
          const unsigned tkn = ctl[C_TICK + (n >= NS ? n - NS : 0)], phs = ctl[C_PHASE];
          if (!wait_ge<1>(ctl, C_FULL, 8 * n + 12)) return;
          read_v(L, off + 8192 + vof, V);
#pragma unroll
          for (int blk = 0; blk < 8; ++blk) acc[blk] = mfma(V[blk], P1, acc[blk]);
          if (!pubn) lds_add(L, C_FREE + 4 + cw, 4, lane);
          if (pubn) {
            v2f o[8];
#pragma unroll
            for (int k = 0; k < 8; ++k) o[k] = sel2(mk0, v2f{acc[k][2], acc[k][3]}, v2f{acc[k][0], acc[k][1]});
#pragma unroll
            for (int k = 0; k < 4; ++k) o[k] = sel2(mk3, o[k + 4], o[k]);
#pragma unroll
            for (int k = 0; k < 2; ++k) o[k] = sel2(mk2, o[k + 2], o[k]);
            o[0] = sel2(mk1, o[1], o[0]);
            int slot = 4 + n - NS, rec = 0, gi = (c >> 1) * 8 + 2 * r + (c & 1);
            bool emit = true;
            if (n >= NS) rec = 8 + rfl(tkn);
            else {
              const int b = n >= 20;
              LAS unsigned char* rp = L + L_REC + (16 + b * 4) * REC_B;
              *(LAS v2f*)(rp + cw * REC_B + 16 + gi * 8) = o[0];
              if (lane == 0) *(LAS v2f*)(rp + cw * REC_B) = v2f{m, Ls};
              emit = last_arriver(L, C_CNT + 1 + b, 4, lane);
              if (emit) o[0] = merge_rec<4>(rp, lane, m, Ls);
              slot = 2 + b; rec = j; gi = lane;
            }
            if (emit) {
              emit_rec(L, ctl, ws, slot, n >= 20 ? W_P1 + g * P1_STR : W_P + (64 + g) * P_STR, n >= 20 ? D1 : 1024, rec, gi, lane, o[0], m, Ls, ta, tb, slot >= NHOLD || rfl(phs) >= GOAT);
              ST(n >= 20 ? 7 : 5);
            }
            lds_add(L, C_FREE + 4 + cw, 4, lane);
            fresh = true;
          }
        }
        jb = 2; jend = 3;
      } else {
      const int k = w == 3 ? 4 : w - 4;
      if (WOAT) { if (!wait_ge<16>(ctl, C_WANT, 1)) return; if (!wait_ge<2>(ctl, C_PHASE, WOAT)) return; }
      else if (!wait_ge<8>(ctl, C_QRDY, 1)) return;
      ta = rfl(ctl[C_TAGA]); tb = rfl(ctl[C_TAGB]);
      unsigned Wo[32];
      {
        const int head = j < 4 ? 2 * (g & 15) + (g >> 4) : g;
        const unsigned char* pw = (j < 4 ? a.woA : a.woB) + (size_t)(head * 128 + (j & 3) * 32) * 1152 + k * 256 + (k < 4 ? lane : lane & 31) * 4;
#pragma unroll
        for (int i = 0; i < 32; ++i) Wo[i] = __builtin_nontemporal_load((const GAS unsigned*)(pw + i * 1152));
#pragma unroll
        for (int s = 0; s < 16; ++s) {
          const unsigned a0 = Wo[2 * s], a1 = Wo[2 * s + 1];
          Wo[2 * s] = __builtin_amdgcn_perm(a1, a0, 0x05040100u); Wo[2 * s + 1] = __builtin_amdgcn_perm(a1, a0, 0x07060302u);
        }
#pragma unroll
        for (int s = 0; s < 32; s += 4) asm volatile("" : "+v"(Wo[s]), "+v"(Wo[s + 1]), "+v"(Wo[s + 2]), "+v"(Wo[s + 3]));
      }
      int yreal = W_Y + ((j < 4 ? 0 : 3) * 36 + k * 8 + (lane >> 3)) * YS + prod * 128 + (lane & 7) * 16;
      if (k == 4 && lane >= 32) yreal = OOB;
#pragma unroll 1
      for (int pass = DRY ? 0 : 1; pass < 2; ++pass) {
      if (!wait_ge<16>(ctl, C_WANT, pass)) return;
      if (!wait_ge<2>(ctl, C_PHASE, pass * GOAT)) return;
      {
        const unsigned held = pass ? rfl(ctl[C_HELD]) : 31u;
        asm volatile("" ::: "memory");
#pragma unroll 1
        for (int slot = k; slot < NHOLD; slot += 5) {
          if (!(held >> slot & 1)) continue;
          LAS unsigned char* h = L + L_HOLD + slot * HOLD_B;
          const v4u hd = *(LAS v4u*)h;
          const v2u ml = *(LAS v2u*)(h + 16), o = *(LAS v2u*)(h + 32 + lane * 8);
          gst(ws, pass ? hd.x + (lane >> 3) * hd.y + (lane & 7) * 16 : OOB, o.x, o.y, ta, tb);
          gst(ws, pass && lane == 0 ? hd.z : OOB, ml.x, ml.y, ta, tb);
        }
        if (w == 4) ST(14);
      }
      const int yoff = pass ? yreal : OOB;
      if (!wait_ge<1>(ctl, C_OSEQ, 2 * pass)) return;
#pragma unroll
      for (int i = 0; i < 2; ++i) {
        const LAS v4u* op = (const LAS v4u*)(L + L_OP + i * 64);
        float y0 = 0.f, y1 = 0.f, z0 = 0.f, z1 = 0.f;
#pragma unroll
        for (int h = 0; h < 2; ++h) {
          const v4u oa = op[2 * h], ob = op[2 * h + 1];
#pragma unroll
          for (int s = 0; s < 4; ++s) {
            y0 = dot2(Wo[16 * h + 2 * s], oa[s], y0); y1 = dot2(Wo[16 * h + 2 * s + 1], oa[s], y1);
            z0 = dot2(Wo[16 * h + 8 + 2 * s], ob[s], z0); z1 = dot2(Wo[16 * h + 9 + 2 * s], ob[s], z1);
          }
        }
        gst(ws, yoff + (i ? (j < 4 ? 36 * YS : -36 * YS) : 0), ubits(y0 + z0), ubits(y1 + z1), ta, tb);
      }
      }
      if (w == 4 && j >= 4) ST(12);
      return;
      }
    }
  }
#pragma unroll 1
  for (; jb < jend; ++jb) {
    if (jb == 1 && w == 1 && lane == 0) {
      const unsigned char* ctr = a.ws + W_CTR + g * 256;
      const unsigned zero = 0, one = 1;
      unsigned t0, t1, t2 = 0;
#define TICKET(t) asm volatile("global_atomic_add %0, %1, %2, %3 sc0" : "=v"(t) : "v"(zero), "v"(one), "s"(ctr) : "memory")
#define STEP(tnew, told, kk) \
  if (!wait_ge<2>(ctl, C_WANT, (kk) + AHEAD)) break; \
  TICKET(tnew); \
  asm volatile("s_waitcnt vmcnt(%1)" : "+v"(told) : "n"(AHEAD - 1) : "memory"); \
  ctl[C_TICK + (kk)] = told; \
  if (rfl(told) >= (unsigned)POOL) break;
      if (wait_ge<8>(ctl, C_WANT, 1)) {
        TICKET(t0);
#pragma unroll 1
        for (int kk = 0; kk <= POOL; kk += 2) { STEP(t1, t0, kk) STEP(t0, t1, kk + 1) }
      }
      asm volatile("s_waitcnt vmcnt(0)" : "+v"(t0), "+v"(t1), "+v"(t2)::"memory");
    }
    if (jb == 0 && w == 2) {
      if (!wait_ge<16>(ctl, C_WANT, 1)) return;
      if (TOUCH) {
        const unsigned t0 = __builtin_amdgcn_raw_buffer_load_b32(ws, lane & 1 ? W_SAFE1 : W_SAFE2, 0, SC1), t1 = __builtin_amdgcn_raw_buffer_load_b32(mkrs(a.out, 4608), 0, 0, SC1);
        asm volatile("" ::"v"(t0), "v"(t1));
      }
      continue;
    }
    const bool dry = jb < 0, fin = jb == 2 || jb == -1;
    const int qt = w & 3;
    const int c = cbase + (prod & 1), rho = prod >> 1;
    int voff, voff7, voff8, hoff = OOB;
    unsigned hdt = 0;
    unsigned mask;
    const int nr1 = j < 4 ? 8 : 56;
    if (fin) {
      if (rho >= 36 && !dry) return;
      voff8 = voff7 = voff = W_Y + (c * 36 + rho) * YS + qt * 4096 + lane * 16;
      mask = 0x0fu;
      hoff = W_F2 + (cbase / 2 * NREP + prod % NREP) * 1024 + qt * 256 + lane * 4; hdt = DT2;
      if (!wait_ge<2>(ctl, C_PHASE, dry ? 0 : 3)) return;
    } else if (jb == 0) {
      voff8 = voff = W_QA + g * 1024 + lane * 16;
      voff7 = voff + (W_QB - W_QA);
      mask = 0x81u;
    } else {
      const int ta_ = (g & 15) * 2 + (g >> 4);
      const int b0 = j < 4 ? W_P + ta_ * P_STR : W_P1 + g * P1_STR, b1 = W_P + (j < 4 ? 32 + ta_ : 64 + g) * P_STR, d0 = j < 4 ? 1024 : D1, dest = 2 * (j & 3) + w - 1;
      voff = b0 + dest * d0 + lane * 16;
      voff7 = b1 + dest * 1024 + lane * 16;
      voff8 = lane >= 56 ? b1 + 8192 + (lane - 56) * 16 : b0 + 8 * d0 + (lane < nr1 ? lane : 0) * 16;
      mask = j < 4 ? 0x181u : 0x1ffu;
      hoff = j < 4 ? (lane < 16 ? W_F1 + ((g & 15) * 8 + (lane >> 3) * 128 + (lane & 7)) * 128 : OOB) : (lane < 8 ? W_F1 + (g * 8 + lane) * 128 : OOB); hdt = DT1;
      if (!wait_ge<2>(ctl, C_PHASE, dry ? 0 : POLLAT)) return;
    }
    if (dry) { mask = 0; hoff = OOB; hdt = 0; }
    unsigned hx = 0, hy = 0;
    if (jb != 0) hint_wait(ws, hoff, tstart, hdt, hx, hy);
    v2u y[9];
    unsigned long long dn[9];
#pragma unroll
    for (int i = 0; i < 9; ++i) { y[i] = v2u{0u, 0u}; dn[i] = (mask >> i & 1) ? 0ull : ~0ull; }
    unsigned sp = 0;
#pragma unroll 1
    for (; sp < SPIN_CAP; ++sp) {
      v4u X[9];
#pragma unroll
      for (int i = 0; i < 9; ++i) X[i] = gld(ws, i == 8 ? voff8 : i == 7 ? voff7 : voff, dn[i] != ~0ull ? (i < 7 ? i * 1024 : 0) : OOB);
      __builtin_amdgcn_sched_barrier(0);
#pragma unroll
      for (int i = 0; i < 9; ++i) {
        const bool ok = gok(X[i], ta, tb);
        y[i].x = ok ? X[i].x : y[i].x; y[i].y = ok ? X[i].z : y[i].y;
        dn[i] |= __builtin_amdgcn_ballot_w64(X[i].y == ta) & __builtin_amdgcn_ballot_w64(X[i].w == tb);
      }
      if (((dn[0] & dn[1]) & (dn[2] & dn[3]) & (dn[4] & dn[5]) & (dn[6] & dn[7]) & dn[8]) == ~0ull) break;
      __builtin_amdgcn_s_sleep(4);
    }
    asm volatile("" ::"v"(hx), "v"(hy));
    if (sp >= SPIN_CAP) return;
    if (jb == 0) {
      *(LAS v2u*)(L + L_Q + lane * 8) = y[0];
      *(LAS v2u*)(L + L_Q + 512 + lane * 8) = y[7];
      ctl[C_TAGA] = ta; ctl[C_TAGB] = tb;
      lgkm0();
      ctl[C_QRDY] = 1;
      ST(2);
    } else if (fin) {
      const float s0 = (fbits(y[0].x) + fbits(y[1].x)) + (fbits(y[2].x) + fbits(y[3].x)), s1 = (fbits(y[0].y) + fbits(y[1].y)) + (fbits(y[2].y) + fbits(y[3].y));
      float s = rs32(s0, s1);
      s = rs16(s, s);
      s += dpp<0x128>(s);
      LAS float* fp = (LAS float*)(L + L_FP);
      if ((lane & 24) == 0) fp[((lane & 7) * 2 + (lane >> 5)) * 4 + qt] = s;
      if (last_arriver(L, dry ? C_DUMMY : C_FCNT, dry ? 1 : 4, lane)) {
        const v4f p = *(LAS v4f*)(fp + (lane & 15) * 4);
        const float t = (p.x + p.y) + (p.z + p.w);
        __builtin_amdgcn_raw_buffer_store_b16((unsigned short)pk_bf16(t, t), mkrs(a.out, 4608), lane < 16 && !dry ? ((c & 1) * 1152 + (c >> 1) * 576 + rho * 16 + lane) * 2 : OOB, 0, 0);
        __builtin_amdgcn_raw_buffer_store_b64(v2u{ta, tb}, ws, c == 3 && rho == 0 && lane == 0 && !dry ? W_X : OOB, 0, SC1);
        if (!dry) ST(c == 3 ? 13 : 6);
      }
    } else {
      hint_set(ws, lane < NREP && !dry ? W_F2 + (cbase / 2 * NREP + lane) * 1024 + prod * 8 + (w - 1) * 4 : OOB);
      const bool seg1 = lane >= 56;
      const float mr = fbits(y[8].x);
      float M = fmaxf(mr, dpp<0xB1>(mr)); M = fmaxf(M, dpp<0x4E>(M)); M = fmaxf(M, dpp<0x141>(M));
      float M0 = seg1 ? -INFINITY : M;
      M0 = fmaxf(M0, dpp<0x128>(M0)); M0 = xmax(M0);
      const float f = (lane >= nr1 && !seg1) ? 0.f : ex2(mr - (seg1 ? M : M0));
      float fb[8];
#pragma unroll
      for (int b = 0; b < 8; ++b) fb[b] = fbits(__builtin_amdgcn_ds_bpermute((8 * b + (lane >> 3)) * 4, ubits(f)));
      float Ls = f * fbits(y[8].y);
      Ls += dpp<0xB1>(Ls); Ls += dpp<0x4E>(Ls); Ls += dpp<0x141>(Ls);
      const float L1 = fbits(__builtin_amdgcn_readlane(ubits(Ls), 56));
      float L0 = seg1 ? 0.f : Ls;
      L0 += dpp<0x128>(L0); L0 = xsum(L0);
      const float inv = __builtin_amdgcn_rcpf(lane >= 32 ? L1 : L0);
      float o0 = 0.f, o1 = 0.f;
#pragma unroll
      for (int b = 0; b < 7; ++b) { o0 = fmaf(fb[b], fbits(y[b].x), o0); o1 = fmaf(fb[b], fbits(y[b].y), o1); }
      const float oe = rs32(o0, fb[7] * fbits(y[7].x)), oo = rs32(o1, fb[7] * fbits(y[7].y));
      float o = rs16(oe, oo);
      o += dpp<0x128>(o);
      o *= inv;
      if ((lane & 8) == 0) *(LAS unsigned short*)(L + L_OP + (lane >> 5) * 64 + (w - 1) * 32 + (lane & 7) * 4 + ((lane >> 4) & 1) * 2) = (unsigned short)pk_bf16(o, o);
      lds_add(L, C_OSEQ, dry ? 0 : 1, lane);
    }
  }
}

extern "C" void kernel_launch(void* const* d_in, const int* in_sizes, int n_in, void* d_out, int out_size, void* d_ws, size_t ws_size,
                              hipStream_t stream) {
  Args a;
  a.xA = (const unsigned char*)d_in[0]; a.kA = (const unsigned char*)d_in[1]; a.vA = (const unsigned char*)d_in[2];
  a.wqA = (const unsigned char*)d_in[3]; a.woA = (const unsigned char*)d_in[4];
  a.xB = (const unsigned char*)d_in[5]; a.kB = (const unsigned char*)d_in[6]; a.vB = (const unsigned char*)d_in[7];
  a.wqB = (const unsigned char*)d_in[8]; a.woB = (const unsigned char*)d_in[9];
  a.out = (unsigned char*)d_out; a.ws = (unsigned char*)d_ws;
  fused_decode<<<256, NTHR, 0, stream>>>(a);
}
```

```cpp
#include <hip/hip_runtime.h>
#include <stdint.h>

typedef unsigned v4u __attribute__((ext_vector_type(4)));
typedef unsigned v2u __attribute__((ext_vector_type(2)));
typedef float v4f __attribute__((ext_vector_type(4)));
typedef float v2f __attribute__((ext_vector_type(2)));
typedef short v4s __attribute__((ext_vector_type(4)));
typedef __bf16 bf16x2 __attribute__((ext_vector_type(2)));
typedef __bf16 bf16x8 __attribute__((ext_vector_type(8)));
#define LAS __attribute__((address_space(3)))
#define GAS __attribute__((address_space(1)))
#define DI __device__ __forceinline__

#define NSTAT 8
#define WINU 8
#define ROT 8
#define PSTAG 8
#define GOAT 1
#define POLLAT 2
#define DRY 1
#define DT1 50
#define DT2 110
#define FLAGK 2
#define NREP 16
#define WOAT 2
#define QSLP 8
#define WPACE 0
#define FMODE 1
#define QB 0
#define QM 8
#define TOUCH 1
#define AH2 3
#define WINL 8
#define LATE 40
constexpr int NWAVE = 12, NTHR = NWAVE * 64;
constexpr int NS = 4 + 16 + NSTAT;
constexpr int POOLC = (16 - NSTAT) * 8;
constexpr int NT = 48, TP = POOLC - NT;
static_assert(TP >= 0 && TP <= NT && NSTAT >= 4 && WINU % 2 == 0 && WINU >= 4 && WINU <= WINL && WINL <= 12 && WINL % 2 == 0, "");
constexpr unsigned SPIN_CAP = 1u << 18;
constexpr int NHOLD = 16, HOLD_B = 544, REC_B = 528;
constexpr int L_RK = 0, L_RV = 65536, L_CTL = 131072, L_Q = L_CTL + 1024, L_OP = L_Q + 1024, L_FP = L_CTL + 768  , L_QP = L_OP + 128, L_REC = L_QP + 9 * 256,
              L_HOLD = L_REC + 24 * REC_B, L_END = L_HOLD + NHOLD * HOLD_B;
static_assert(L_END <= 163840, "");
enum { C_FULL = 0, C_END = 1, C_PHASE = 2, C_QRDY = 3, C_WANT = 4  , C_T1 = 5  , C_TAGA = 6, C_TAGB = 7, C_FREE = 8  , C_CNT = 16,
       C_QCNT = 19, C_HELD = 21, C_OSEQ = 22, C_T2 = 24  , C_TICK = 64, C_DESC = 128 };
static_assert(NT + 2 <= 64 && POOLC + 0 <= 64, "");
constexpr int W_X = 0, W_CTR = 4096, W_F1 = 16384  , W_F2 = W_F1 + 32768  , W_QA = W_F2 + 2 * NREP * 1024, W_QB = W_QA + 32768,
              W_P = W_QB + 32768, P_STR = 9216  , W_P1 = W_P + 96 * P_STR, P1_STR = 66560  ,
              W_Y = W_P1 + 32 * P1_STR, YS = 16384, W_ENDB = 256 + W_Y + 4 * 36 * YS  , W_STAMP = (W_ENDB + 4095) / 4096 * 4096;
constexpr int W_SAFE1 = W_P1 + 24 * P1_STR + 7424, W_SAFE2 = W_ENDB - 128;
static_assert(W_SAFE1 >> 21 == 1 && W_SAFE2 >> 21 == 2, "");
constexpr float SCALE = 0.088388347648318440f, L2E = 1.4426950408889634f;
constexpr int SC1 = 16, OOB = 0x70000000;

struct Args {
  const unsigned char *xA, *kA, *vA, *wqA, *woA, *xB, *kB, *vB, *wqB, *woB;
  unsigned char *out, *ws;
};

DI float dot2(unsigned a, unsigned b, float c) { return __builtin_amdgcn_fdot2_f32_bf16(__builtin_bit_cast(bf16x2, a), __builtin_bit_cast(bf16x2, b), c, false); }
template <int CTRL> DI float dpp(float x) { return __builtin_bit_cast(float, __builtin_amdgcn_mov_dpp(__builtin_bit_cast(int, x), CTRL, 0xf, 0xf, true)); }
DI unsigned pk_bf16(float lo, float hi) {
  unsigned r = __builtin_bit_cast(unsigned, bf16x2{(__bf16)lo, (__bf16)hi});
  asm("" : "+v"(r));
  return r;
}
DI float bf_lo(unsigned u) { return __builtin_bit_cast(float, u << 16); }
DI float bf_hi(unsigned u) { return __builtin_bit_cast(float, u & 0xffff0000u); }
DI float fbits(unsigned u) { return __builtin_bit_cast(float, u); }
DI unsigned ubits(float f) { return __builtin_bit_cast(unsigned, f); }
DI unsigned rfl(unsigned x) { return __builtin_amdgcn_readfirstlane(x); }
DI float ex2(float x) { return __builtin_amdgcn_exp2f(x); }
DI float mx2(float a, float b) { return __builtin_amdgcn_fmed3f(a, b, INFINITY); }
DI v4f mfma(v4u a, v4u b, v4f c) { return __builtin_amdgcn_mfma_f32_16x16x32_bf16(__builtin_bit_cast(bf16x8, a), __builtin_bit_cast(bf16x8, b), c, 0, 0, 0); }
DI v2u trd(LAS unsigned char* p) { return __builtin_bit_cast(v2u, __builtin_amdgcn_ds_read_tr16_b64_v4i16((LAS v4s*)p)); }
DI float rs32(float x, float y) { auto s = __builtin_amdgcn_permlane32_swap(ubits(x), ubits(y), false, false); return fbits(s[0]) + fbits(s[1]); }
DI float rs16(float x, float y) { auto s = __builtin_amdgcn_permlane16_swap(ubits(x), ubits(y), false, false); return fbits(s[0]) + fbits(s[1]); }
DI float xsum(float x) { x = rs16(x, x); return rs32(x, x); }
DI float xmax(float x) {
  auto s = __builtin_amdgcn_permlane16_swap(ubits(x), ubits(x), false, false); x = mx2(fbits(s[0]), fbits(s[1]));
  auto t = __builtin_amdgcn_permlane32_swap(ubits(x), ubits(x), false, false); return mx2(fbits(t[0]), fbits(t[1]));
}
DI float max8(float x) {
  asm("s_nop 1\n\tv_max_f32_dpp %0, %0, %0 quad_perm:[1,0,3,2] row_mask:0xf bank_mask:0xf\n\ts_nop 1\n\tv_max_f32_dpp %0, %0, %0 quad_perm:[2,3,0,1] row_mask:0xf bank_mask:0xf\n\t"
      "s_nop 1\n\tv_max_f32_dpp %0, %0, %0 row_half_mirror row_mask:0xf bank_mask:0xf" : "+v"(x));
  return x;
}
DI float max8to64(float x) {
  float t;
  asm("s_nop 1\n\tv_max_f32_dpp %0, %0, %0 row_ror:8 row_mask:0xf bank_mask:0xf\n\tv_mov_b32 %1, %0\n\ts_nop 1\n\tv_permlane16_swap_b32 %0, %1\n\tv_max_f32 %0, %0, %1\n\t"
      "v_mov_b32 %1, %0\n\ts_nop 1\n\tv_permlane32_swap_b32 %0, %1\n\tv_max_f32 %0, %0, %1" : "+v"(x), "=&v"(t));
  return x;
}
DI float max4to64(float x) {
  asm("s_nop 1\n\tv_max_f32_dpp %0, %0, %0 row_ror:4 row_mask:0xf bank_mask:0xf" : "+v"(x));
  return max8to64(x);
}
template <int LANE> DI unsigned bcast16(unsigned x) { return __builtin_amdgcn_mov_dpp(x, 0x150 + LANE, 0xf, 0xf, true); }
DI void lgkm0() { asm volatile("s_waitcnt lgkmcnt(0)" ::: "memory"); }
template <int N> DI void waitvm() { asm volatile("s_waitcnt vmcnt(%0)" ::"n"(N) : "memory"); }
DI __amdgpu_buffer_rsrc_t mkrs(const void* p, unsigned bytes) { return __builtin_amdgcn_make_buffer_rsrc((void*)p, 0, bytes, 0x00020000); }
DI void dma8(__amdgpu_buffer_rsrc_t rs, unsigned voff, unsigned soff, unsigned lds) {
  unsigned keep, s2;
  asm volatile("s_mov_b32 %0, m0\n\ts_mov_b32 m0, %5\n\ts_add_u32 %1, %4, 0x1000\n\ts_nop 2\n\t"
               "buffer_load_dwordx4 %2, %3, %4 offen nt lds\n\t"
               "buffer_load_dwordx4 %2, %3, %4 offen offset:1024 nt lds\n\t"
               "buffer_load_dwordx4 %2, %3, %4 offen offset:2048 nt lds\n\t"
               "buffer_load_dwordx4 %2, %3, %4 offen offset:3072 nt lds\n\t"
               "s_add_u32 m0, m0, 0x1000\n\ts_nop 0\n\t"
               "buffer_load_dwordx4 %2, %3, %1 offen nt lds\n\t"
               "buffer_load_dwordx4 %2, %3, %1 offen offset:1024 nt lds\n\t"
               "buffer_load_dwordx4 %2, %3, %1 offen offset:2048 nt lds\n\t"
               "buffer_load_dwordx4 %2, %3, %1 offen offset:3072 nt lds\n\t"
               "s_mov_b32 m0, %0"
               : "=&s"(keep), "=&s"(s2) : "v"(voff), "s"(rs), "s"(soff), "s"(lds) : "memory", "scc");
}
DI v4u gld(__amdgpu_buffer_rsrc_t rs, int off, int soff = 0) { return __builtin_amdgcn_raw_buffer_load_b128(rs, off, soff, SC1); }
DI unsigned gbad(v4u g, unsigned ta, unsigned tb, unsigned acc) { return __builtin_amdgcn_bitop3_b32(g.w, tb, __builtin_amdgcn_bitop3_b32(g.y, ta, acc, 0xBE), 0xBE); }
DI void gst(__amdgpu_buffer_rsrc_t rs, int off, unsigned a, unsigned b, unsigned ta, unsigned tb) { __builtin_amdgcn_raw_buffer_store_b128(v4u{a, ta, b, tb}, rs, off, 0, SC1); }

#define ST(i) do { } while (0)

typedef volatile LAS unsigned* ctl_t;
template <int SLP> DI bool wait_ge(ctl_t ctl, int idx, unsigned v) {
#pragma unroll 1
  for (unsigned s = 0; rfl(ctl[idx]) < v; ++s) {
    if (s > SPIN_CAP) return false;
    __builtin_amdgcn_s_sleep(SLP);
  }
  asm volatile("" ::: "memory");
  return true;
}
DI void lds_add(LAS unsigned char* L, int idx, unsigned v, int lane) {
  lgkm0();
  if (lane == 0) __hip_atomic_fetch_add((LAS unsigned*)(L + L_CTL) + idx, v, __ATOMIC_RELAXED, __HIP_MEMORY_SCOPE_WORKGROUP);
}
DI bool last_arriver(LAS unsigned char* L, int idx, unsigned n, int lane) {
  lgkm0();
  unsigned old = 0;
  if (lane == 0) old = __hip_atomic_fetch_add((LAS unsigned*)(L + L_CTL) + idx, 1u, __ATOMIC_RELAXED, __HIP_MEMORY_SCOPE_WORKGROUP);
  old = rfl(old);
  asm volatile("" ::: "memory");
  return old == n - 1;
}
DI void read_k(LAS unsigned char* L, unsigned off, v4u (&K)[4]) {
#pragma unroll
  for (int kk = 0; kk < 4; ++kk) K[kk] = *(const LAS v4u*)(L + L_RK + (off ^ (kk * 64)));
}
DI void read_v(LAS unsigned char* L, unsigned off, v4u (&V)[8]) {
#pragma unroll
  for (int blk = 0; blk < 8; ++blk) {
    LAS unsigned char* p = L + L_RV + (off ^ ((blk >> 1) * 64)) + (blk & 1) * 32;
    const v2u x = trd(p), y = trd(p + 4096);
    V[blk] = v4u{x.x, x.y, y.x, y.y};
  }
  __builtin_amdgcn_sched_barrier(0);
}
DI v4f qk(const v4u (&K)[4], const v4u (&q)[4]) {
  v4f d = {0.f, 0.f, 0.f, 0.f};
#pragma unroll
  for (int kk = 0; kk < 4; ++kk) d = mfma(K[kk], q[kk], d);
  return d;
}
DI v4f round2(v4f d) {
  const unsigned x01 = pk_bf16(d[0], d[1]), x23 = pk_bf16(d[2], d[3]);
  const unsigned b01 = pk_bf16(bf_lo(x01) * SCALE, bf_hi(x01) * SCALE), b23 = pk_bf16(bf_lo(x23) * SCALE, bf_hi(x23) * SCALE);
  return v4f{bf_lo(b01), bf_hi(b01), bf_lo(b23), bf_hi(b23)};
}
DI float sel(unsigned mask, float a, float b) { return fbits(__builtin_amdgcn_bitop3_b32(mask, ubits(a), ubits(b), 0xCA)); }
DI v4f sel4(unsigned mask, v4f a, v4f b) { return v4f{sel(mask, a[0], b[0]), sel(mask, a[1], b[1]), sel(mask, a[2], b[2]), sel(mask, a[3], b[3])}; }
DI v2f sel2(unsigned mask, v2f a, v2f b) { return v2f{sel(mask, a[0], b[0]), sel(mask, a[1], b[1])}; }
DI unsigned bitmask(int x, int bit) { unsigned m = (unsigned)((x << (31 - bit)) >> 31); asm("" : "+v"(m)); return m; }
DI float max4(v4f d) { return mx2(mx2(d[0], d[1]), mx2(d[2], d[3])); }
DI float sum4(v4f d) { return (d[0] + d[1]) + (d[2] + d[3]); }
DI v4f expo(v4f s, float m2) { return v4f{ex2(fmaf(s[0], L2E, -m2)), ex2(fmaf(s[1], L2E, -m2)), ex2(fmaf(s[2], L2E, -m2)), ex2(fmaf(s[3], L2E, -m2))}; }
DI v4u pack_p(v4f p0, v4f p1) { return v4u{pk_bf16(p0[0], p0[1]), pk_bf16(p0[2], p0[3]), pk_bf16(p1[0], p1[1]), pk_bf16(p1[2], p1[3])}; }

DI unsigned now32() { return (unsigned)__builtin_amdgcn_s_memrealtime(); }
DI void wait_until(unsigned t0, unsigned until) {
#pragma unroll 1
  for (unsigned sp = 0; sp < 64 && now32() - t0 < until; ++sp) { }
}
DI void hint_set(__amdgpu_buffer_rsrc_t ws, int off, unsigned t, unsigned tag) {
  asm volatile("buffer_atomic_swap_x2 %0, %1, %2, 0 offen" ::"v"(v2u{t, tag}), "v"(off), "s"(ws) : "memory");
}
template <bool TWO> DI unsigned hint_time(__amdgpu_buffer_rsrc_t ws, int off, int off2, unsigned tag, unsigned t0) {
  unsigned rel = 0;
  waitvm<0>();
#define HP1(A0, A1, o) asm volatile("v_mov_b32 v16" #A0 ", 0\n\tv_mov_b32 v16" #A1 ", 0\n\tbuffer_atomic_add_x2 v[16" #A0 ":16" #A1 "], %0, %1, 0 offen sc0" \
                                    ::"v"(o), "s"(ws) : "memory", "v16" #A0, "v16" #A1)
#define HC1(A0, A1, o, N) { unsigned t_, g_; asm volatile("s_waitcnt vmcnt(%2)\n\tv_mov_b32 %0, v16" #A0 "\n\tv_mov_b32 %1, v16" #A1 : "=v"(t_), "=v"(g_) : "n"(N) : "memory", "v16" #A0, "v16" #A1); \
                            const bool ok_ = g_ == tag; rel = ok_ ? max(rel, t_ - t0) : rel; o = ok_ ? OOB : o; }
  HP1(0, 1, off);
  if (TWO) HP1(2, 3, off2);
  __builtin_amdgcn_s_sleep(PSTAG);
#pragma unroll 1
  for (unsigned sp = 0; sp < 4096; ++sp) {
    HP1(4, 5, off);
    if (TWO) HP1(6, 7, off2);
    HC1(0, 1, off, TWO ? 3 : 1)
    if (TWO) HC1(2, 3, off2, 2)
    if (__all(off == OOB && off2 == OOB)) break;
    HP1(0, 1, off);
    if (TWO) HP1(2, 3, off2);
    HC1(4, 5, off, TWO ? 3 : 1)
    if (TWO) HC1(6, 7, off2, 2)
    if (__all(off == OOB && off2 == OOB)) break;
  }
#undef HP1
#undef HC1
  return rfl((unsigned)max8to64(max8((float)rel)));
}
DI bool park_rec(LAS unsigned char* L, ctl_t ctl, int slot, int adr, int dstr, int mladr, int gi, int lane, v2f o, float m, float l) {
  LAS unsigned char* h = L + L_HOLD + slot * HOLD_B;
  *(LAS v2f*)(h + 32 + gi * 8) = o;
  if (lane == 0) { *(LAS v4u*)h = v4u{(unsigned)adr, (unsigned)dstr, (unsigned)mladr, 0u}; *(LAS v2f*)(h + 16) = v2f{m, l}; }
  lgkm0();
  if (lane == 0) __hip_atomic_fetch_or((LAS unsigned*)(L + L_CTL) + C_HELD, 1u << slot, __ATOMIC_RELAXED, __HIP_MEMORY_SCOPE_WORKGROUP);
  lgkm0();
  return rfl(ctl[C_PHASE]) >= GOAT;
}
template <int NR> DI v2f merge_rec(LAS unsigned char* rec, int lane, float& Mo, float& Lo) {
  v2f av[NR];
  const v2f ml = *(LAS v2f*)(rec + (lane & (NR - 1)) * REC_B);
#pragma unroll
  for (int w = 0; w < NR; ++w) av[w] = *(LAS v2f*)(rec + w * REC_B + 16 + lane * 8);
  float M = ml.x; M = mx2(M, dpp<0xB1>(M)); M = mx2(M, dpp<0x4E>(M));
  if (NR == 8) M = mx2(M, dpp<0x141>(M));
  const float f = ex2(ml.x - M);
  float Ls = f * ml.y; Ls += dpp<0xB1>(Ls); Ls += dpp<0x4E>(Ls);
  if (NR == 8) Ls += dpp<0x141>(Ls);
  float a0 = 0.f, a1 = 0.f;
#pragma unroll
  for (int w = 0; w < NR; ++w) { const float fw = fbits(__builtin_amdgcn_readlane(ubits(f), w)); a0 = fmaf(fw, av[w].x, a0); a1 = fmaf(fw, av[w].y, a1); }
  Mo = M; Lo = Ls;
  return v2f{a0, a1};
}

__global__ void __launch_bounds__(NTHR) fused_decode(Args a) {
  __shared__ __attribute__((aligned(16))) unsigned char smem[L_END];
  LAS unsigned char* L = (LAS unsigned char*)smem;
  ctl_t ctl = (ctl_t)(L + L_CTL);
  asm volatile("" ::"s"(a.xA), "s"(a.kA), "s"(a.vA), "s"(a.wqA), "s"(a.woA), "s"(a.xB), "s"(a.kB), "s"(a.vB), "s"(a.wqB), "s"(a.woB), "s"(a.out), "s"(a.ws));
  const int wg = blockIdx.x, g = wg >> 3, j = wg & 7;
  const int lane = threadIdx.x & 63, w = rfl(threadIdx.x >> 6);
  const __amdgpu_buffer_rsrc_t ws = mkrs(a.ws, W_ENDB);
  ST(w == 1 ? 0 : 31);

  if (w == 0) {
    __builtin_amdgcn_s_waitcnt(0);
    const unsigned lb = (unsigned)(uintptr_t)L;
    const unsigned voff = (lane * 16) ^ ((lane >> 4) << 6);
    const unsigned offA = g * (2u << 20) + (3u << 19) + j * 16384;
    __amdgpu_buffer_rsrc_t rk = mkrs(a.kA, 64u << 20), rv = mkrs(a.vA, 64u << 20);
    dma8(rk, voff, offA, lb + L_RK); dma8(rk, voff, offA + 8192, lb + L_RK + 8192);
    asm volatile("s_barrier" ::: "memory");
    int u = 4, pub = 0, n = 0, win = WINU;
    unsigned soff = offA;
    unsigned k = 0, cid = 0, dsc = 0;
    bool second = false;
#define TB()
#define TE(x)
#define ISSUE_CHUNK(rs, so, ld) do { \
      dma8(rs, voff, so, ld); if (win == WINU) waitvm<4 * WINU>(); else waitvm<4 * WINL>(); u += 2; if (u - win > pub) { pub = u - win; ctl[C_FULL] = pub; } \
      dma8(rs, voff, (so) + 8192, (ld) + 8192); if (win == WINU) waitvm<4 * WINU>(); else waitvm<4 * WINL>(); u += 2; if (u - win > pub) { pub = u - win; ctl[C_FULL] = pub; } } while (0)
#pragma unroll 1
    for (;; ++n) {
      const int nk = n + 1;
      const bool need = nk >= NS && !second;
      unsigned t;
#pragma unroll 1
      for (unsigned sp = 0;; ++sp) {
        const unsigned tk = ctl[C_TICK + k], fk = ctl[C_FREE + (nk & 3)], fv = ctl[C_FREE + 4 + (n & 3)];
        t = need ? rfl(tk) : 0u;
        if (t != 0xffffffffu && rfl(fk) >= (unsigned)(nk & ~3) && rfl(fv) >= (unsigned)(n & ~3)) break;
        if (sp > SPIN_CAP) return;
        TB();
        if (sp == 6) { waitvm<0>(); pub = u; ctl[C_FULL] = pub; }
        __builtin_amdgcn_s_sleep(1);
      }
      const bool last = t >= (unsigned)NT;
      if (t >= (unsigned)LATE) win = WINL;
      const unsigned sv = soff;
      unsigned want2 = 0;
      if (!last) {
        if (nk >= NS) {
          if (second) { ++cid; dsc |= 1u << 10; second = false; }
          else {
            second = t < (unsigned)TP;
            cid = second ? 2 * t : TP + t;
            dsc = (8 + t) | ((NS + k) & 3) << 8 | (second ? 0u : 1u << 10) | (4 + k) << 12;
            ++k;
          }
          ctl[C_DESC + nk - NS] = dsc;
        }
        want2 = nk + 2 < NS ? 0 : nk + 2 == NS || second || (nk < NS ? TP > 8 : cid + 16 < 2 * TP) ? k + 1 : k + 2;
        if (AH2 == 4) { if (want2) ctl[C_WANT] = want2; }
        else if (AH2 == 2 && nk + 1 >= NS && !second) ctl[C_WANT] = k + 1;
        if (nk == 4) rk = mkrs(a.kB, 128u << 20);
        soff = nk < 4 ? offA + nk * 131072
                      : (nk < 20 ? g : 32 + g) * (2u << 20) + (((nk < 20 ? (nk - 4) * 8 + j : nk < NS ? (nk - 20) * 8 + j : NSTAT * 8 + cid) + g * ROT) & 127) * 16384;
        ISSUE_CHUNK(rk, soff, lb + L_RK + (nk & 3) * 16384);
      } else {
        ctl[C_END] = nk; ctl[C_PHASE] = 1; ST(8);
      }
      if (AH2 == 3 && want2) ctl[C_WANT] = want2;
      if (n == 4) rv = mkrs(a.vB, 128u << 20);
      ISSUE_CHUNK(rv, sv, lb + L_RV + (n & 3) * 16384);
      if (last) break;
    }
    ctl[C_PHASE] = 2;
    ST(9);
#define DRAIN(k_) if (WINL > k_) { waitvm<4 * k_>(); const int p_ = u - k_ + (k_ < 4 ? 4 : 0); if (p_ > pub) { pub = p_; ctl[C_FULL] = pub; } \
      if (k_ == 0) ctl[C_PHASE] = 3; }
    DRAIN(10) DRAIN(8) DRAIN(6) DRAIN(4) DRAIN(2) DRAIN(0)
#undef DRAIN
    ST(10);
    return;
  }
  const unsigned tstart = now32();
  const int cb = (wg & ~15) + ((wg & 7) << 1) + ((wg >> 3) & 1);
  const int side = j >> 2, prod = g * 4 + (j & 3);
  unsigned ta, tb;
  int jb, jend;
  if (w == 1) {
    const v2u ep = __builtin_amdgcn_raw_buffer_load_b64(ws, W_X, 0, SC1);
    if (cb >= 128 && (cb & 3) == 0 && lane == 0) __builtin_amdgcn_raw_buffer_store_b32(0u, ws, W_CTR + ((cb - 128) >> 2) * 256, 0, SC1);
    ctl[lane] = lane == C_END ? 0x7fffffffu : 0u;
    ctl[lane + 64] = 0xffffffffu; ctl[lane + 128] = 0xffffffffu; ctl[lane + 192] = 0xffffffffu;
    asm volatile("s_waitcnt lgkmcnt(0)\n\ts_barrier" ::: "memory");
    ta = rfl(ep.x) + 1u; tb = rfl(ep.y) + 1u;
    ta += ta == 0u; tb += tb == 0u;
    waitvm<0>();
    if (!wait_ge<1>(ctl, C_QCNT, 9)) return;
    ST(1);
    if (lane < 16) {
      v4f s = {0.f, 0.f, 0.f, 0.f};
#pragma unroll
      for (int i = 0; i < 9; ++i) s += *(LAS v4f*)(L + L_QP + i * 256 + lane * 16);
      const int b = lane >> 3, quad = lane & 7;
      const int off = cb < 128 ? W_QA + ((b * 16 + (cb >> 3)) * 64 + (cb & 7) * 8 + quad) * 16 : W_QB + (((cb - 128) >> 2) * 64 + b * 32 + (cb & 3) * 8 + quad) * 16;
      gst(ws, off, pk_bf16(s.x, s.y), pk_bf16(s.z, s.w), ta, tb);
    }
    v4u qa, qb;
#pragma unroll 1
    for (unsigned sp = 0;; ++sp) {
      __builtin_amdgcn_s_sleep(QSLP);
      qa = gld(ws, W_QA + g * 1024 + lane * 16);
      qb = gld(ws, W_QB + g * 1024 + lane * 16);
      if (__all(gbad(qb, ta, tb, gbad(qa, ta, tb, 0u)) == 0u)) break;
      if (sp > SPIN_CAP) return;
    }
    *(LAS v2u*)(L + L_Q + lane * 8) = v2u{qa.x, qa.z};
    *(LAS v2u*)(L + L_Q + 512 + lane * 8) = v2u{qb.x, qb.z};
    ctl[C_TAGA] = ta; ctl[C_TAGB] = tb;
    lgkm0();
    ctl[C_QRDY] = 1;
    ST(2);
    {
      int c0 = 0, c1 = 0, mine = 0, pend = 0;
      bool may_quit = QB > 0;
#pragma unroll 1
      for (int k = 0; k <= NT; ++k) {
        if (!wait_ge<2>(ctl, C_WANT, k + 1)) return;
        const int dd = c1 - c0;
        const bool quit = may_quit && mine > 0 && (POOLC - c1) * mine - dd < QB * (dd - QM * mine);
        unsigned t = 0;
        if (lane == 0) t = __hip_atomic_fetch_add((GAS unsigned*)(a.ws + W_CTR + g * 256), quit ? 0x10000u : 1u, __ATOMIC_RELAXED, __HIP_MEMORY_SCOPE_AGENT);
        t = rfl(t);
        if (quit) {
          if (t >> 16 >= 5) { may_quit = false; --k; continue; }
          t = NT;
        }
        t &= 0xffffu;
        ctl[C_TICK + k] = t;
        if (t >= (unsigned)NT) break;
        c1 = t < (unsigned)TP ? 2 * t : TP + t;
        if (k == 0) c0 = c1;
        mine += pend;
        pend = t < (unsigned)TP ? 2 : 1;
      }
    }
    if (!wait_ge<1>(ctl, C_T1, 1)) return;
    hint_set(ws, lane < NREP ? W_F2 + (side * NREP + lane) * 1024 + prod * 8 : OOB, ctl[C_T1], ta);
    if ((prod >> 1) < 36) {
      const int ho = W_F2 + (side * NREP + prod % NREP) * 1024 + lane * 8;
      ctl[C_T2] = (hint_time<true>(ws, ho, ho + 512, ta, tstart) + tstart + DT2) | 1u;
    }
    return;
  } else if (w == 2) {
    asm volatile("s_barrier" ::: "memory");
    if (!wait_ge<16>(ctl, C_QRDY, 1)) return;
    ta = rfl(ctl[C_TAGA]); tb = rfl(ctl[C_TAGB]);
    jb = DRY ? 0 : 2; jend = 3;
  } else {
    {
      const int qw = w - 3;
      const int r0 = qw * 64 + (lane >> 2) * 4;
      const unsigned char* xp = (cb < 128 ? a.xA : a.xB) + r0 * 2;
      const unsigned char* wp = (cb < 128 ? a.wqA : a.wqB) + (size_t)r0 * 8192 + (cb & 127) * 64 + (lane & 3) * 16;
      v4u Wq[4];
#pragma unroll
      for (int i = 0; i < 4; ++i) Wq[i] = __builtin_nontemporal_load((const GAS v4u*)(wp + i * 8192));
      const v2u X0 = *(const GAS v2u*)xp, X1 = *(const GAS v2u*)(xp + 1152);
      asm volatile("s_barrier" ::: "memory");
      float ac[2][8];
#pragma unroll
      for (int k = 0; k < 8; ++k) { ac[0][k] = 0.f; ac[1][k] = 0.f; }
#pragma unroll
      for (int s = 0; s < 2; ++s)
#pragma unroll
        for (int d = 0; d < 4; ++d) {
          const unsigned lo = __builtin_amdgcn_perm(Wq[2 * s + 1][d], Wq[2 * s][d], 0x05040100u), hi = __builtin_amdgcn_perm(Wq[2 * s + 1][d], Wq[2 * s][d], 0x07060302u);
          ac[0][2 * d] = dot2(lo, X0[s], ac[0][2 * d]); ac[0][2 * d + 1] = dot2(hi, X0[s], ac[0][2 * d + 1]);
          ac[1][2 * d] = dot2(lo, X1[s], ac[1][2 * d]); ac[1][2 * d + 1] = dot2(hi, X1[s], ac[1][2 * d + 1]);
        }
      float b8[8], c4[4];
#pragma unroll
      for (int k = 0; k < 8; ++k) b8[k] = rs32(ac[0][k], ac[1][k]);
#pragma unroll
      for (int k = 0; k < 4; ++k) { float t = rs16(b8[k], b8[k + 4]); t += dpp<0x128>(t); t += dpp<0x124>(t); c4[k] = t; }
      if ((lane & 12) == 0) *(LAS v4f*)(L + L_QP + qw * 256 + (lane >> 5) * 128 + (lane & 3) * 32 + ((lane >> 4) & 1) * 16) = v4f{c4[0], c4[1], c4[2], c4[3]};
      lds_add(L, C_QCNT, 1, lane);
    }
    const int r = lane >> 4, c = lane & 15;
    const unsigned kof = c * 256 + r * 16 + (c & 3) * 64;
    const unsigned vof = (4 * r + (c >> 2)) * 256 + (c >> 2) * 64 + (c & 3) * 8;
    if (w >= 4) {
      const unsigned aw = w - 4, off = aw * 8192, an = aw >> 1, ah = 2 * (aw & 1) + 2;
      v4u K0[4], K1[4], V[8];
      if (!wait_ge<1>(ctl, C_FULL, (an ? 8 * an - 4 : 0) + ah)) return;
      read_k(L, off + kof, K0); read_k(L, off + 4096 + kof, K1);
      lds_add(L, C_FREE + an, 2, lane);
      if (!wait_ge<1>(ctl, C_FULL, 8 * an + 8 + ah)) return;
      read_v(L, off + vof, V);
      lds_add(L, C_FREE + 4 + an, 2, lane);
      if (!wait_ge<1>(ctl, C_QRDY, 1)) return;
      const LAS v4u* qp = (const LAS v4u*)(L + L_Q) + r + (c >> 3) * 16;
      const v4u q[4] = {qp[0], qp[4], qp[8], qp[12]};
      v4f s0 = round2(qk(K0, q)), s1 = round2(qk(K1, q));
      const float M = xmax(mx2(max4(s0), max4(s1))) * L2E;
      s0 = expo(s0, M); s1 = expo(s1, M);
      const v4u P = pack_p(s0, s1);
      const float Ls = xsum(sum4(s0) + sum4(s1));
      v4f acc[8];
#pragma unroll
      for (int blk = 0; blk < 8; ++blk) acc[blk] = mfma(V[blk], P, v4f{0.f, 0.f, 0.f, 0.f});
#pragma unroll
      for (int k = 0; k < 4; ++k) acc[k] = sel4(bitmask(c, 2), acc[k + 4], acc[k]);
#pragma unroll
      for (int k = 0; k < 2; ++k) acc[k] = sel4(bitmask(c, 1), acc[k + 2], acc[k]);
      acc[0] = sel4(bitmask(c, 0), acc[1], acc[0]);
      LAS unsigned char* rec = L + L_REC + ((c >> 3) * 8 + aw) * REC_B;
      *(LAS v4f*)(rec + 16 + ((c & 7) * 16 + r * 4) * 4) = acc[0];
      if ((lane & 0x37) == 0) *(LAS v2f*)rec = v2f{M, Ls};
      if (last_arriver(L, C_CNT, 8, lane)) {
#pragma unroll 1
        for (int e = 0; e < 2; ++e) {
          float Mo, Lo;
          const v2f o = merge_rec<8>(L + L_REC + e * 8 * REC_B, lane, Mo, Lo);
          const int base = W_P + (g * 2 + e) * P_STR;
          if (park_rec(L, ctl, e, base + j * 128, 1024, base + 8192 + j * 16, lane, lane, o, Mo, Lo)) {
            const unsigned ta_ = ctl[C_TAGA], tb_ = ctl[C_TAGB];
            gst(ws, base + j * 128 + (lane >> 3) * 1024 + (lane & 7) * 16, ubits(o.x), ubits(o.y), ta_, tb_);
            if (lane == 0) gst(ws, base + 8192 + j * 16, ubits(Mo), ubits(Lo), ta_, tb_);
          }
        }
        ST(4);
      }
    } else if (!wait_ge<8>(ctl, C_QRDY, 1)) return;
    ta = rfl(ctl[C_TAGA]); tb = rfl(ctl[C_TAGB]);
    if (w >= 8) {
      const int cw = w - 8;
      v4u q[4];
      float m = -INFINITY, l = 0.f;
      v4f acc[8];
      bool fresh = true;
      const unsigned mk0 = bitmask(c, 0), mk1 = bitmask(c, 1), mk2 = bitmask(c, 2), mk3 = bitmask(c, 3);
      const int gid = (c >> 1) * 8 + 2 * r + (c & 1);
#pragma unroll 1
      for (unsigned n = 4 + cw;; n = n + 4 < NS ? n + 4 : n < NS ? NS : n + 1) {
        bool stop = false;
        unsigned dsc = cw << 8;
        const bool dyn = n >= NS;
        if (dyn) {
#pragma unroll 1
          for (unsigned sp = 0;; ++sp) {
            const unsigned dd = ctl[C_DESC + n - NS], en = ctl[C_END];
            dsc = rfl(dd);
            if (dsc != 0xffffffffu) break;
            if (n >= rfl(en) || sp > SPIN_CAP) { stop = true; break; }
            __builtin_amdgcn_s_sleep(2);
          }
          if (stop) break;
          if ((dsc >> 8 & 3) != (unsigned)cw) continue;
        }
        const bool first = fresh;
        if (fresh) {
          const LAS v4u* qp = (const LAS v4u*)(L + L_Q) + r + (n < 20 ? 32 : 48);
#pragma unroll
          for (int kk = 0; kk < 4; ++kk) q[kk] = qp[4 * kk];
          m = -INFINITY; l = 0.f;
#pragma unroll
          for (int blk = 0; blk < 8; ++blk) acc[blk] = v4f{0.f, 0.f, 0.f, 0.f};
          fresh = false;
        }
        const bool pubn = dyn ? (dsc >> 10 & 1) : (n >= NS - 4 || (n & ~3u) == 16);
        const int base = n >= 20 ? W_P1 + g * P1_STR : W_P + (64 + g) * P_STR, dstr = n >= 20 ? 8192 : 1024, rec = dyn ? dsc & 255 : j, slot = dyn ? dsc >> 12 : 2 + (n >= 20);
        const int gi = dyn ? gid : lane;
        int ad = base + rec * 128 + (gi >> 3) * dstr + (gi & 7) * 16, am = lane == 0 ? base + 8 * dstr + rec * 16 : OOB;
        unsigned ka = (n & 3) * 16384 + kof, va = (n & 3) * 16384 + vof;
        asm volatile("" : "+v"(ad), "+v"(am), "+v"(ka), "+v"(va), "+v"(q[0]), "+v"(q[1]), "+v"(q[2]), "+v"(q[3]));
        if (!wait_ge<1>(ctl, C_FULL, 8 * n)) return;
        v4f s[4];
        v4u K[4][4];
#pragma unroll
        for (int uu = 0; uu < 4; ++uu) read_k(L, ka + uu * 4096, K[uu]);
        __builtin_amdgcn_sched_barrier(0);
#pragma unroll
        for (int uu = 0; uu < 4; ++uu) s[uu] = qk(K[uu], q);
        __builtin_amdgcn_sched_barrier(0);
        lgkm0();
        ctl[C_FREE + (n & 3)] = (n & ~3u) + 4;
        v4f sc = round2(sel4(mk3, sel4(mk2, s[3], s[2]), sel4(mk2, s[1], s[0])));
        const float mn = mx2(max4to64(max4(sc)) * L2E, m);
        v4u st = *(volatile LAS v4u*)(L + L_CTL);
        sc = expo(sc, mn);
        const unsigned px = pk_bf16(sc[0], sc[1]), py = pk_bf16(sc[2], sc[3]);
        v4u P0 = {bcast16<0>(px), bcast16<0>(py), bcast16<4>(px), bcast16<4>(py)}, P1 = {bcast16<8>(px), bcast16<8>(py), bcast16<12>(px), bcast16<12>(py)};
        if (!first) {
          const float al = ex2(m - mn);
          l *= al;
#pragma unroll
          for (int blk = 0; blk < 8; ++blk) acc[blk] *= al;
        }
        m = mn;
        asm volatile("" : "+v"(P0), "+v"(P1), "+v"(acc[0]), "+v"(acc[1]), "+v"(acc[2]), "+v"(acc[3]), "+v"(acc[4]), "+v"(acc[5]), "+v"(acc[6]), "+v"(acc[7]));
        v4u V[8];
#define WAITV(need) _Pragma("unroll 1") for (unsigned sp = 0; rfl(st.x) < (need); ++sp) { if (sp > SPIN_CAP) return; if (sp) __builtin_amdgcn_s_sleep(1); st = *(volatile LAS v4u*)(L + L_CTL); } \
        asm volatile("" ::: "memory");
        WAITV(8 * n + 10)
        read_v(L, va, V);
        l += sum4(sc);
        float Ls = l + dpp<0x124>(l);
        Ls += dpp<0x128>(Ls);
        Ls = xsum(Ls);
        asm volatile("" : "+v"(Ls));
#pragma unroll
        for (int blk = 0; blk < 8; ++blk) acc[blk] = mfma(V[blk], P0, acc[blk]);
        WAITV(8 * n + 12)
#undef WAITV
        const unsigned tl = now32(), nend = rfl(st.y);
        read_v(L, va + 8192, V);
        hint_set(ws, lane == 0 && n + 1 == nend ? W_F1 + wg * 128 : OOB, tl, ta);
#pragma unroll
        for (int blk = 0; blk < 8; ++blk) acc[blk] = mfma(V[blk], P1, acc[blk]);
        if (pubn) {
          v2f o[8];
#pragma unroll
          for (int k = 0; k < 8; ++k) o[k] = sel2(mk0, v2f{acc[k][2], acc[k][3]}, v2f{acc[k][0], acc[k][1]});
#pragma unroll
          for (int k = 0; k < 4; ++k) o[k] = sel2(mk3, o[k + 4], o[k]);
#pragma unroll
          for (int k = 0; k < 2; ++k) o[k] = sel2(mk2, o[k + 2], o[k]);
          o[0] = sel2(mk1, o[1], o[0]);
          bool now = dyn && (slot >= NHOLD || rfl(st.z) >= GOAT);
          if (!now) {
            bool emit = true;
            if (!dyn) {
              LAS unsigned char* rp = L + L_REC + (8 + slot * 4) * REC_B;
              *(LAS v2f*)(rp + cw * REC_B + 16 + gid * 8) = o[0];
              if (lane == 0) *(LAS v2f*)(rp + cw * REC_B) = v2f{m, Ls};
              emit = last_arriver(L, C_CNT + slot - 1, 4, lane);
              if (emit) o[0] = merge_rec<4>(rp, lane, m, Ls);
            }
            if (emit) now = park_rec(L, ctl, slot, base + rec * 128, dstr, base + 8 * dstr + rec * 16, gi, lane, o[0], m, Ls);
            if (!now) { ad = OOB; am = OOB; }
          }
          gst(ws, ad, ubits(o[0].x), ubits(o[0].y), ta, tb);
          gst(ws, am, ubits(m), ubits(Ls), ta, tb);
          ST(n >= 20 ? 7 : 5);
          fresh = true;
        }
        lgkm0();
        ctl[C_FREE + 4 + (n & 3)] = (n & ~3u) + 4;
      }
      jb = 3; jend = 4;
    } else {
      const int k = w - 3;
      v4u Wf[2][4];
      v2u Wr[2][8];
      const unsigned char* pw = (side ? a.woB : a.woA) + (size_t)((side ? g : 2 * (g & 15) + (g >> 4)) * 128 + (j & 3) * 32 + 8 * r) * 1152 + c * 8;
      const int yreal = W_Y + ((side ? 3 - (r & 1) : (r & 1)) * 36 + k * 4 + (c >> 2)) * YS + prod * 128 + (2 * (c & 3) + (r >> 1)) * 16;
      const unsigned mh = bitmask(r, 1);
#define WO_LOAD() _Pragma("unroll") for (int b = 0; b < 2; ++b) _Pragma("unroll") for (int i = 0; i < 8; ++i) \
        Wr[b][i] = __builtin_nontemporal_load((const GAS v2u*)(pw + i * 1152 + (b && k < 4 ? k + 5 : k) * 128));
#define WO_PACK() _Pragma("unroll") for (int b = 0; b < 2; ++b) { _Pragma("unroll") for (int d = 0; d < 4; ++d) { \
          Wf[b][0][d] = __builtin_amdgcn_perm(Wr[b][2 * d + 1].x, Wr[b][2 * d].x, 0x05040100u); Wf[b][1][d] = __builtin_amdgcn_perm(Wr[b][2 * d + 1].x, Wr[b][2 * d].x, 0x07060302u); \
          Wf[b][2][d] = __builtin_amdgcn_perm(Wr[b][2 * d + 1].y, Wr[b][2 * d].y, 0x05040100u); Wf[b][3][d] = __builtin_amdgcn_perm(Wr[b][2 * d + 1].y, Wr[b][2 * d].y, 0x07060302u); } \
        asm volatile("" : "+v"(Wf[b][0]), "+v"(Wf[b][1]), "+v"(Wf[b][2]), "+v"(Wf[b][3])); }
      if (!WOAT) { WO_LOAD() WO_PACK() }
#pragma unroll 1
      for (int pass = DRY && !WOAT ? 0 : 1; pass < 2; ++pass) {
        if (!wait_ge<16>(ctl, C_WANT, pass)) return;
        if (TOUCH && k == 0) {
          const unsigned t0 = __builtin_nontemporal_load((const GAS unsigned*)(pw + 7 * 1152 - c * 8)), t1 = __builtin_amdgcn_raw_buffer_load_b32(ws, lane & 1 ? W_SAFE1 : W_SAFE2, 0, SC1);
          asm volatile("" ::"v"(t0), "v"(t1));
        }
        if (!wait_ge<2>(ctl, C_PHASE, pass * GOAT)) return;
        const unsigned held = pass ? rfl(ctl[C_HELD]) : 31u;
        asm volatile("" ::: "memory");
#pragma unroll 1
        for (int slot = k; slot < NHOLD; slot += 5) {
          if (!(held >> slot & 1)) continue;
          LAS unsigned char* h = L + L_HOLD + slot * HOLD_B;
          const v4u hd = *(LAS v4u*)h;
          const v2u ml = *(LAS v2u*)(h + 16), o = *(LAS v2u*)(h + 32 + lane * 8);
          gst(ws, pass ? hd.x + (lane >> 3) * hd.y + (lane & 7) * 16 : OOB, o.x, o.y, ta, tb);
          gst(ws, pass && lane == 0 ? hd.z : OOB, ml.x, ml.y, ta, tb);
        }
        if (w == 4) ST(14);
        if (WOAT) {
          if (!wait_ge<2>(ctl, C_PHASE, WOAT)) return;
          if (WPACE) { const int kk = k + WPACE - 1; if (kk > 0 && !wait_ge<1>(ctl, C_FULL, 8 * rfl(ctl[C_END]) + (kk == 1 ? -6 : kk == 2 ? -4 : kk == 3 ? 2 : 4))) return; }
          WO_LOAD() WO_PACK()
        }
        int y0 = pass ? yreal : OOB, y1 = pass && k < 4 ? yreal + 20 * YS : OOB;
        asm volatile("" : "+v"(y0), "+v"(y1));
        v4u oa;
#pragma unroll 1
        for (unsigned sp = 0;; ++sp) {
          const unsigned fl = ctl[C_OSEQ];
          oa = *(volatile LAS v4u*)(L + L_OP + ((c >> 2) & 1) * 64 + r * 16);
          if (rfl(fl) >= (unsigned)pass) break;
          if (sp > SPIN_CAP) return;
          __builtin_amdgcn_s_sleep(1);
        }
#pragma unroll
        for (int b = 0; b < 2; ++b) {
          v4f d[4];
#pragma unroll
          for (int f = 0; f < 4; ++f) d[f] = mfma(oa, Wf[b][f], v4f{0.f, 0.f, 0.f, 0.f});
          gst(ws, b ? y1 : y0, ubits(sel(mh, d[2][0], d[0][0])), ubits(sel(mh, d[3][0], d[1][0])), ta, tb);
        }
      }
      if (w == 4 && side) ST(12);
      return;
    }
  }
#pragma unroll 1
  for (; jb < jend; ++jb) {
    const bool dry = jb < 2;
    unsigned sp = 0;
    if (jb & 1) {
      const int qt = w & 3, c = side * 2 + (prod & 1), rho = prod >> 1;
      if (rho >= 36 && !dry) return;
      int voff = dry ? OOB : W_Y + (c * 36 + rho) * YS + qt * 4096 + lane * 16;
      int oo = lane < 16 && !dry ? ((c & 1) * 1152 + (c >> 1) * 576 + rho * 16 + lane) * 2 : OOB;
      asm volatile("" : "+v"(voff), "+v"(oo));
      if (!wait_ge<4>(ctl, C_T1, dry ? 0 : 1)) return;
      if (!wait_ge<1>(ctl, C_T2, dry ? 0 : 1)) return;
      const unsigned until = rfl(ctl[C_T2]) - tstart;
      wait_until(tstart, dry ? 0u : until);
      v4u X[4];
#pragma unroll 1
      for (;; ++sp) {
#pragma unroll
        for (int i = 0; i < 4; ++i) X[i] = gld(ws, voff, i * (FMODE ? 1024 : 4096));
        unsigned bad = 0;
#pragma unroll
        for (int i = 0; i < 4; ++i) bad = gbad(X[i], ta, tb, bad);
        if (__all(bad == 0u) || dry) break;
        if (sp > SPIN_CAP) return;
        __builtin_amdgcn_s_sleep(4);
      }
      float s = rs32((fbits(X[0].x) + fbits(X[1].x)) + (fbits(X[2].x) + fbits(X[3].x)), (fbits(X[0].z) + fbits(X[1].z)) + (fbits(X[2].z) + fbits(X[3].z)));
      s = rs16(s, s);
      s += dpp<0x128>(s);
      *(volatile LAS unsigned*)(L + L_FP + (((lane & 7) * 2 + (lane >> 5)) * 4 + qt) * 4) = dry ? ~0u : ubits(s);
      if (qt != 0 && !dry) return;
#pragma unroll 1
      for (unsigned s2 = 0;; ++s2) {
        const v4u p = *(volatile LAS v4u*)(L + L_FP + (lane & 15) * 16);
        s = (fbits(p.x) + fbits(p.y)) + (fbits(p.z) + fbits(p.w));
        if (__all(p.y != ~0u && p.z != ~0u && p.w != ~0u) || dry || s2 > SPIN_CAP) break;
      }
      __builtin_amdgcn_raw_buffer_store_b16((unsigned short)pk_bf16(s, s), mkrs(a.out, 4608), oo, 0, 0);
      __builtin_amdgcn_raw_buffer_store_b64(v2u{ta, tb}, ws, c == 3 && rho == 0 && qt == 0 && lane == 0 && !dry ? W_X : OOB, 0, SC1);
      if (!dry && qt == 0) ST(c == 3 ? 13 : 6);
    } else {
      const int nr1 = side ? 56 : 8;
      const int ta_ = (g & 15) * 2 + (g >> 4);
      const int b0 = side ? W_P1 + g * P1_STR : W_P + ta_ * P_STR, b1 = W_P + (side ? 64 + g : 32 + ta_) * P_STR, d0 = side ? 8192 : 1024, dest = 2 * (j & 3);
      int voff = b0 + dest * d0 + lane * 16;
      int voff7 = b1 + dest * 1024 + lane * 16;
      int voff8 = lane >= 56 ? b1 + 8192 + (lane - 56) * 16 : b0 + 8 * d0 + (lane < nr1 ? lane : 0) * 16;
      if (dry) { voff = OOB; voff7 = OOB; voff8 = OOB; }
      int voffb = voff + d0;
      asm volatile("" : "+v"(voff), "+v"(voffb), "+v"(voff7), "+v"(voff8));
      if (!wait_ge<16>(ctl, C_WANT, dry ? 0 : 1)) return;
      if (!wait_ge<2>(ctl, C_PHASE, dry ? 0 : POLLAT)) return;
      const int hoff = side ? (lane < 8 ? W_F1 + (g * 8 + lane) * 128 : OOB) : (lane < 16 ? W_F1 + ((g & 15) * 8 + (lane >> 3) * 128 + (lane & 7)) * 128 : OOB);
      const unsigned until = hint_time<false>(ws, dry ? OOB : hoff, OOB, ta, tstart) + DT1;
      wait_until(tstart, dry ? 0u : until);
      v4u X[2][8], ML;
#pragma unroll 1
      for (;; ++sp) {
        const unsigned tr = now32();
        ML = gld(ws, voff8);
#pragma unroll
        for (int i = 0; i < 7; ++i) { X[0][i] = gld(ws, voff, side ? i * 1024 : 0); X[1][i] = gld(ws, voffb, side ? i * 1024 : 0); }
        X[0][7] = gld(ws, voff7); X[1][7] = gld(ws, voff7 + 1024);
        if (!dry) ctl[C_T1] = tr | 1u;
        __builtin_amdgcn_sched_barrier(0);
        unsigned bad = gbad(ML, ta, tb, 0u);
#pragma unroll
        for (int i = 0; i < 8; ++i) bad = gbad(X[1][i], ta, tb, gbad(X[0][i], ta, tb, bad));
        if (__all(bad == 0u) || dry) break;
        if (sp > SPIN_CAP) return;
        __builtin_amdgcn_s_sleep(4);
      }
      const bool seg1 = lane >= 56;
      const float mr = fbits(ML.x);
      const float M = max8(mr);
      const float M0 = max8to64(seg1 ? -INFINITY : M);
      const float f = (lane >= nr1 && !seg1) ? 0.f : ex2(mr - (seg1 ? M : M0));
      float fb[8];
#pragma unroll
      for (int b = 0; b < 8; ++b) fb[b] = fbits(__builtin_amdgcn_ds_bpermute((8 * b + (lane >> 3)) * 4, ubits(f)));
      float Ls = f * fbits(ML.z);
      Ls += dpp<0xB1>(Ls); Ls += dpp<0x4E>(Ls); Ls += dpp<0x141>(Ls);
      const float L1 = fbits(__builtin_amdgcn_readlane(ubits(Ls), 56));
      float L0 = seg1 ? 0.f : Ls;
      L0 += dpp<0x128>(L0); L0 = xsum(L0);
      float inv = __builtin_amdgcn_rcpf(lane >= 32 ? L1 : L0);
      asm volatile("" : "+v"(inv));
      float oe[2], oo[2];
#pragma unroll
      for (int h = 0; h < 2; ++h) {
        float o0 = 0.f, o1 = 0.f;
#pragma unroll
        for (int b = 0; b < 7; ++b) { o0 = fmaf(fb[b], fbits(X[h][b].x), o0); o1 = fmaf(fb[b], fbits(X[h][b].z), o1); }
        oe[h] = rs32(o0, fb[7] * fbits(X[h][7].x)); oo[h] = rs32(o1, fb[7] * fbits(X[h][7].z));
      }
      float e = rs16(oe[0], oe[1]), o = rs16(oo[0], oo[1]);
      e += dpp<0x128>(e); o += dpp<0x128>(o);
      *(volatile LAS unsigned*)(L + L_OP + (lane >> 4) * 32 + (lane & 7) * 4) = pk_bf16(e * inv, o * inv);
      ctl[C_OSEQ] = dry ? 0u : 1u;
    }
  }
}

extern "C" void kernel_launch(void* const* d_in, const int* in_sizes, int n_in, void* d_out, int out_size, void* d_ws, size_t ws_size,
                              hipStream_t stream) {
  Args a;
  a.xA = (const unsigned char*)d_in[0]; a.kA = (const unsigned char*)d_in[1]; a.vA = (const unsigned char*)d_in[2];
  a.wqA = (const unsigned char*)d_in[3]; a.woA = (const unsigned char*)d_in[4];
  a.xB = (const unsigned char*)d_in[5]; a.kB = (const unsigned char*)d_in[6]; a.vB = (const unsigned char*)d_in[7];
  a.wqB = (const unsigned char*)d_in[8]; a.woB = (const unsigned char*)d_in[9];
  a.out = (unsigned char*)d_out; a.ws = (unsigned char*)d_ws;
  fused_decode<<<256, NTHR, 0, stream>>>(a);
}
```

```cpp
#include <hip/hip_runtime.h>
#include <stdint.h>

typedef unsigned v4u __attribute__((ext_vector_type(4)));
typedef unsigned v2u __attribute__((ext_vector_type(2)));
typedef float v4f __attribute__((ext_vector_type(4)));
typedef float v2f __attribute__((ext_vector_type(2)));
typedef short v4s __attribute__((ext_vector_type(4)));
typedef __bf16 bf16x2 __attribute__((ext_vector_type(2)));
typedef __bf16 bf16x8 __attribute__((ext_vector_type(8)));
#define LAS __attribute__((address_space(3)))
#define GAS __attribute__((address_space(1)))
#define DI __device__ __forceinline__

#define NSTAT 8
#define WINU 6
#define ROT 8
#define PSTAG 8
#define GOAT 1
#define POLLAT 2
#define DRY 1
#define DT1 50
#define DT2 110
#define FLAGK 2
#define NREP 16
#define WOAT 2
#define QSLP 8
#define WPACE 0
#define FMODE 1
#define QB 0
#define QM 8
#define TOUCH 1
#define AH2 3
#define PERM 0
#define LPRIO 3
#define WINL 8
#define LATE 40
constexpr int NWAVE = 12, NTHR = NWAVE * 64;
constexpr int NS = 4 + 16 + NSTAT;
constexpr int POOLC = (16 - NSTAT) * 8;
constexpr int NT = 48, TP = POOLC - NT;
static_assert(TP >= 0 && TP <= NT && NSTAT >= 4 && WINU >= 2 && WINU <= 12 && NSTAT <= 16, "");
#define PHASED 1
constexpr int NSP = PHASED ? NS : 5;
__host__ __device__ constexpr unsigned segk(unsigned n) { return n < 4 ? n : n < NSP ? n + 4 : 2 * n - 1; }
__host__ __device__ constexpr unsigned segv(unsigned n) { return n < 4 ? n + 4 : n + 1 < NSP ? n + NSP : 2 * n + 2; }
constexpr int WINE = (WINU + 1) & ~1;
constexpr unsigned SPIN_CAP = 1u << 18;
constexpr int NHOLD = 16, HOLD_B = 544, REC_B = 528;
constexpr int L_RK = 0, L_RV = 0  , L_CTL = 131072, L_Q = L_CTL + 1024, L_OP = L_Q + 1024, L_FP = L_CTL + 768  , L_QP = L_OP + 128, L_REC = L_QP + 9 * 256,
              L_HOLD = L_REC + 24 * REC_B, L_PB = L_HOLD + NHOLD * HOLD_B, PB_B = 144, L_END = L_PB + (NS - 4) * PB_B;
static_assert(L_END <= 163840, "");
enum { C_FULL = 0, C_END = 1, C_PHASE = 2, C_QRDY = 3, C_WANT = 4  , C_T1 = 5  , C_TAGA = 6, C_TAGB = 7, C_FREE = 8  , C_CNT = 16,
       C_QCNT = 19, C_HELD = 21, C_OSEQ = 22, C_T2 = 24  , C_TICK = 64, C_DESC = 128 };
static_assert(NT + 2 <= 64 && POOLC + 0 <= 64, "");
constexpr int W_X = 0, W_CTR = 4096, W_F1 = 16384  , W_F2 = W_F1 + 32768  , W_QA = W_F2 + 2 * NREP * 1024, W_QB = W_QA + 32768,
              W_P = W_QB + 32768, P_STR = 9216  , W_P1 = W_P + 96 * P_STR, P1_STR = 66560  ,
              W_Y = W_P1 + 32 * P1_STR, YS = 16384, W_ENDB = 256 + W_Y + 4 * 36 * YS  , W_STAMP = (W_ENDB + 4095) / 4096 * 4096;
constexpr int W_SAFE1 = W_P1 + 24 * P1_STR + 7424, W_SAFE2 = W_ENDB - 128;
static_assert(W_SAFE1 >> 21 == 1 && W_SAFE2 >> 21 == 2, "");
constexpr float SCALE = 0.088388347648318440f, L2E = 1.4426950408889634f;
constexpr int SC1 = 16, OOB = 0x70000000;

struct Args {
  const unsigned char *xA, *kA, *vA, *wqA, *woA, *xB, *kB, *vB, *wqB, *woB;
  unsigned char *out, *ws;
};

DI float dot2(unsigned a, unsigned b, float c) { return __builtin_amdgcn_fdot2_f32_bf16(__builtin_bit_cast(bf16x2, a), __builtin_bit_cast(bf16x2, b), c, false); }
template <int CTRL> DI float dpp(float x) { return __builtin_bit_cast(float, __builtin_amdgcn_mov_dpp(__builtin_bit_cast(int, x), CTRL, 0xf, 0xf, true)); }
DI unsigned pk_bf16(float lo, float hi) {
  unsigned r = __builtin_bit_cast(unsigned, bf16x2{(__bf16)lo, (__bf16)hi});
  asm("" : "+v"(r));
  return r;
}
DI float bf_lo(unsigned u) { return __builtin_bit_cast(float, u << 16); }
DI float bf_hi(unsigned u) { return __builtin_bit_cast(float, u & 0xffff0000u); }
DI float fbits(unsigned u) { return __builtin_bit_cast(float, u); }
DI unsigned ubits(float f) { return __builtin_bit_cast(unsigned, f); }
DI unsigned rfl(unsigned x) { return __builtin_amdgcn_readfirstlane(x); }
DI float ex2(float x) { return __builtin_amdgcn_exp2f(x); }
DI float mx2(float a, float b) { return __builtin_amdgcn_fmed3f(a, b, INFINITY); }
DI v4f mfma(v4u a, v4u b, v4f c) { return __builtin_amdgcn_mfma_f32_16x16x32_bf16(__builtin_bit_cast(bf16x8, a), __builtin_bit_cast(bf16x8, b), c, 0, 0, 0); }
DI v2u trd(LAS unsigned char* p) { return __builtin_bit_cast(v2u, __builtin_amdgcn_ds_read_tr16_b64_v4i16((LAS v4s*)p)); }
DI float rs32(float x, float y) { auto s = __builtin_amdgcn_permlane32_swap(ubits(x), ubits(y), false, false); return fbits(s[0]) + fbits(s[1]); }
DI float rs16(float x, float y) { auto s = __builtin_amdgcn_permlane16_swap(ubits(x), ubits(y), false, false); return fbits(s[0]) + fbits(s[1]); }
DI float xsum(float x) { x = rs16(x, x); return rs32(x, x); }
DI float xmax(float x) {
  auto s = __builtin_amdgcn_permlane16_swap(ubits(x), ubits(x), false, false); x = mx2(fbits(s[0]), fbits(s[1]));
  auto t = __builtin_amdgcn_permlane32_swap(ubits(x), ubits(x), false, false); return mx2(fbits(t[0]), fbits(t[1]));
}
DI float max8(float x) {
  asm("s_nop 1\n\tv_max_f32_dpp %0, %0, %0 quad_perm:[1,0,3,2] row_mask:0xf bank_mask:0xf\n\ts_nop 1\n\tv_max_f32_dpp %0, %0, %0 quad_perm:[2,3,0,1] row_mask:0xf bank_mask:0xf\n\t"
      "s_nop 1\n\tv_max_f32_dpp %0, %0, %0 row_half_mirror row_mask:0xf bank_mask:0xf" : "+v"(x));
  return x;
}
DI float max8to64(float x) {
  float t;
  asm("s_nop 1\n\tv_max_f32_dpp %0, %0, %0 row_ror:8 row_mask:0xf bank_mask:0xf\n\tv_mov_b32 %1, %0\n\ts_nop 1\n\tv_permlane16_swap_b32 %0, %1\n\tv_max_f32 %0, %0, %1\n\t"
      "v_mov_b32 %1, %0\n\ts_nop 1\n\tv_permlane32_swap_b32 %0, %1\n\tv_max_f32 %0, %0, %1" : "+v"(x), "=&v"(t));
  return x;
}
DI float max4to64(float x) {
  asm("s_nop 1\n\tv_max_f32_dpp %0, %0, %0 row_ror:4 row_mask:0xf bank_mask:0xf" : "+v"(x));
  return max8to64(x);
}
template <int LANE> DI unsigned bcast16(unsigned x) { return __builtin_amdgcn_mov_dpp(x, 0x150 + LANE, 0xf, 0xf, true); }
DI void lgkm0() { asm volatile("s_waitcnt lgkmcnt(0)" ::: "memory"); }
template <int N> DI void waitvm() { asm volatile("s_waitcnt vmcnt(%0)" ::"n"(N) : "memory"); }
DI __amdgpu_buffer_rsrc_t mkrs(const void* p, unsigned bytes) { return __builtin_amdgcn_make_buffer_rsrc((void*)p, 0, bytes, 0x00020000); }
DI void dma8(__amdgpu_buffer_rsrc_t rs, unsigned voff, unsigned soff, unsigned lds) {
  unsigned keep, s2;
  asm volatile("s_mov_b32 %0, m0\n\ts_mov_b32 m0, %5\n\ts_add_u32 %1, %4, 0x1000\n\ts_nop 2\n\t"
               "buffer_load_dwordx4 %2, %3, %4 offen nt lds\n\t"
               "buffer_load_dwordx4 %2, %3, %4 offen offset:1024 nt lds\n\t"
               "buffer_load_dwordx4 %2, %3, %4 offen offset:2048 nt lds\n\t"
               "buffer_load_dwordx4 %2, %3, %4 offen offset:3072 nt lds\n\t"
               "s_add_u32 m0, m0, 0x1000\n\ts_nop 0\n\t"
               "buffer_load_dwordx4 %2, %3, %1 offen nt lds\n\t"
               "buffer_load_dwordx4 %2, %3, %1 offen offset:1024 nt lds\n\t"
               "buffer_load_dwordx4 %2, %3, %1 offen offset:2048 nt lds\n\t"
               "buffer_load_dwordx4 %2, %3, %1 offen offset:3072 nt lds\n\t"
               "s_mov_b32 m0, %0"
               : "=&s"(keep), "=&s"(s2) : "v"(voff), "s"(rs), "s"(soff), "s"(lds) : "memory", "scc");
}
DI v4u gld(__amdgpu_buffer_rsrc_t rs, int off, int soff = 0) { return __builtin_amdgcn_raw_buffer_load_b128(rs, off, soff, SC1); }
DI unsigned gbad(v4u g, unsigned ta, unsigned tb, unsigned acc) { return __builtin_amdgcn_bitop3_b32(g.w, tb, __builtin_amdgcn_bitop3_b32(g.y, ta, acc, 0xBE), 0xBE); }
DI void gst(__amdgpu_buffer_rsrc_t rs, int off, unsigned a, unsigned b, unsigned ta, unsigned tb) { __builtin_amdgcn_raw_buffer_store_b128(v4u{a, ta, b, tb}, rs, off, 0, SC1); }

#define ST(i) do { } while (0)

typedef volatile LAS unsigned* ctl_t;
template <int SLP> DI bool wait_ge(ctl_t ctl, int idx, unsigned v) {
#pragma unroll 1
  for (unsigned s = 0; rfl(ctl[idx]) < v; ++s) {
    if (s > SPIN_CAP) return false;
    __builtin_amdgcn_s_sleep(SLP);
  }
  asm volatile("" ::: "memory");
  return true;
}
DI void lds_add(LAS unsigned char* L, int idx, unsigned v, int lane) {
  lgkm0();
  if (lane == 0) __hip_atomic_fetch_add((LAS unsigned*)(L + L_CTL) + idx, v, __ATOMIC_RELAXED, __HIP_MEMORY_SCOPE_WORKGROUP);
}
DI bool last_arriver(LAS unsigned char* L, int idx, unsigned n, int lane) {
  lgkm0();
  unsigned old = 0;
  if (lane == 0) old = __hip_atomic_fetch_add((LAS unsigned*)(L + L_CTL) + idx, 1u, __ATOMIC_RELAXED, __HIP_MEMORY_SCOPE_WORKGROUP);
  old = rfl(old);
  asm volatile("" ::: "memory");
  return old == n - 1;
}
DI void read_k(LAS unsigned char* L, unsigned off, int imm, v4u (&K)[4]) {
#pragma unroll
  for (int kk = 0; kk < 4; ++kk) K[kk] = *(const LAS v4u*)(L + L_RK + (off ^ (kk * 64)) + imm);
}
DI void read_v(LAS unsigned char* L, unsigned off, int imm, v4u (&V)[8]) {
#pragma unroll
  for (int blk = 0; blk < 8; ++blk) {
    LAS unsigned char* p = L + L_RV + (off ^ ((blk >> 1) * 64)) + (blk & 1) * 32 + imm;
    const v2u x = trd(p), y = trd(p + 4096);
    V[blk] = v4u{x.x, x.y, y.x, y.y};
  }
  __builtin_amdgcn_sched_barrier(0);
}
DI v4f qk(const v4u (&K)[4], const v4u (&q)[4]) {
  v4f d = {0.f, 0.f, 0.f, 0.f};
#pragma unroll
  for (int kk = 0; kk < 4; ++kk) d = mfma(K[kk], q[kk], d);
  return d;
}
DI v4f round2(v4f d) {
  const unsigned x01 = pk_bf16(d[0], d[1]), x23 = pk_bf16(d[2], d[3]);
  const unsigned b01 = pk_bf16(bf_lo(x01) * SCALE, bf_hi(x01) * SCALE), b23 = pk_bf16(bf_lo(x23) * SCALE, bf_hi(x23) * SCALE);
  return v4f{bf_lo(b01), bf_hi(b01), bf_lo(b23), bf_hi(b23)};
}
DI float sel(unsigned mask, float a, float b) { return fbits(__builtin_amdgcn_bitop3_b32(mask, ubits(a), ubits(b), 0xCA)); }
DI v4f sel4(unsigned mask, v4f a, v4f b) { return v4f{sel(mask, a[0], b[0]), sel(mask, a[1], b[1]), sel(mask, a[2], b[2]), sel(mask, a[3], b[3])}; }
DI v2f sel2(unsigned mask, v2f a, v2f b) { return v2f{sel(mask, a[0], b[0]), sel(mask, a[1], b[1])}; }
DI unsigned bitmask(int x, int bit) { unsigned m = (unsigned)((x << (31 - bit)) >> 31); asm("" : "+v"(m)); return m; }
DI float max4(v4f d) { return mx2(mx2(d[0], d[1]), mx2(d[2], d[3])); }
DI float sum4(v4f d) { return (d[0] + d[1]) + (d[2] + d[3]); }
DI v4f expo(v4f s, float m2) { return v4f{ex2(fmaf(s[0], L2E, -m2)), ex2(fmaf(s[1], L2E, -m2)), ex2(fmaf(s[2], L2E, -m2)), ex2(fmaf(s[3], L2E, -m2))}; }
DI v4u pack_p(v4f p0, v4f p1) { return v4u{pk_bf16(p0[0], p0[1]), pk_bf16(p0[2], p0[3]), pk_bf16(p1[0], p1[1]), pk_bf16(p1[2], p1[3])}; }

DI unsigned now32() { return (unsigned)__builtin_amdgcn_s_memrealtime(); }
DI void wait_until(unsigned t0, unsigned until) {
#pragma unroll 1
  for (unsigned sp = 0; sp < 64 && now32() - t0 < until; ++sp) { }
}
DI void hint_set(__amdgpu_buffer_rsrc_t ws, int off, unsigned t, unsigned tag) {
  asm volatile("buffer_atomic_swap_x2 %0, %1, %2, 0 offen" ::"v"(v2u{t, tag}), "v"(off), "s"(ws) : "memory");
}
template <bool TWO> DI unsigned hint_time(__amdgpu_buffer_rsrc_t ws, int off, int off2, unsigned tag, unsigned t0) {
  unsigned rel = 0;
  waitvm<0>();
#define HP1(A0, A1, o) asm volatile("v_mov_b32 v16" #A0 ", 0\n\tv_mov_b32 v16" #A1 ", 0\n\tbuffer_atomic_add_x2 v[16" #A0 ":16" #A1 "], %0, %1, 0 offen sc0" \
                                    ::"v"(o), "s"(ws) : "memory", "v16" #A0, "v16" #A1)
#define HC1(A0, A1, o, N) { unsigned t_, g_; asm volatile("s_waitcnt vmcnt(%2)\n\tv_mov_b32 %0, v16" #A0 "\n\tv_mov_b32 %1, v16" #A1 : "=v"(t_), "=v"(g_) : "n"(N) : "memory", "v16" #A0, "v16" #A1); \
                            const bool ok_ = g_ == tag; rel = ok_ ? max(rel, t_ - t0) : rel; o = ok_ ? OOB : o; }
  HP1(0, 1, off);
  if (TWO) HP1(2, 3, off2);
  __builtin_amdgcn_s_sleep(PSTAG);
#pragma unroll 1
  for (unsigned sp = 0; sp < 4096; ++sp) {
    HP1(4, 5, off);
    if (TWO) HP1(6, 7, off2);
    HC1(0, 1, off, TWO ? 3 : 1)
    if (TWO) HC1(2, 3, off2, 2)
    if (__all(off == OOB && off2 == OOB)) break;
    HP1(0, 1, off);
    if (TWO) HP1(2, 3, off2);
    HC1(4, 5, off, TWO ? 3 : 1)
    if (TWO) HC1(6, 7, off2, 2)
    if (__all(off == OOB && off2 == OOB)) break;
  }
#undef HP1
#undef HC1
  return rfl((unsigned)max8to64(max8((float)rel)));
}
DI bool park_rec(LAS unsigned char* L, ctl_t ctl, int slot, int adr, int dstr, int mladr, int gi, int lane, v2f o, float m, float l) {
  LAS unsigned char* h = L + L_HOLD + slot * HOLD_B;
  *(LAS v2f*)(h + 32 + gi * 8) = o;
  if (lane == 0) { *(LAS v4u*)h = v4u{(unsigned)adr, (unsigned)dstr, (unsigned)mladr, 0u}; *(LAS v2f*)(h + 16) = v2f{m, l}; }
  lgkm0();
  if (lane == 0) __hip_atomic_fetch_or((LAS unsigned*)(L + L_CTL) + C_HELD, 1u << slot, __ATOMIC_RELAXED, __HIP_MEMORY_SCOPE_WORKGROUP);
  lgkm0();
  return rfl(ctl[C_PHASE]) >= GOAT;
}
template <int NR> DI v2f merge_rec(LAS unsigned char* rec, int lane, float& Mo, float& Lo) {
  v2f av[NR];
  const v2f ml = *(LAS v2f*)(rec + (lane & (NR - 1)) * REC_B);
#pragma unroll
  for (int w = 0; w < NR; ++w) av[w] = *(LAS v2f*)(rec + w * REC_B + 16 + lane * 8);
  float M = ml.x; M = mx2(M, dpp<0xB1>(M)); M = mx2(M, dpp<0x4E>(M));
  if (NR == 8) M = mx2(M, dpp<0x141>(M));
  const float f = ex2(ml.x - M);
  float Ls = f * ml.y; Ls += dpp<0xB1>(Ls); Ls += dpp<0x4E>(Ls);
  if (NR == 8) Ls += dpp<0x141>(Ls);
  float a0 = 0.f, a1 = 0.f;
#pragma unroll
  for (int w = 0; w < NR; ++w) { const float fw = fbits(__builtin_amdgcn_readlane(ubits(f), w)); a0 = fmaf(fw, av[w].x, a0); a1 = fmaf(fw, av[w].y, a1); }
  Mo = M; Lo = Ls;
  return v2f{a0, a1};
}

__global__ void __launch_bounds__(NTHR) fused_decode(Args a) {
  __shared__ __attribute__((aligned(16))) unsigned char smem[L_END];
  LAS unsigned char* L = (LAS unsigned char*)smem;
  ctl_t ctl = (ctl_t)(L + L_CTL);
  asm volatile("" ::"s"(a.xA), "s"(a.kA), "s"(a.vA), "s"(a.wqA), "s"(a.woA), "s"(a.xB), "s"(a.kB), "s"(a.vB), "s"(a.wqB), "s"(a.woB), "s"(a.out), "s"(a.ws));
  const int wg = blockIdx.x, g = wg >> 3, j = wg & 7;
  const int lane = threadIdx.x & 63, w = rfl(threadIdx.x >> 6);
  const __amdgpu_buffer_rsrc_t ws = mkrs(a.ws, W_ENDB);
  ST(w == 1 ? 0 : 31);

  if (w == 0) {
    __builtin_amdgcn_s_waitcnt(0);
    const unsigned lb = (unsigned)(uintptr_t)L;
    const unsigned voff = (lane * 16) ^ ((lane >> 4) << 6);
    const unsigned offA = g * (2u << 20) + (3u << 19) + j * 16384;
    const __amdgpu_buffer_rsrc_t rk = mkrs(a.kA, 64u << 20);
    dma8(rk, voff, offA, lb + L_RK); dma8(rk, voff, offA + 8192, lb + L_RK + 8192);
    asm volatile("s_barrier" ::: "memory");
    __builtin_amdgcn_s_setprio(LPRIO);
    int u = 4, pub = 0;
    unsigned sg = 1;
#define TB()
#define TE(x)
#define LWAIT(idx, v, out, stl) _Pragma("unroll 1") for (unsigned sp = 0;; ++sp) { \
      out = rfl(ctl[idx]); if (out + 1u > (v)) break; \
      if (sp > SPIN_CAP) return; \
      TB(); if (sp == 6) { waitvm<0>(); pub = u; ctl[C_FULL] = pub; } __builtin_amdgcn_s_sleep(1); TE(stl); }
#define HALF(rs, so, ld) do { waitvm<4 * WINU>(); if (u - WINE > pub) { pub = u - WINE; ctl[C_FULL] = pub; } dma8(rs, voff, so, ld); u += 2; } while (0)
#define SEG(rs, so, stl) do { unsigned fr_; LWAIT(C_FREE + (sg & 7), sg < 8 ? 0u : sg < 16 ? 2u : sg - 7, fr_, stl) \
      const unsigned ld_ = lb + (sg & 7) * 16384; HALF(rs, so, ld_); HALF(rs, (so) + 8192, ld_ + 8192); ++sg; } while (0)
    const unsigned jr = j + g * ROT;
#pragma unroll 1
    for (int run = 0; run < 4; ++run) {
      const __amdgpu_buffer_rsrc_t rs = mkrs(run == 0 ? a.kA : run == 1 ? a.vA : run == 2 ? a.kB : a.vB, run < 2 ? 64u << 20 : 128u << 20);
      const int cnt = run < 2 ? 4 : NS - 2 - run;
#pragma unroll 1
      for (int i = run == 0; i < cnt; ++i) {
        const unsigned so = run < 2 ? offA + i * 131072 : (g + (i & 16) * 2) * (2u << 20) + ((i * 8 + jr) & 127) * 16384;
        if (sg + 4 == 2 * NS) ctl[C_WANT] = 1;
        if (TP <= 8 && sg + 2 == 2 * NS) ctl[C_WANT] = 2;
        SEG(rs, so, stallF);
      }
    }
    {
      const __amdgpu_buffer_rsrc_t rkb = mkrs(a.kB, 128u << 20), rvb = mkrs(a.vB, 128u << 20);
      const unsigned b1 = (32 + g) * (2u << 20);
      unsigned sv = b1 + (((NS - 5) * 8 + jr) & 127) * 16384;
      unsigned k = 0, cid = 0, dsc = 0;
      bool second = false;
#pragma unroll 1
      for (unsigned n = NS;; ++n) {
        unsigned t = 0;
        if (!second) { LWAIT(C_TICK + k, 0u, t, stallT) }
        const bool last = t >= (unsigned)NT;
        unsigned sk = 0;
        if (!last) {
          if (second) { ++cid; dsc |= 1u << 10; second = false; }
          else {
            second = t < (unsigned)TP;
            cid = second ? 2 * t : TP + t;
            dsc = (8 + t) | ((NS + k) & 3) << 8 | (second ? 0u : 1u << 10) | (4 + k) << 12;
            ++k;
          }
          ctl[C_DESC + n - NS] = dsc;
          sk = b1 + ((NSTAT * 8 + cid + g * ROT) & 127) * 16384;
          SEG(rkb, sk, stallF2);
          ctl[C_WANT] = second || cid + 16 < 2 * TP ? k + 1 : k + 2;
        } else {
          ctl[C_END] = n; ctl[C_PHASE] = 1; ++sg; ST(8);
        }
        SEG(rvb, sv, stallF2);
        if (last) break;
        sv = sk;
      }
    }
    ctl[C_PHASE] = 2;
    ST(9);
#define DRAIN(k_) if (WINU + 2 > k_) { waitvm<4 * k_>(); const int p_ = u - k_ + (k_ < 4 ? 4 : 0); if (p_ > pub) { pub = p_; ctl[C_FULL] = pub; } \
      if (k_ == 0) ctl[C_PHASE] = 3; }
    DRAIN(12) DRAIN(10) DRAIN(8) DRAIN(6) DRAIN(4) DRAIN(2) DRAIN(0)
#undef DRAIN
    ST(10);
    return;
  }
  const unsigned tstart = now32();
  const int cb = (wg & ~15) + ((wg & 7) << 1) + ((wg >> 3) & 1);
  const int side = j >> 2, prod = g * 4 + (j & 3);
  unsigned ta, tb;
  int jb, jend;
  if (w == 1) {
    const v2u ep = __builtin_amdgcn_raw_buffer_load_b64(ws, W_X, 0, SC1);
    if (cb >= 128 && (cb & 3) == 0 && lane == 0) __builtin_amdgcn_raw_buffer_store_b32(0u, ws, W_CTR + ((cb - 128) >> 2) * 256, 0, SC1);
    ctl[lane] = lane == C_END ? 0x7fffffffu : 0u;
    ctl[lane + 64] = 0xffffffffu; ctl[lane + 128] = 0xffffffffu; ctl[lane + 192] = 0xffffffffu;
    asm volatile("s_waitcnt lgkmcnt(0)\n\ts_barrier" ::: "memory");
    ta = rfl(ep.x) + 1u; tb = rfl(ep.y) + 1u;
    ta += ta == 0u; tb += tb == 0u;
    waitvm<0>();
    if (!wait_ge<1>(ctl, C_QCNT, 9)) return;
    ST(1);
    if (lane < 16) {
      v4f s = {0.f, 0.f, 0.f, 0.f};
#pragma unroll
      for (int i = 0; i < 9; ++i) s += *(LAS v4f*)(L + L_QP + i * 256 + lane * 16);
      const int b = lane >> 3, quad = lane & 7;
      const int off = cb < 128 ? W_QA + ((b * 16 + (cb >> 3)) * 64 + (cb & 7) * 8 + quad) * 16 : W_QB + (((cb - 128) >> 2) * 64 + b * 32 + (cb & 3) * 8 + quad) * 16;
      gst(ws, off, pk_bf16(s.x, s.y), pk_bf16(s.z, s.w), ta, tb);
    }
    v4u qa, qb;
#pragma unroll 1
    for (unsigned sp = 0;; ++sp) {
      __builtin_amdgcn_s_sleep(QSLP);
      qa = gld(ws, W_QA + g * 1024 + lane * 16);
      qb = gld(ws, W_QB + g * 1024 + lane * 16);
      if (__all(gbad(qb, ta, tb, gbad(qa, ta, tb, 0u)) == 0u)) break;
      if (sp > SPIN_CAP) return;
    }
    *(LAS v2u*)(L + L_Q + lane * 8) = v2u{qa.x, qa.z};
    *(LAS v2u*)(L + L_Q + 512 + lane * 8) = v2u{qb.x, qb.z};
    ctl[C_TAGA] = ta; ctl[C_TAGB] = tb;
    lgkm0();
    ctl[C_QRDY] = 1;
    ST(2);
    {
      int c0 = 0, c1 = 0, mine = 0, pend = 0;
      bool may_quit = QB > 0;
#pragma unroll 1
      for (int k = 0; k <= NT; ++k) {
        if (!wait_ge<2>(ctl, C_WANT, k + 1)) return;
        const int dd = c1 - c0;
        const bool quit = may_quit && mine > 0 && (POOLC - c1) * mine - dd < QB * (dd - QM * mine);
        unsigned t = 0;
        if (lane == 0) t = __hip_atomic_fetch_add((GAS unsigned*)(a.ws + W_CTR + g * 256), quit ? 0x10000u : 1u, __ATOMIC_RELAXED, __HIP_MEMORY_SCOPE_AGENT);
        t = rfl(t);
        if (quit) {
          if (t >> 16 >= 5) { may_quit = false; --k; continue; }
          t = NT;
        }
        t &= 0xffffu;
        ctl[C_TICK + k] = t;
        if (t >= (unsigned)NT) break;
        c1 = t < (unsigned)TP ? 2 * t : TP + t;
        if (k == 0) c0 = c1;
        mine += pend;
        pend = t < (unsigned)TP ? 2 : 1;
      }
    }
    if (!wait_ge<1>(ctl, C_T1, 1)) return;
    hint_set(ws, lane < NREP ? W_F2 + (side * NREP + lane) * 1024 + prod * 8 : OOB, ctl[C_T1], ta);
    if ((prod >> 1) < 36) {
      const int ho = W_F2 + (side * NREP + prod % NREP) * 1024 + lane * 8;
      ctl[C_T2] = (hint_time<true>(ws, ho, ho + 512, ta, tstart) + tstart + DT2) | 1u;
    }
    return;
  } else if (w == 2) {
    asm volatile("s_barrier" ::: "memory");
    if (!wait_ge<16>(ctl, C_QRDY, 1)) return;
    ta = rfl(ctl[C_TAGA]); tb = rfl(ctl[C_TAGB]);
    jb = DRY ? 0 : 2; jend = 3;
  } else {
    {
      const int qw = w - 3;
      const int r0 = qw * 64 + (lane >> 2) * 4;
      const unsigned char* xp = (cb < 128 ? a.xA : a.xB) + r0 * 2;
      const unsigned char* wp = (cb < 128 ? a.wqA : a.wqB) + (size_t)r0 * 8192 + (cb & 127) * 64 + (lane & 3) * 16;
      v4u Wq[4];
#pragma unroll
      for (int i = 0; i < 4; ++i) Wq[i] = __builtin_nontemporal_load((const GAS v4u*)(wp + i * 8192));
      const v2u X0 = *(const GAS v2u*)xp, X1 = *(const GAS v2u*)(xp + 1152);
      asm volatile("s_barrier" ::: "memory");
      float ac[2][8];
#pragma unroll
      for (int k = 0; k < 8; ++k) { ac[0][k] = 0.f; ac[1][k] = 0.f; }
#pragma unroll
      for (int s = 0; s < 2; ++s)
#pragma unroll
        for (int d = 0; d < 4; ++d) {
          const unsigned lo = __builtin_amdgcn_perm(Wq[2 * s + 1][d], Wq[2 * s][d], 0x05040100u), hi = __builtin_amdgcn_perm(Wq[2 * s + 1][d], Wq[2 * s][d], 0x07060302u);
          ac[0][2 * d] = dot2(lo, X0[s], ac[0][2 * d]); ac[0][2 * d + 1] = dot2(hi, X0[s], ac[0][2 * d + 1]);
          ac[1][2 * d] = dot2(lo, X1[s], ac[1][2 * d]); ac[1][2 * d + 1] = dot2(hi, X1[s], ac[1][2 * d + 1]);
        }
      float b8[8], c4[4];
#pragma unroll
      for (int k = 0; k < 8; ++k) b8[k] = rs32(ac[0][k], ac[1][k]);
#pragma unroll
      for (int k = 0; k < 4; ++k) { float t = rs16(b8[k], b8[k + 4]); t += dpp<0x128>(t); t += dpp<0x124>(t); c4[k] = t; }
      if ((lane & 12) == 0) *(LAS v4f*)(L + L_QP + qw * 256 + (lane >> 5) * 128 + (lane & 3) * 32 + ((lane >> 4) & 1) * 16) = v4f{c4[0], c4[1], c4[2], c4[3]};
      lds_add(L, C_QCNT, 1, lane);
    }
    const int r = lane >> 4, c = lane & 15;
    const unsigned kof = c * 256 + r * 16 + (c & 3) * 64;
    const unsigned vof = (4 * r + (c >> 2)) * 256 + (c >> 2) * 64 + (c & 3) * 8;
    if (w >= 4) {
      const unsigned aw = w - 4, off = aw * 8192, an = aw >> 1, ah = 2 * (aw & 1) + 2;
      v4u K0[4], K1[4], V[8];
      if (!wait_ge<1>(ctl, C_FULL, 4 * an + ah)) return;
      read_k(L, off + kof, 0, K0); read_k(L, off + kof, 4096, K1);
      lds_add(L, C_FREE + an, 1, lane);
      if (!wait_ge<1>(ctl, C_FULL, 4 * an + 16 + ah)) return;
      read_v(L, 65536 + off + vof, 0, V);
      lds_add(L, C_FREE + 4 + an, 1, lane);
      if (!wait_ge<1>(ctl, C_QRDY, 1)) return;
      const LAS v4u* qp = (const LAS v4u*)(L + L_Q) + r + (c >> 3) * 16;
      const v4u q[4] = {qp[0], qp[4], qp[8], qp[12]};
      v4f s0 = round2(qk(K0, q)), s1 = round2(qk(K1, q));
      const float M = xmax(mx2(max4(s0), max4(s1))) * L2E;
      s0 = expo(s0, M); s1 = expo(s1, M);
      const v4u P = pack_p(s0, s1);
      const float Ls = xsum(sum4(s0) + sum4(s1));
      v4f acc[8];
#pragma unroll
      for (int blk = 0; blk < 8; ++blk) acc[blk] = mfma(V[blk], P, v4f{0.f, 0.f, 0.f, 0.f});
#pragma unroll
      for (int k = 0; k < 4; ++k) acc[k] = sel4(bitmask(c, 2), acc[k + 4], acc[k]);
#pragma unroll
      for (int k = 0; k < 2; ++k) acc[k] = sel4(bitmask(c, 1), acc[k + 2], acc[k]);
      acc[0] = sel4(bitmask(c, 0), acc[1], acc[0]);
      LAS unsigned char* rec = L + L_REC + ((c >> 3) * 8 + aw) * REC_B;
      *(LAS v4f*)(rec + 16 + ((c & 7) * 16 + r * 4) * 4) = acc[0];
      if ((lane & 0x37) == 0) *(LAS v2f*)rec = v2f{M, Ls};
      if (last_arriver(L, C_CNT, 8, lane)) {
#pragma unroll 1
        for (int e = 0; e < 2; ++e) {
          float Mo, Lo;
          const v2f o = merge_rec<8>(L + L_REC + e * 8 * REC_B, lane, Mo, Lo);
          const int base = W_P + (g * 2 + e) * P_STR;
          if (park_rec(L, ctl, e, base + j * 128, 1024, base + 8192 + j * 16, lane, lane, o, Mo, Lo)) {
            const unsigned ta_ = ctl[C_TAGA], tb_ = ctl[C_TAGB];
            gst(ws, base + j * 128 + (lane >> 3) * 1024 + (lane & 7) * 16, ubits(o.x), ubits(o.y), ta_, tb_);
            if (lane == 0) gst(ws, base + 8192 + j * 16, ubits(Mo), ubits(Lo), ta_, tb_);
          }
        }
        ST(4);
      }
    } else if (!wait_ge<8>(ctl, C_QRDY, 1)) return;
    ta = rfl(ctl[C_TAGA]); tb = rfl(ctl[C_TAGB]);
    if (w >= 8) {
      const int cw = w - 8;
      float m = -INFINITY, l = 0.f;
      v4f acc[8];
      bool fresh = true;
      const unsigned mk0 = bitmask(c, 0), mk1 = bitmask(c, 1), mk2 = bitmask(c, 2), mk3 = bitmask(c, 3);
      const int gid = (c >> 1) * 8 + 2 * r + (c & 1);
      bool freshv = true;
      int mode = PHASED ? 1 : 3;
#pragma unroll 1
      for (unsigned n = 4 + cw;;) {
        bool stop = false;
        unsigned dsc = cw << 8;
        const bool dyn = n >= NS;
        if (dyn) {
#pragma unroll 1
          for (unsigned sp = 0;; ++sp) {
            const unsigned dd = ctl[C_DESC + n - NS], en = ctl[C_END];
            dsc = rfl(dd);
            if (dsc != 0xffffffffu) break;
            if (n >= rfl(en) || sp > SPIN_CAP) { stop = true; break; }
            __builtin_amdgcn_s_sleep(2);
          }
          if (stop) break;
          if ((dsc >> 8 & 3) != (unsigned)cw) { ++n; continue; }
        }
        const bool pubn = dyn ? (dsc >> 10 & 1) : (n >= NS - 4 || (n & ~3u) == 16);
        const int base = n >= 20 ? W_P1 + g * P1_STR : W_P + (64 + g) * P_STR, dstr = n >= 20 ? 8192 : 1024, rec = dyn ? dsc & 255 : j, slot = dyn ? dsc >> 12 : 2 + (n >= 20);
        const int gi = dyn ? gid : lane;
        const unsigned sk = segk(n), sv = segv(n);
        int ad = base + rec * 128 + (gi >> 3) * dstr + (gi & 7) * 16, am = lane == 0 ? base + 8 * dstr + rec * 16 : OOB;
        unsigned ka = (sk & 7) * 16384 + kof, va = (sv & 7) * 16384 + vof;
        LAS unsigned char* pb = L + L_PB + (dyn ? 0 : n - 4) * PB_B;
        v4u P0, P1, st;
        float al, Ls, mrec;
        const bool first = freshv;
        if ((mode & 2) && freshv) {
#pragma unroll
          for (int blk = 0; blk < 8; ++blk) acc[blk] = v4f{0.f, 0.f, 0.f, 0.f};
          freshv = false;
        }
        if (mode & 1) {
          const LAS v4u* qp = (const LAS v4u*)(L + L_Q) + r + (n < 20 ? 32 : 48);
          v4u q[4] = {qp[0], qp[4], qp[8], qp[12]};
          if (fresh) { m = -INFINITY; l = 0.f; }
          fresh = pubn;
          asm volatile("" : "+v"(ad), "+v"(am), "+v"(ka), "+v"(va), "+v"(q[0]), "+v"(q[1]), "+v"(q[2]), "+v"(q[3]));
          if (!wait_ge<1>(ctl, C_FULL, 4 * sk + 4)) return;
          v4f s[4];
          v4u K[4][4];
#pragma unroll
          for (int uu = 0; uu < 4; ++uu) read_k(L, ka, uu * 4096, K[uu]);
          __builtin_amdgcn_sched_barrier(0);
#pragma unroll
          for (int uu = 0; uu < 4; ++uu) s[uu] = qk(K[uu], q);
          __builtin_amdgcn_sched_barrier(0);
          lgkm0();
          ctl[C_FREE + (sk & 7)] = sk + 1;
          v4f sc = round2(sel4(mk3, sel4(mk2, s[3], s[2]), sel4(mk2, s[1], s[0])));
          const float mn = mx2(max4to64(max4(sc)) * L2E, m);
          st = *(volatile LAS v4u*)(L + L_CTL);
          sc = expo(sc, mn);
          const unsigned px = pk_bf16(sc[0], sc[1]), py = pk_bf16(sc[2], sc[3]);
          P0 = v4u{bcast16<0>(px), bcast16<0>(py), bcast16<4>(px), bcast16<4>(py)}; P1 = v4u{bcast16<8>(px), bcast16<8>(py), bcast16<12>(px), bcast16<12>(py)};
          al = ex2(m - mn);
          mrec = m = mn;
          l = fmaf(l, al, sum4(sc));
          Ls = l + dpp<0x124>(l);
          Ls += dpp<0x128>(Ls);
          Ls = xsum(Ls);
          if (mode == 1) {
            *(LAS v4u*)(pb + r * 16) = P0; *(LAS v4u*)(pb + 64 + r * 16) = P1; *(LAS v4f*)(pb + 128) = v4f{al, Ls, mn, 0.f};
            n += 4;
            if (n >= NS) { n = 4 + cw; mode = 2; }
            continue;
          }
        } else {
          P0 = *(LAS v4u*)(pb + r * 16); P1 = *(LAS v4u*)(pb + 64 + r * 16);
          const v4f t = *(LAS v4f*)(pb + 128);
          al = t.x; Ls = t.y; mrec = t.z;
          st = *(volatile LAS v4u*)(L + L_CTL);
        }
        if (!first) {
#pragma unroll
          for (int blk = 0; blk < 8; ++blk) acc[blk] *= al;
        }
        asm volatile("" : "+v"(P0), "+v"(P1), "+v"(Ls), "+v"(acc[0]), "+v"(acc[1]), "+v"(acc[2]), "+v"(acc[3]), "+v"(acc[4]), "+v"(acc[5]), "+v"(acc[6]), "+v"(acc[7]));
        v4u V[8];
#define WAITV(need) _Pragma("unroll 1") for (unsigned sp = 0; rfl(st.x) < (need); ++sp) { if (sp > SPIN_CAP) return; if (sp) __builtin_amdgcn_s_sleep(1); st = *(volatile LAS v4u*)(L + L_CTL); } \
        asm volatile("" ::: "memory");
        WAITV(4 * sv + 2)
        read_v(L, va, 0, V);
#pragma unroll
        for (int blk = 0; blk < 8; ++blk) acc[blk] = mfma(V[blk], P0, acc[blk]);
        WAITV(4 * sv + 4)
#undef WAITV
        const unsigned tl = now32(), nend = rfl(st.y);
        read_v(L, va, 8192, V);
        hint_set(ws, lane == 0 && n + 1 == nend ? W_F1 + wg * 128 : OOB, tl, ta);
#pragma unroll
        for (int blk = 0; blk < 8; ++blk) acc[blk] = mfma(V[blk], P1, acc[blk]);
        if (pubn) {
          v2f o[8];
#pragma unroll
          for (int k = 0; k < 8; ++k) o[k] = sel2(mk0, v2f{acc[k][2], acc[k][3]}, v2f{acc[k][0], acc[k][1]});
#pragma unroll
          for (int k = 0; k < 4; ++k) o[k] = sel2(mk3, o[k + 4], o[k]);
#pragma unroll
          for (int k = 0; k < 2; ++k) o[k] = sel2(mk2, o[k + 2], o[k]);
          o[0] = sel2(mk1, o[1], o[0]);
          bool now = dyn && (slot >= NHOLD || rfl(st.z) >= GOAT);
          if (!now) {
            bool emit = true;
            if (!dyn) {
              LAS unsigned char* rp = L + L_REC + (8 + slot * 4) * REC_B;
              *(LAS v2f*)(rp + cw * REC_B + 16 + gid * 8) = o[0];
              if (lane == 0) *(LAS v2f*)(rp + cw * REC_B) = v2f{mrec, Ls};
              emit = last_arriver(L, C_CNT + slot - 1, 4, lane);
              if (emit) o[0] = merge_rec<4>(rp, lane, mrec, Ls);
            }
            if (emit) now = park_rec(L, ctl, slot, base + rec * 128, dstr, base + 8 * dstr + rec * 16, gi, lane, o[0], mrec, Ls);
            if (!now) { ad = OOB; am = OOB; }
          }
          gst(ws, ad, ubits(o[0].x), ubits(o[0].y), ta, tb);
          gst(ws, am, ubits(mrec), ubits(Ls), ta, tb);
          ST(n >= 20 ? 7 : 5);
          freshv = true;
        }
        lgkm0();
        ctl[C_FREE + (sv & 7)] = sv + 1;
        if (n + 4 < NS) n += 4;
        else { n = dyn ? n + 1 : NS; mode = 3; }
      }
      jb = 3; jend = 4;
    } else {
      const int k = w - 3;
      v4u Wf[2][4];
      v2u Wr[2][8];
      const unsigned char* pw = (side ? a.woB : a.woA) + (size_t)((side ? g : 2 * (g & 15) + (g >> 4)) * 128 + (j & 3) * 32 + 8 * r) * 1152 + c * 8;
      const int yreal = W_Y + ((side ? 3 - (r & 1) : (r & 1)) * 36 + k * 4 + (c >> 2)) * YS + prod * 128 + (2 * (c & 3) + (r >> 1)) * 16;
      const unsigned mh = bitmask(r, 1);
#define WO_LOAD() _Pragma("unroll") for (int b = 0; b < 2; ++b) _Pragma("unroll") for (int i = 0; i < 8; ++i) \
        Wr[b][i] = __builtin_nontemporal_load((const GAS v2u*)(pw + i * 1152 + (b && k < 4 ? k + 5 : k) * 128));
#define WO_PACK() _Pragma("unroll") for (int b = 0; b < 2; ++b) { _Pragma("unroll") for (int d = 0; d < 4; ++d) { \
          Wf[b][0][d] = __builtin_amdgcn_perm(Wr[b][2 * d + 1].x, Wr[b][2 * d].x, 0x05040100u); Wf[b][1][d] = __builtin_amdgcn_perm(Wr[b][2 * d + 1].x, Wr[b][2 * d].x, 0x07060302u); \
          Wf[b][2][d] = __builtin_amdgcn_perm(Wr[b][2 * d + 1].y, Wr[b][2 * d].y, 0x05040100u); Wf[b][3][d] = __builtin_amdgcn_perm(Wr[b][2 * d + 1].y, Wr[b][2 * d].y, 0x07060302u); } \
        asm volatile("" : "+v"(Wf[b][0]), "+v"(Wf[b][1]), "+v"(Wf[b][2]), "+v"(Wf[b][3])); }
      if (!WOAT) { WO_LOAD() WO_PACK() }
#pragma unroll 1
      for (int pass = DRY && !WOAT ? 0 : 1; pass < 2; ++pass) {
        if (!wait_ge<16>(ctl, C_WANT, pass)) return;
        if (TOUCH && k == 0) {
          const unsigned t0 = __builtin_nontemporal_load((const GAS unsigned*)(pw + 7 * 1152 - c * 8)), t1 = __builtin_amdgcn_raw_buffer_load_b32(ws, lane & 1 ? W_SAFE1 : W_SAFE2, 0, SC1);
          asm volatile("" ::"v"(t0), "v"(t1));
        }
        if (!wait_ge<2>(ctl, C_PHASE, pass * GOAT)) return;
        const unsigned held = pass ? rfl(ctl[C_HELD]) : 31u;
        asm volatile("" ::: "memory");
#pragma unroll 1
        for (int slot = k; slot < NHOLD; slot += 5) {
          if (!(held >> slot & 1)) continue;
          LAS unsigned char* h = L + L_HOLD + slot * HOLD_B;
          const v4u hd = *(LAS v4u*)h;
          const v2u ml = *(LAS v2u*)(h + 16), o = *(LAS v2u*)(h + 32 + lane * 8);
          gst(ws, pass ? hd.x + (lane >> 3) * hd.y + (lane & 7) * 16 : OOB, o.x, o.y, ta, tb);
          gst(ws, pass && lane == 0 ? hd.z : OOB, ml.x, ml.y, ta, tb);
        }
        if (w == 4) ST(14);
        if (WOAT) {
          if (!wait_ge<2>(ctl, C_PHASE, WOAT)) return;
          WO_LOAD() WO_PACK()
        }
        int y0 = pass ? yreal : OOB, y1 = pass && k < 4 ? yreal + 20 * YS : OOB;
        asm volatile("" : "+v"(y0), "+v"(y1));
        v4u oa;
#pragma unroll 1
        for (unsigned sp = 0;; ++sp) {
          const unsigned fl = ctl[C_OSEQ];
          oa = *(volatile LAS v4u*)(L + L_OP + ((c >> 2) & 1) * 64 + r * 16);
          if (rfl(fl) >= (unsigned)pass) break;
          if (sp > SPIN_CAP) return;
          __builtin_amdgcn_s_sleep(1);
        }
#pragma unroll
        for (int b = 0; b < 2; ++b) {
          v4f d[4];
#pragma unroll
          for (int f = 0; f < 4; ++f) d[f] = mfma(oa, Wf[b][f], v4f{0.f, 0.f, 0.f, 0.f});
          gst(ws, b ? y1 : y0, ubits(sel(mh, d[2][0], d[0][0])), ubits(sel(mh, d[3][0], d[1][0])), ta, tb);
        }
      }
      if (w == 4 && side) ST(12);
      return;
    }
  }
#pragma unroll 1
  for (; jb < jend; ++jb) {
    const bool dry = jb < 2;
    unsigned sp = 0;
    if (jb & 1) {
      const int qt = w & 3, c = side * 2 + (prod & 1), rho = prod >> 1;
      if (rho >= 36 && !dry) return;
      int voff = dry ? OOB : W_Y + (c * 36 + rho) * YS + qt * 4096 + lane * 16;
      int oo = lane < 16 && !dry ? ((c & 1) * 1152 + (c >> 1) * 576 + rho * 16 + lane) * 2 : OOB;
      asm volatile("" : "+v"(voff), "+v"(oo));
      if (!wait_ge<4>(ctl, C_T1, dry ? 0 : 1)) return;
      if (!wait_ge<1>(ctl, C_T2, dry ? 0 : 1)) return;
      const unsigned until = rfl(ctl[C_T2]) - tstart;
      wait_until(tstart, dry ? 0u : until);
      v4u X[4];
#pragma unroll 1
      for (;; ++sp) {
#pragma unroll
        for (int i = 0; i < 4; ++i) X[i] = gld(ws, voff, i * (FMODE ? 1024 : 4096));
        unsigned bad = 0;
#pragma unroll
        for (int i = 0; i < 4; ++i) bad = gbad(X[i], ta, tb, bad);
        if (__all(bad == 0u) || dry) break;
        if (sp > SPIN_CAP) return;
        __builtin_amdgcn_s_sleep(4);
      }
      float s = rs32((fbits(X[0].x) + fbits(X[1].x)) + (fbits(X[2].x) + fbits(X[3].x)), (fbits(X[0].z) + fbits(X[1].z)) + (fbits(X[2].z) + fbits(X[3].z)));
      s = rs16(s, s);
      s += dpp<0x128>(s);
      *(volatile LAS unsigned*)(L + L_FP + (((lane & 7) * 2 + (lane >> 5)) * 4 + qt) * 4) = dry ? ~0u : ubits(s);
      if (qt != 0 && !dry) return;
#pragma unroll 1
      for (unsigned s2 = 0;; ++s2) {
        const v4u p = *(volatile LAS v4u*)(L + L_FP + (lane & 15) * 16);
        s = (fbits(p.x) + fbits(p.y)) + (fbits(p.z) + fbits(p.w));
        if (__all(p.y != ~0u && p.z != ~0u && p.w != ~0u) || dry || s2 > SPIN_CAP) break;
      }
      __builtin_amdgcn_raw_buffer_store_b16((unsigned short)pk_bf16(s, s), mkrs(a.out, 4608), oo, 0, 0);
      __builtin_amdgcn_raw_buffer_store_b64(v2u{ta, tb}, ws, c == 3 && rho == 0 && qt == 0 && lane == 0 && !dry ? W_X : OOB, 0, SC1);
      if (!dry && qt == 0) ST(c == 3 ? 13 : 6);
    } else {
      const int nr1 = side ? 56 : 8;
      const int ta_ = (g & 15) * 2 + (g >> 4);
      const int b0 = side ? W_P1 + g * P1_STR : W_P + ta_ * P_STR, b1 = W_P + (side ? 64 + g : 32 + ta_) * P_STR, d0 = side ? 8192 : 1024, dest = 2 * (j & 3);
      int voff = b0 + dest * d0 + lane * 16;
      int voff7 = b1 + dest * 1024 + lane * 16;
      int voff8 = lane >= 56 ? b1 + 8192 + (lane - 56) * 16 : b0 + 8 * d0 + (lane < nr1 ? lane : 0) * 16;
      if (dry) { voff = OOB; voff7 = OOB; voff8 = OOB; }
      int voffb = voff + d0;
      asm volatile("" : "+v"(voff), "+v"(voffb), "+v"(voff7), "+v"(voff8));
      if (!wait_ge<16>(ctl, C_WANT, dry ? 0 : 1)) return;
      if (!wait_ge<2>(ctl, C_PHASE, dry ? 0 : POLLAT)) return;
      const int hoff = side ? (lane < 8 ? W_F1 + (g * 8 + lane) * 128 : OOB) : (lane < 16 ? W_F1 + ((g & 15) * 8 + (lane >> 3) * 128 + (lane & 7)) * 128 : OOB);
      const unsigned until = hint_time<false>(ws, dry ? OOB : hoff, OOB, ta, tstart) + DT1;
      wait_until(tstart, dry ? 0u : until);
      v4u X[2][8], ML;
#pragma unroll 1
      for (;; ++sp) {
        const unsigned tr = now32();
        ML = gld(ws, voff8);
#pragma unroll
        for (int i = 0; i < 7; ++i) { X[0][i] = gld(ws, voff, side ? i * 1024 : 0); X[1][i] = gld(ws, voffb, side ? i * 1024 : 0); }
        X[0][7] = gld(ws, voff7); X[1][7] = gld(ws, voff7 + 1024);
        if (!dry) ctl[C_T1] = tr | 1u;
        __builtin_amdgcn_sched_barrier(0);
        unsigned bad = gbad(ML, ta, tb, 0u);
#pragma unroll
        for (int i = 0; i < 8; ++i) bad = gbad(X[1][i], ta, tb, gbad(X[0][i], ta, tb, bad));
        if (__all(bad == 0u) || dry) break;
        if (sp > SPIN_CAP) return;
        __builtin_amdgcn_s_sleep(4);
      }
      const bool seg1 = lane >= 56;
      const float mr = fbits(ML.x);
      const float M = max8(mr);
      const float M0 = max8to64(seg1 ? -INFINITY : M);
      const float f = (lane >= nr1 && !seg1) ? 0.f : ex2(mr - (seg1 ? M : M0));
      float fb[8];
#pragma unroll
      for (int b = 0; b < 8; ++b) fb[b] = fbits(__builtin_amdgcn_ds_bpermute((8 * b + (lane >> 3)) * 4, ubits(f)));
      float Ls = f * fbits(ML.z);
      Ls += dpp<0xB1>(Ls); Ls += dpp<0x4E>(Ls); Ls += dpp<0x141>(Ls);
      const float L1 = fbits(__builtin_amdgcn_readlane(ubits(Ls), 56));
      float L0 = seg1 ? 0.f : Ls;
      L0 += dpp<0x128>(L0); L0 = xsum(L0);
      float inv = __builtin_amdgcn_rcpf(lane >= 32 ? L1 : L0);
      asm volatile("" : "+v"(inv));
      float oe[2], oo[2];
#pragma unroll
      for (int h = 0; h < 2; ++h) {
        float o0 = 0.f, o1 = 0.f;
#pragma unroll
        for (int b = 0; b < 7; ++b) { o0 = fmaf(fb[b], fbits(X[h][b].x), o0); o1 = fmaf(fb[b], fbits(X[h][b].z), o1); }
        oe[h] = rs32(o0, fb[7] * fbits(X[h][7].x)); oo[h] = rs32(o1, fb[7] * fbits(X[h][7].z));
      }
      float e = rs16(oe[0], oe[1]), o = rs16(oo[0], oo[1]);
      e += dpp<0x128>(e); o += dpp<0x128>(o);
      *(volatile LAS unsigned*)(L + L_OP + (lane >> 4) * 32 + (lane & 7) * 4) = pk_bf16(e * inv, o * inv);
      ctl[C_OSEQ] = dry ? 0u : 1u;
    }
  }
}

extern "C" void kernel_launch(void* const* d_in, const int* in_sizes, int n_in, void* d_out, int out_size, void* d_ws, size_t ws_size,
                              hipStream_t stream) {
  Args a;
  a.xA = (const unsigned char*)d_in[0]; a.kA = (const unsigned char*)d_in[1]; a.vA = (const unsigned char*)d_in[2];
  a.wqA = (const unsigned char*)d_in[3]; a.woA = (const unsigned char*)d_in[4];
  a.xB = (const unsigned char*)d_in[5]; a.kB = (const unsigned char*)d_in[6]; a.vB = (const unsigned char*)d_in[7];
  a.wqB = (const unsigned char*)d_in[8]; a.woB = (const unsigned char*)d_in[9];
  a.out = (unsigned char*)d_out; a.ws = (unsigned char*)d_ws;
  fused_decode<<<256, NTHR, 0, stream>>>(a);
}
```

```cpp
#include <hip/hip_runtime.h>
#include <stdint.h>

typedef unsigned v4u __attribute__((ext_vector_type(4)));
typedef unsigned v2u __attribute__((ext_vector_type(2)));
typedef float v4f __attribute__((ext_vector_type(4)));
typedef float v2f __attribute__((ext_vector_type(2)));
typedef short v4s __attribute__((ext_vector_type(4)));
typedef __bf16 bf16x2 __attribute__((ext_vector_type(2)));
typedef __bf16 bf16x8 __attribute__((ext_vector_type(8)));
#define LAS __attribute__((address_space(3)))
#define GAS __attribute__((address_space(1)))
#define DI __device__ __forceinline__

#define NSTAT 8
#define WINU 5
#define ROT 8
#define PSTAG 8
#define GOAT 1
#define POLLAT 2
#define DRY 1
#define DT1 50
#define DT2 110
#define FLAGK 2
#define NREP 16
#define WOAT 2
#define QSLP 8
#define WPACE 0
#define FMODE 1
#define QB 0
#define QM 8
#define TOUCH 1
#define AH2 2
#define PERM 0
#define LPRIO 3
#define STOUCH 1
#define WINL 12
#define VDEF 2
#define SHORTAT 32
constexpr int NWAVE = 12, NTHR = NWAVE * 64;
constexpr int NS = 4 + 16 + NSTAT;
constexpr int POOLC = (16 - NSTAT) * 8;
constexpr int NT = 48, TP = POOLC - NT;
static_assert(TP >= 0 && TP <= NT && NSTAT >= 4 && WINU >= 2 && WINU <= 12 && NSTAT <= 16, "");
#define PHASED 1
constexpr int NSP = PHASED ? NS : 5;
__host__ __device__ constexpr unsigned segk(unsigned n) { return n < 4 ? n : n < NSP ? n + 4 : 2 * n - 1; }
__host__ __device__ constexpr unsigned segv(unsigned n) { return n < 4 ? n + 4 : n + 1 < NSP ? n + NSP : 2 * n + 2; }
constexpr int WINE = (WINU + 1) & ~1;
constexpr unsigned SPIN_CAP = 1u << 18;
constexpr int NHOLD = 16, HOLD_B = 544, REC_B = 528;
constexpr int L_RK = 0, L_RV = 0  , L_CTL = 131072, L_Q = L_CTL + 1024, L_OP = L_Q + 1024, L_FP = L_CTL + 768  , L_QP = L_OP + 128, L_REC = L_QP + 9 * 256,
              L_HOLD = L_REC + 24 * REC_B, L_PB = L_HOLD + NHOLD * HOLD_B, PB_B = 144, L_END = L_PB + (NS - 4) * PB_B;
static_assert(L_END <= 163840, "");
enum { C_FULL = 0, C_END = 1, C_PHASE = 2, C_QRDY = 3, C_WANT = 4  , C_T1 = 5  , C_TAGA = 6, C_TAGB = 7, C_FREE = 8  , C_CNT = 16,
       C_QCNT = 19, C_HELD = 21, C_OSEQ = 22, C_T2 = 24  , C_TICK = 64, C_DESC = 128 };
static_assert(NT + 2 <= 64 && POOLC + 0 <= 64, "");
constexpr int W_X = 0, W_CTR = 4096, W_F1 = 16384  , W_F2 = W_F1 + 32768  , W_QA = W_F2 + 2 * NREP * 1024, W_QB = W_QA + 32768,
              W_P = W_QB + 32768, P_STR = 9216  , W_P1 = W_P + 96 * P_STR, P1_STR = 66560  ,
              W_Y = W_P1 + 32 * P1_STR, YS = 16384, W_ENDB = 256 + W_Y + 4 * 36 * YS  , W_STAMP = (W_ENDB + 4095) / 4096 * 4096;
constexpr int W_SAFE1 = W_P1 + 24 * P1_STR + 7424, W_SAFE2 = W_ENDB - 128;
static_assert(W_SAFE1 >> 21 == 1 && W_SAFE2 >> 21 == 2, "");
constexpr float SCALE = 0.088388347648318440f, L2E = 1.4426950408889634f;
constexpr int SC1 = 16, OOB = 0x70000000;

struct Args {
  const unsigned char *xA, *kA, *vA, *wqA, *woA, *xB, *kB, *vB, *wqB, *woB;
  unsigned char *out, *ws;
};

DI float dot2(unsigned a, unsigned b, float c) { return __builtin_amdgcn_fdot2_f32_bf16(__builtin_bit_cast(bf16x2, a), __builtin_bit_cast(bf16x2, b), c, false); }
template <int CTRL> DI float dpp(float x) { return __builtin_bit_cast(float, __builtin_amdgcn_mov_dpp(__builtin_bit_cast(int, x), CTRL, 0xf, 0xf, true)); }
DI unsigned pk_bf16(float lo, float hi) {
  unsigned r = __builtin_bit_cast(unsigned, bf16x2{(__bf16)lo, (__bf16)hi});
  asm("" : "+v"(r));
  return r;
}
DI float bf_lo(unsigned u) { return __builtin_bit_cast(float, u << 16); }
DI float bf_hi(unsigned u) { return __builtin_bit_cast(float, u & 0xffff0000u); }
DI float fbits(unsigned u) { return __builtin_bit_cast(float, u); }
DI unsigned ubits(float f) { return __builtin_bit_cast(unsigned, f); }
DI unsigned rfl(unsigned x) { return __builtin_amdgcn_readfirstlane(x); }
DI float ex2(float x) { return __builtin_amdgcn_exp2f(x); }
DI float mx2(float a, float b) { return __builtin_amdgcn_fmed3f(a, b, INFINITY); }
DI v4f mfma(v4u a, v4u b, v4f c) { return __builtin_amdgcn_mfma_f32_16x16x32_bf16(__builtin_bit_cast(bf16x8, a), __builtin_bit_cast(bf16x8, b), c, 0, 0, 0); }
DI v2u trd(LAS unsigned char* p) { return __builtin_bit_cast(v2u, __builtin_amdgcn_ds_read_tr16_b64_v4i16((LAS v4s*)p)); }
DI float rs32(float x, float y) { auto s = __builtin_amdgcn_permlane32_swap(ubits(x), ubits(y), false, false); return fbits(s[0]) + fbits(s[1]); }
DI float rs16(float x, float y) { auto s = __builtin_amdgcn_permlane16_swap(ubits(x), ubits(y), false, false); return fbits(s[0]) + fbits(s[1]); }
DI float xsum(float x) { x = rs16(x, x); return rs32(x, x); }
DI float xmax(float x) {
  auto s = __builtin_amdgcn_permlane16_swap(ubits(x), ubits(x), false, false); x = mx2(fbits(s[0]), fbits(s[1]));
  auto t = __builtin_amdgcn_permlane32_swap(ubits(x), ubits(x), false, false); return mx2(fbits(t[0]), fbits(t[1]));
}
DI float max8(float x) {
  asm("s_nop 1\n\tv_max_f32_dpp %0, %0, %0 quad_perm:[1,0,3,2] row_mask:0xf bank_mask:0xf\n\ts_nop 1\n\tv_max_f32_dpp %0, %0, %0 quad_perm:[2,3,0,1] row_mask:0xf bank_mask:0xf\n\t"
      "s_nop 1\n\tv_max_f32_dpp %0, %0, %0 row_half_mirror row_mask:0xf bank_mask:0xf" : "+v"(x));
  return x;
}
DI float max8to64(float x) {
  float t;
  asm("s_nop 1\n\tv_max_f32_dpp %0, %0, %0 row_ror:8 row_mask:0xf bank_mask:0xf\n\tv_mov_b32 %1, %0\n\ts_nop 1\n\tv_permlane16_swap_b32 %0, %1\n\tv_max_f32 %0, %0, %1\n\t"
      "v_mov_b32 %1, %0\n\ts_nop 1\n\tv_permlane32_swap_b32 %0, %1\n\tv_max_f32 %0, %0, %1" : "+v"(x), "=&v"(t));
  return x;
}
DI float max4to64(float x) {
  asm("s_nop 1\n\tv_max_f32_dpp %0, %0, %0 row_ror:4 row_mask:0xf bank_mask:0xf" : "+v"(x));
  return max8to64(x);
}
template <int LANE> DI unsigned bcast16(unsigned x) { return __builtin_amdgcn_mov_dpp(x, 0x150 + LANE, 0xf, 0xf, true); }
DI void lgkm0() { asm volatile("s_waitcnt lgkmcnt(0)" ::: "memory"); }
template <int N> DI void waitvm() { asm volatile("s_waitcnt vmcnt(%0)" ::"n"(N) : "memory"); }
DI __amdgpu_buffer_rsrc_t mkrs(const void* p, unsigned bytes) { return __builtin_amdgcn_make_buffer_rsrc((void*)p, 0, bytes, 0x00020000); }
DI void dma8(__amdgpu_buffer_rsrc_t rs, unsigned voff, unsigned soff, unsigned lds) {
  unsigned keep, s2;
  asm volatile("s_mov_b32 %0, m0\n\ts_mov_b32 m0, %5\n\ts_add_u32 %1, %4, 0x1000\n\ts_nop 2\n\t"
               "buffer_load_dwordx4 %2, %3, %4 offen nt lds\n\t"
               "buffer_load_dwordx4 %2, %3, %4 offen offset:1024 nt lds\n\t"
               "buffer_load_dwordx4 %2, %3, %4 offen offset:2048 nt lds\n\t"
               "buffer_load_dwordx4 %2, %3, %4 offen offset:3072 nt lds\n\t"
               "s_add_u32 m0, m0, 0x1000\n\ts_nop 0\n\t"
               "buffer_load_dwordx4 %2, %3, %1 offen nt lds\n\t"
               "buffer_load_dwordx4 %2, %3, %1 offen offset:1024 nt lds\n\t"
               "buffer_load_dwordx4 %2, %3, %1 offen offset:2048 nt lds\n\t"
               "buffer_load_dwordx4 %2, %3, %1 offen offset:3072 nt lds\n\t"
               "s_mov_b32 m0, %0"
               : "=&s"(keep), "=&s"(s2) : "v"(voff), "s"(rs), "s"(soff), "s"(lds) : "memory", "scc");
}
DI void dma8d(__amdgpu_buffer_rsrc_t rs, unsigned voff, unsigned soff, unsigned lds) {
  unsigned keep, s2;
  asm volatile("s_mov_b32 %0, m0\n\ts_mov_b32 m0, %5\n\ts_add_u32 %1, %4, 0x1000\n\ts_nop 2\n\t"
               "buffer_load_dwordx4 %2, %3, %4 offen lds\n\t"
               "buffer_load_dwordx4 %2, %3, %4 offen offset:1024 lds\n\t"
               "buffer_load_dwordx4 %2, %3, %4 offen offset:2048 lds\n\t"
               "buffer_load_dwordx4 %2, %3, %4 offen offset:3072 lds\n\t"
               "s_add_u32 m0, m0, 0x1000\n\ts_nop 0\n\t"
               "buffer_load_dwordx4 %2, %3, %1 offen lds\n\t"
               "buffer_load_dwordx4 %2, %3, %1 offen offset:1024 lds\n\t"
               "buffer_load_dwordx4 %2, %3, %1 offen offset:2048 lds\n\t"
               "buffer_load_dwordx4 %2, %3, %1 offen offset:3072 lds\n\t"
               "s_mov_b32 m0, %0"
               : "=&s"(keep), "=&s"(s2) : "v"(voff), "s"(rs), "s"(soff), "s"(lds) : "memory", "scc");
}
DI v4u gld(__amdgpu_buffer_rsrc_t rs, int off, int soff = 0) { return __builtin_amdgcn_raw_buffer_load_b128(rs, off, soff, SC1); }
DI unsigned gbad(v4u g, unsigned ta, unsigned tb, unsigned acc) { return __builtin_amdgcn_bitop3_b32(g.w, tb, __builtin_amdgcn_bitop3_b32(g.y, ta, acc, 0xBE), 0xBE); }
DI void gst(__amdgpu_buffer_rsrc_t rs, int off, unsigned a, unsigned b, unsigned ta, unsigned tb) { __builtin_amdgcn_raw_buffer_store_b128(v4u{a, ta, b, tb}, rs, off, 0, SC1); }

#define ST(i) do { } while (0)

typedef volatile LAS unsigned* ctl_t;
template <int SLP> DI bool wait_ge(ctl_t ctl, int idx, unsigned v) {
#pragma unroll 1
  for (unsigned s = 0; rfl(ctl[idx]) < v; ++s) {
    if (s > SPIN_CAP) return false;
    __builtin_amdgcn_s_sleep(SLP);
  }
  asm volatile("" ::: "memory");
  return true;
}
DI void lds_add(LAS unsigned char* L, int idx, unsigned v, int lane) {
  lgkm0();
  if (lane == 0) __hip_atomic_fetch_add((LAS unsigned*)(L + L_CTL) + idx, v, __ATOMIC_RELAXED, __HIP_MEMORY_SCOPE_WORKGROUP);
}
DI bool last_arriver(LAS unsigned char* L, int idx, unsigned n, int lane) {
  lgkm0();
  unsigned old = 0;
  if (lane == 0) old = __hip_atomic_fetch_add((LAS unsigned*)(L + L_CTL) + idx, 1u, __ATOMIC_RELAXED, __HIP_MEMORY_SCOPE_WORKGROUP);
  old = rfl(old);
  asm volatile("" ::: "memory");
  return old == n - 1;
}
DI void read_k(LAS unsigned char* L, unsigned off, int imm, v4u (&K)[4]) {
#pragma unroll
  for (int kk = 0; kk < 4; ++kk) K[kk] = *(const LAS v4u*)(L + L_RK + (off ^ (kk * 64)) + imm);
}
DI void read_v(LAS unsigned char* L, unsigned off, int imm, v4u (&V)[8]) {
#pragma unroll
  for (int blk = 0; blk < 8; ++blk) {
    LAS unsigned char* p = L + L_RV + (off ^ ((blk >> 1) * 64)) + (blk & 1) * 32 + imm;
    const v2u x = trd(p), y = trd(p + 4096);
    V[blk] = v4u{x.x, x.y, y.x, y.y};
  }
  __builtin_amdgcn_sched_barrier(0);
}
DI v4f qk(const v4u (&K)[4], const v4u (&q)[4]) {
  v4f d = {0.f, 0.f, 0.f, 0.f};
#pragma unroll
  for (int kk = 0; kk < 4; ++kk) d = mfma(K[kk], q[kk], d);
  return d;
}
DI v4f round2(v4f d) {
  const unsigned x01 = pk_bf16(d[0], d[1]), x23 = pk_bf16(d[2], d[3]);
  const unsigned b01 = pk_bf16(bf_lo(x01) * SCALE, bf_hi(x01) * SCALE), b23 = pk_bf16(bf_lo(x23) * SCALE, bf_hi(x23) * SCALE);
  return v4f{bf_lo(b01), bf_hi(b01), bf_lo(b23), bf_hi(b23)};
}
DI float sel(unsigned mask, float a, float b) { return fbits(__builtin_amdgcn_bitop3_b32(mask, ubits(a), ubits(b), 0xCA)); }
DI v4f sel4(unsigned mask, v4f a, v4f b) { return v4f{sel(mask, a[0], b[0]), sel(mask, a[1], b[1]), sel(mask, a[2], b[2]), sel(mask, a[3], b[3])}; }
DI v2f sel2(unsigned mask, v2f a, v2f b) { return v2f{sel(mask, a[0], b[0]), sel(mask, a[1], b[1])}; }
DI unsigned bitmask(int x, int bit) { unsigned m = (unsigned)((x << (31 - bit)) >> 31); asm("" : "+v"(m)); return m; }
DI float max4(v4f d) { return mx2(mx2(d[0], d[1]), mx2(d[2], d[3])); }
DI float sum4(v4f d) { return (d[0] + d[1]) + (d[2] + d[3]); }
DI v4f expo(v4f s, float m2) { return v4f{ex2(fmaf(s[0], L2E, -m2)), ex2(fmaf(s[1], L2E, -m2)), ex2(fmaf(s[2], L2E, -m2)), ex2(fmaf(s[3], L2E, -m2))}; }
DI v4u pack_p(v4f p0, v4f p1) { return v4u{pk_bf16(p0[0], p0[1]), pk_bf16(p0[2], p0[3]), pk_bf16(p1[0], p1[1]), pk_bf16(p1[2], p1[3])}; }

DI unsigned now32() { return (unsigned)__builtin_amdgcn_s_memrealtime(); }
DI void wait_until(unsigned t0, unsigned until) {
#pragma unroll 1
  for (unsigned sp = 0; sp < 64 && now32() - t0 < until; ++sp) { }
}
DI void hint_set(__amdgpu_buffer_rsrc_t ws, int off, unsigned t, unsigned tag) {
  asm volatile("buffer_atomic_swap_x2 %0, %1, %2, 0 offen" ::"v"(v2u{t, tag}), "v"(off), "s"(ws) : "memory");
}
template <bool TWO> DI unsigned hint_time(__amdgpu_buffer_rsrc_t ws, int off, int off2, unsigned tag, unsigned t0) {
  unsigned rel = 0;
  waitvm<0>();
#define HP1(A0, A1, o) asm volatile("v_mov_b32 v16" #A0 ", 0\n\tv_mov_b32 v16" #A1 ", 0\n\tbuffer_atomic_add_x2 v[16" #A0 ":16" #A1 "], %0, %1, 0 offen sc0" \
                                    ::"v"(o), "s"(ws) : "memory", "v16" #A0, "v16" #A1)
#define HC1(A0, A1, o, N) { unsigned t_, g_; asm volatile("s_waitcnt vmcnt(%2)\n\tv_mov_b32 %0, v16" #A0 "\n\tv_mov_b32 %1, v16" #A1 : "=v"(t_), "=v"(g_) : "n"(N) : "memory", "v16" #A0, "v16" #A1); \
                            const bool ok_ = g_ == tag; rel = ok_ ? max(rel, t_ - t0) : rel; o = ok_ ? OOB : o; }
  HP1(0, 1, off);
  if (TWO) HP1(2, 3, off2);
  __builtin_amdgcn_s_sleep(PSTAG);
#pragma unroll 1
  for (unsigned sp = 0; sp < 4096; ++sp) {
    HP1(4, 5, off);
    if (TWO) HP1(6, 7, off2);
    HC1(0, 1, off, TWO ? 3 : 1)
    if (TWO) HC1(2, 3, off2, 2)
    if (__all(off == OOB && off2 == OOB)) break;
    HP1(0, 1, off);
    if (TWO) HP1(2, 3, off2);
    HC1(4, 5, off, TWO ? 3 : 1)
    if (TWO) HC1(6, 7, off2, 2)
    if (__all(off == OOB && off2 == OOB)) break;
  }
#undef HP1
#undef HC1
  return rfl((unsigned)max8to64(max8((float)rel)));
}
DI bool park_rec(LAS unsigned char* L, ctl_t ctl, int slot, int adr, int dstr, int mladr, int gi, int lane, v2f o, float m, float l) {
  LAS unsigned char* h = L + L_HOLD + slot * HOLD_B;
  *(LAS v2f*)(h + 32 + gi * 8) = o;
  if (lane == 0) { *(LAS v4u*)h = v4u{(unsigned)adr, (unsigned)dstr, (unsigned)mladr, 0u}; *(LAS v2f*)(h + 16) = v2f{m, l}; }
  lgkm0();
  if (lane == 0) __hip_atomic_fetch_or((LAS unsigned*)(L + L_CTL) + C_HELD, 1u << slot, __ATOMIC_RELAXED, __HIP_MEMORY_SCOPE_WORKGROUP);
  lgkm0();
  return rfl(ctl[C_PHASE]) >= GOAT;
}
template <int NR> DI v2f merge_rec(LAS unsigned char* rec, int lane, float& Mo, float& Lo) {
  v2f av[NR];
  const v2f ml = *(LAS v2f*)(rec + (lane & (NR - 1)) * REC_B);
#pragma unroll
  for (int w = 0; w < NR; ++w) av[w] = *(LAS v2f*)(rec + w * REC_B + 16 + lane * 8);
  float M = ml.x; M = mx2(M, dpp<0xB1>(M)); M = mx2(M, dpp<0x4E>(M));
  if (NR == 8) M = mx2(M, dpp<0x141>(M));
  const float f = ex2(ml.x - M);
  float Ls = f * ml.y; Ls += dpp<0xB1>(Ls); Ls += dpp<0x4E>(Ls);
  if (NR == 8) Ls += dpp<0x141>(Ls);
  float a0 = 0.f, a1 = 0.f;
#pragma unroll
  for (int w = 0; w < NR; ++w) { const float fw = fbits(__builtin_amdgcn_readlane(ubits(f), w)); a0 = fmaf(fw, av[w].x, a0); a1 = fmaf(fw, av[w].y, a1); }
  Mo = M; Lo = Ls;
  return v2f{a0, a1};
}

__global__ void __launch_bounds__(NTHR) fused_decode(Args a) {
  __shared__ __attribute__((aligned(16))) unsigned char smem[L_END];
  LAS unsigned char* L = (LAS unsigned char*)smem;
  ctl_t ctl = (ctl_t)(L + L_CTL);
  asm volatile("" ::"s"(a.xA), "s"(a.kA), "s"(a.vA), "s"(a.wqA), "s"(a.woA), "s"(a.xB), "s"(a.kB), "s"(a.vB), "s"(a.wqB), "s"(a.woB), "s"(a.out), "s"(a.ws));
  const int wg = blockIdx.x, g = wg >> 3, j = wg & 7;
  const int lane = threadIdx.x & 63, w = rfl(threadIdx.x >> 6);
  const __amdgpu_buffer_rsrc_t ws = mkrs(a.ws, W_ENDB);
  ST(w == 1 ? 0 : 31);

  if (w == 0) {
    __builtin_amdgcn_s_waitcnt(0);
    const unsigned lb = (unsigned)(uintptr_t)L;
    const unsigned voff = (lane * 16) ^ ((lane >> 4) << 6);
    const unsigned offA = g * (2u << 20) + (3u << 19) + j * 16384;
    const __amdgpu_buffer_rsrc_t rk = mkrs(a.kA, 64u << 20);
    dma8(rk, voff, offA, lb + L_RK); dma8(rk, voff, offA + 8192, lb + L_RK + 8192);
    asm volatile("s_barrier" ::: "memory");
    __builtin_amdgcn_s_setprio(LPRIO);
    int u = 4, pub = 0;
    unsigned sg = 1;
#define TB()
#define TE(x)
#define LWAIT(idx, v, out, stl) _Pragma("unroll 1") for (unsigned sp = 0;; ++sp) { \
      out = rfl(ctl[idx]); if (out + 1u > (v)) break; \
      if (sp > SPIN_CAP) return; \
      TB(); if (sp == 6) { waitvm<0>(); pub = u; ctl[C_FULL] = pub; } __builtin_amdgcn_s_sleep(1); TE(stl); }
#define HALF(rs, so, ld) do { waitvm<4 * WINU>(); if (u - WINE > pub) { pub = u - WINE; ctl[C_FULL] = pub; } if (df) dma8d(rs, voff, so, ld); else dma8(rs, voff, so, ld); u += 2; } while (0)
#define SEG(rs, so, stl, dfl) do { const bool df = dfl; unsigned fr_; LWAIT(C_FREE + (sg & 7), sg < 8 ? 0u : sg < 16 ? 2u : sg - 7, fr_, stl) \
      const unsigned ld_ = lb + (sg & 7) * 16384; HALF(rs, so, ld_); HALF(rs, (so) + 8192, ld_ + 8192); ++sg; } while (0)
    const unsigned jr = j + g * ROT;
#pragma unroll 1
    for (int run = 0; run < 4; ++run) {
      const __amdgpu_buffer_rsrc_t rs = mkrs(run == 0 ? a.kA : run == 1 ? a.vA : run == 2 ? a.kB : a.vB, run < 2 ? 64u << 20 : 128u << 20);
      const int cnt = run < 2 ? 4 : NS - 2 - run;
#pragma unroll 1
      for (int i = run == 0; i < cnt; ++i) {
        const unsigned so = run < 2 ? offA + i * 131072 : (g + (i & 16) * 2) * (2u << 20) + ((i * 8 + jr) & 127) * 16384;
        if (sg + 4 == 2 * NS) ctl[C_WANT] = 1;
        if (TP <= 8 && sg + 2 == 2 * NS) ctl[C_WANT] = 2;
        SEG(rs, so, stallF, run == 3 && ((VDEF & 1) && i < 16 || (VDEF & 2) && i >= 16));
      }
    }
    {
      const __amdgpu_buffer_rsrc_t rkb = mkrs(a.kB, 128u << 20), rvb = mkrs(a.vB, 128u << 20);
      const unsigned b1 = (32 + g) * (2u << 20);
      unsigned sv = b1 + (((NS - 5) * 8 + jr) & 127) * 16384;
      unsigned k = 0, cid = 0, dsc = 0;
      bool second = false;
      unsigned want = 0;
#pragma unroll 1
      for (unsigned n = NS;; ++n) {
        unsigned t = 0;
        if (!second) { LWAIT(C_TICK + k, 0u, t, stallT) }
        const bool last = t >= (unsigned)NT;
        unsigned sk = 0;
        if (!last) {
          if (second) { ++cid; dsc |= 1u << 10; second = false; }
          else {
            second = t < (unsigned)TP;
            cid = second ? 2 * t : TP + t;
            dsc = (8 + t) | ((NS + k) & 3) << 8 | (second ? 0u : 1u << 10) | (4 + k) << 12;
            ++k;
          }
          ctl[C_DESC + n - NS] = dsc;
          sk = b1 + ((NSTAT * 8 + cid + g * ROT) & 127) * 16384;
          const bool shrt = cid >= SHORTAT;
          if (AH2 == 2 && shrt && !second) ctl[C_WANT] = want = max(want, k + 1);
          SEG(rkb, sk, stallF2, false);
          if (AH2 == 3 || !shrt) ctl[C_WANT] = want = second || cid + 16 < 2 * TP ? k + 1 : k + 2;
          else if (!second) ctl[C_WANT] = want = max(want, k + 1);
        } else {
          ctl[C_END] = n; ctl[C_PHASE] = 1; ++sg; ST(8);
        }
        SEG(rvb, sv, stallF2, (VDEF & 2) != 0);
        if (last) break;
        sv = sk;
      }
    }
    ctl[C_PHASE] = 2;
    ST(9);
#define DRAIN(k_) if (WINU + 2 > k_) { waitvm<4 * k_>(); const int p_ = u - k_ + (k_ < 4 ? 4 : 0); if (p_ > pub) { pub = p_; ctl[C_FULL] = pub; } \
      if (k_ == 0) ctl[C_PHASE] = 3; }
    DRAIN(12) DRAIN(10) DRAIN(8) DRAIN(6) DRAIN(4) DRAIN(2) DRAIN(0)
#undef DRAIN
    ST(10);
    return;
  }
  const unsigned tstart = now32();
  const int cb = (wg & ~15) + ((wg & 7) << 1) + ((wg >> 3) & 1);
  const int side = j >> 2, prod = g * 4 + (j & 3);
  unsigned ta, tb;
  int jb, jend;
  if (w == 1) {
    const v2u ep = __builtin_amdgcn_raw_buffer_load_b64(ws, W_X, 0, SC1);
    if (cb >= 128 && (cb & 3) == 0 && lane == 0) __builtin_amdgcn_raw_buffer_store_b32(0u, ws, W_CTR + ((cb - 128) >> 2) * 256, 0, SC1);
    ctl[lane] = lane == C_END ? 0x7fffffffu : 0u;
    ctl[lane + 64] = 0xffffffffu; ctl[lane + 128] = 0xffffffffu; ctl[lane + 192] = 0xffffffffu;
    asm volatile("s_waitcnt lgkmcnt(0)\n\ts_barrier" ::: "memory");
    ta = rfl(ep.x) + 1u; tb = rfl(ep.y) + 1u;
    ta += ta == 0u; tb += tb == 0u;
    waitvm<0>();
    if (!wait_ge<1>(ctl, C_QCNT, 9)) return;
    ST(1);
    if (lane < 16) {
      v4f s = {0.f, 0.f, 0.f, 0.f};
#pragma unroll
      for (int i = 0; i < 9; ++i) s += *(LAS v4f*)(L + L_QP + i * 256 + lane * 16);
      const int b = lane >> 3, quad = lane & 7;
      const int off = cb < 128 ? W_QA + ((b * 16 + (cb >> 3)) * 64 + (cb & 7) * 8 + quad) * 16 : W_QB + (((cb - 128) >> 2) * 64 + b * 32 + (cb & 3) * 8 + quad) * 16;
      gst(ws, off, pk_bf16(s.x, s.y), pk_bf16(s.z, s.w), ta, tb);
    }
    v4u qa, qb;
#pragma unroll 1
    for (unsigned sp = 0;; ++sp) {
      __builtin_amdgcn_s_sleep(QSLP);
      qa = gld(ws, W_QA + g * 1024 + lane * 16);
      qb = gld(ws, W_QB + g * 1024 + lane * 16);
      if (__all(gbad(qb, ta, tb, gbad(qa, ta, tb, 0u)) == 0u)) break;
      if (sp > SPIN_CAP) return;
    }
    *(LAS v2u*)(L + L_Q + lane * 8) = v2u{qa.x, qa.z};
    *(LAS v2u*)(L + L_Q + 512 + lane * 8) = v2u{qb.x, qb.z};
    ctl[C_TAGA] = ta; ctl[C_TAGB] = tb;
    lgkm0();
    ctl[C_QRDY] = 1;
    ST(2);
    {
      int c0 = 0, c1 = 0, mine = 0, pend = 0;
      bool may_quit = QB > 0;
#pragma unroll 1
      for (int k = 0; k <= NT; ++k) {
        if (!wait_ge<2>(ctl, C_WANT, k + 1)) return;
        const int dd = c1 - c0;
        const bool quit = may_quit && mine > 0 && (POOLC - c1) * mine - dd < QB * (dd - QM * mine);
        unsigned t = 0;
        if (lane == 0) t = __hip_atomic_fetch_add((GAS unsigned*)(a.ws + W_CTR + g * 256), quit ? 0x10000u : 1u, __ATOMIC_RELAXED, __HIP_MEMORY_SCOPE_AGENT);
        t = rfl(t);
        if (quit) {
          if (t >> 16 >= 5) { may_quit = false; --k; continue; }
          t = NT;
        }
        t &= 0xffffu;
        ctl[C_TICK + k] = t;
        if (t >= (unsigned)NT) break;
        c1 = t < (unsigned)TP ? 2 * t : TP + t;
        if (k == 0) c0 = c1;
        mine += pend;
        pend = t < (unsigned)TP ? 2 : 1;
      }
    }
    if (!wait_ge<1>(ctl, C_T1, 1)) return;
    hint_set(ws, lane < NREP ? W_F2 + (side * NREP + lane) * 1024 + prod * 8 : OOB, ctl[C_T1], ta);
    if ((prod >> 1) < 36) {
      const int ho = W_F2 + (side * NREP + prod % NREP) * 1024 + lane * 8;
      ctl[C_T2] = (hint_time<true>(ws, ho, ho + 512, ta, tstart) + tstart + DT2) | 1u;
    }
    return;
  } else if (w == 2) {
    if (STOUCH) {
      const size_t pa = (size_t)g * (2u << 20) + (3u << 19), pb = (size_t)g * (2u << 20), pc = pb + (64u << 20);
      const unsigned char* wo = (side ? a.woB : a.woA) + (size_t)((side ? g : 2 * (g & 15) + (g >> 4)) * 128 + (j & 3) * 32) * 1152;
      unsigned d0, d1, d2, d3, d4, d5, d6, d7;
      asm volatile("s_barrier\n\ts_sleep %16\n\ts_load_dword %0, %8, 0\n\ts_load_dword %1, %9, 0\n\ts_load_dword %2, %10, 0\n\ts_load_dword %3, %11, 0\n\ts_load_dword %4, %12, 0\n\t"
                   "s_load_dword %5, %13, 0\n\ts_load_dword %6, %14, 0\n\ts_load_dword %7, %15, 0\n\ts_waitcnt lgkmcnt(0)"
                   : "=&s"(d0), "=&s"(d1), "=&s"(d2), "=&s"(d3), "=&s"(d4), "=&s"(d5), "=&s"(d6), "=&s"(d7)
                   : "s"(a.vA + pa), "s"(a.kB + pb), "s"(a.kB + pc), "s"(a.vB + pb), "s"(a.vB + pc), "s"(wo), "s"(a.ws + W_SAFE1), "s"(a.ws + W_SAFE2), "n"(STOUCH - 1)
                   : "memory");
    } else
    asm volatile("s_barrier" ::: "memory");
    if (!wait_ge<16>(ctl, C_QRDY, 1)) return;
    ta = rfl(ctl[C_TAGA]); tb = rfl(ctl[C_TAGB]);
    jb = DRY ? 0 : 2; jend = 3;
  } else {
    {
      const int qw = w - 3;
      const int r0 = qw * 64 + (lane >> 2) * 4;
      const unsigned char* xp = (cb < 128 ? a.xA : a.xB) + r0 * 2;
      const unsigned char* wp = (cb < 128 ? a.wqA : a.wqB) + (size_t)r0 * 8192 + (cb & 127) * 64 + (lane & 3) * 16;
      v4u Wq[4];
#pragma unroll
      for (int i = 0; i < 4; ++i) Wq[i] = __builtin_nontemporal_load((const GAS v4u*)(wp + i * 8192));
      const v2u X0 = *(const GAS v2u*)xp, X1 = *(const GAS v2u*)(xp + 1152);
      asm volatile("s_barrier" ::: "memory");
      float ac[2][8];
#pragma unroll
      for (int k = 0; k < 8; ++k) { ac[0][k] = 0.f; ac[1][k] = 0.f; }
#pragma unroll
      for (int s = 0; s < 2; ++s)
#pragma unroll
        for (int d = 0; d < 4; ++d) {
          const unsigned lo = __builtin_amdgcn_perm(Wq[2 * s + 1][d], Wq[2 * s][d], 0x05040100u), hi = __builtin_amdgcn_perm(Wq[2 * s + 1][d], Wq[2 * s][d], 0x07060302u);
          ac[0][2 * d] = dot2(lo, X0[s], ac[0][2 * d]); ac[0][2 * d + 1] = dot2(hi, X0[s], ac[0][2 * d + 1]);
          ac[1][2 * d] = dot2(lo, X1[s], ac[1][2 * d]); ac[1][2 * d + 1] = dot2(hi, X1[s], ac[1][2 * d + 1]);
        }
      float b8[8], c4[4];
#pragma unroll
      for (int k = 0; k < 8; ++k) b8[k] = rs32(ac[0][k], ac[1][k]);
#pragma unroll
      for (int k = 0; k < 4; ++k) { float t = rs16(b8[k], b8[k + 4]); t += dpp<0x128>(t); t += dpp<0x124>(t); c4[k] = t; }
      if ((lane & 12) == 0) *(LAS v4f*)(L + L_QP + qw * 256 + (lane >> 5) * 128 + (lane & 3) * 32 + ((lane >> 4) & 1) * 16) = v4f{c4[0], c4[1], c4[2], c4[3]};
      lds_add(L, C_QCNT, 1, lane);
    }
    const int r = lane >> 4, c = lane & 15;
    const unsigned kof = c * 256 + r * 16 + (c & 3) * 64;
    const unsigned vof = (4 * r + (c >> 2)) * 256 + (c >> 2) * 64 + (c & 3) * 8;
    if (w >= 4) {
      const unsigned aw = w - 4, off = aw * 8192, an = aw >> 1, ah = 2 * (aw & 1) + 2;
      v4u K0[4], K1[4], V[8];
      if (!wait_ge<1>(ctl, C_FULL, 4 * an + ah)) return;
      read_k(L, off + kof, 0, K0); read_k(L, off + kof, 4096, K1);
      lds_add(L, C_FREE + an, 1, lane);
      if (!wait_ge<1>(ctl, C_FULL, 4 * an + 16 + ah)) return;
      read_v(L, 65536 + off + vof, 0, V);
      lds_add(L, C_FREE + 4 + an, 1, lane);
      if (!wait_ge<1>(ctl, C_QRDY, 1)) return;
      const LAS v4u* qp = (const LAS v4u*)(L + L_Q) + r + (c >> 3) * 16;
      const v4u q[4] = {qp[0], qp[4], qp[8], qp[12]};
      v4f s0 = round2(qk(K0, q)), s1 = round2(qk(K1, q));
      const float M = xmax(mx2(max4(s0), max4(s1))) * L2E;
      s0 = expo(s0, M); s1 = expo(s1, M);
      const v4u P = pack_p(s0, s1);
      const float Ls = xsum(sum4(s0) + sum4(s1));
      v4f acc[8];
#pragma unroll
      for (int blk = 0; blk < 8; ++blk) acc[blk] = mfma(V[blk], P, v4f{0.f, 0.f, 0.f, 0.f});
#pragma unroll
      for (int k = 0; k < 4; ++k) acc[k] = sel4(bitmask(c, 2), acc[k + 4], acc[k]);
#pragma unroll
      for (int k = 0; k < 2; ++k) acc[k] = sel4(bitmask(c, 1), acc[k + 2], acc[k]);
      acc[0] = sel4(bitmask(c, 0), acc[1], acc[0]);
      LAS unsigned char* rec = L + L_REC + ((c >> 3) * 8 + aw) * REC_B;
      *(LAS v4f*)(rec + 16 + ((c & 7) * 16 + r * 4) * 4) = acc[0];
      if ((lane & 0x37) == 0) *(LAS v2f*)rec = v2f{M, Ls};
      if (last_arriver(L, C_CNT, 8, lane)) {
#pragma unroll 1
        for (int e = 0; e < 2; ++e) {
          float Mo, Lo;
          const v2f o = merge_rec<8>(L + L_REC + e * 8 * REC_B, lane, Mo, Lo);
          const int base = W_P + (g * 2 + e) * P_STR;
          if (park_rec(L, ctl, e, base + j * 128, 1024, base + 8192 + j * 16, lane, lane, o, Mo, Lo)) {
            const unsigned ta_ = ctl[C_TAGA], tb_ = ctl[C_TAGB];
            gst(ws, base + j * 128 + (lane >> 3) * 1024 + (lane & 7) * 16, ubits(o.x), ubits(o.y), ta_, tb_);
            if (lane == 0) gst(ws, base + 8192 + j * 16, ubits(Mo), ubits(Lo), ta_, tb_);
          }
        }
        ST(4);
      }
    } else if (!wait_ge<8>(ctl, C_QRDY, 1)) return;
    ta = rfl(ctl[C_TAGA]); tb = rfl(ctl[C_TAGB]);
    if (w >= 8) {
      const int cw = w - 8;
      float m = -INFINITY, l = 0.f;
      v4f acc[8];
      bool fresh = true;
      const unsigned mk0 = bitmask(c, 0), mk1 = bitmask(c, 1), mk2 = bitmask(c, 2), mk3 = bitmask(c, 3);
      const int gid = (c >> 1) * 8 + 2 * r + (c & 1);
      bool freshv = true;
      int mode = PHASED ? 1 : 3;
#pragma unroll 1
      for (unsigned n = 4 + cw;;) {
        bool stop = false;
        unsigned dsc = cw << 8;
        const bool dyn = n >= NS;
        if (dyn) {
#pragma unroll 1
          for (unsigned sp = 0;; ++sp) {
            const unsigned dd = ctl[C_DESC + n - NS], en = ctl[C_END];
            dsc = rfl(dd);
            if (dsc != 0xffffffffu) break;
            if (n >= rfl(en) || sp > SPIN_CAP) { stop = true; break; }
            __builtin_amdgcn_s_sleep(2);
          }
          if (stop) break;
          if ((dsc >> 8 & 3) != (unsigned)cw) { ++n; continue; }
        }
        const bool pubn = dyn ? (dsc >> 10 & 1) : (n >= NS - 4 || (n & ~3u) == 16);
        const int base = n >= 20 ? W_P1 + g * P1_STR : W_P + (64 + g) * P_STR, dstr = n >= 20 ? 8192 : 1024, rec = dyn ? dsc & 255 : j, slot = dyn ? dsc >> 12 : 2 + (n >= 20);
        const int gi = dyn ? gid : lane;
        const unsigned sk = segk(n), sv = segv(n);
        int ad = base + rec * 128 + (gi >> 3) * dstr + (gi & 7) * 16, am = lane == 0 ? base + 8 * dstr + rec * 16 : OOB;
        unsigned ka = (sk & 7) * 16384 + kof, va = (sv & 7) * 16384 + vof;
        LAS unsigned char* pb = L + L_PB + (dyn ? 0 : n - 4) * PB_B;
        v4u P0, P1, st;
        float al, Ls, mrec;
        const bool first = freshv;
        if ((mode & 2) && freshv) {
#pragma unroll
          for (int blk = 0; blk < 8; ++blk) acc[blk] = v4f{0.f, 0.f, 0.f, 0.f};
          freshv = false;
        }
        if (mode & 1) {
          const LAS v4u* qp = (const LAS v4u*)(L + L_Q) + r + (n < 20 ? 32 : 48);
          v4u q[4] = {qp[0], qp[4], qp[8], qp[12]};
          if (fresh) { m = -INFINITY; l = 0.f; }
          fresh = pubn;
          asm volatile("" : "+v"(ad), "+v"(am), "+v"(ka), "+v"(va), "+v"(q[0]), "+v"(q[1]), "+v"(q[2]), "+v"(q[3]));
          if (!wait_ge<1>(ctl, C_FULL, 4 * sk + 4)) return;
          v4f s[4];
          v4u K[4][4];
#pragma unroll
          for (int uu = 0; uu < 4; ++uu) read_k(L, ka, uu * 4096, K[uu]);
          __builtin_amdgcn_sched_barrier(0);
#pragma unroll
          for (int uu = 0; uu < 4; ++uu) s[uu] = qk(K[uu], q);
          __builtin_amdgcn_sched_barrier(0);
          lgkm0();
          ctl[C_FREE + (sk & 7)] = sk + 1;
          v4f sc = round2(sel4(mk3, sel4(mk2, s[3], s[2]), sel4(mk2, s[1], s[0])));
          const float mn = mx2(max4to64(max4(sc)) * L2E, m);
          st = *(volatile LAS v4u*)(L + L_CTL);
          sc = expo(sc, mn);
          const unsigned px = pk_bf16(sc[0], sc[1]), py = pk_bf16(sc[2], sc[3]);
          P0 = v4u{bcast16<0>(px), bcast16<0>(py), bcast16<4>(px), bcast16<4>(py)}; P1 = v4u{bcast16<8>(px), bcast16<8>(py), bcast16<12>(px), bcast16<12>(py)};
          al = ex2(m - mn);
          mrec = m = mn;
          l = fmaf(l, al, sum4(sc));
          Ls = l + dpp<0x124>(l);
          Ls += dpp<0x128>(Ls);
          Ls = xsum(Ls);
          if (mode == 1) {
            *(LAS v4u*)(pb + r * 16) = P0; *(LAS v4u*)(pb + 64 + r * 16) = P1; *(LAS v4f*)(pb + 128) = v4f{al, Ls, mn, 0.f};
            n += 4;
            if (n >= NS) { n = 4 + cw; mode = 2; }
            continue;
          }
        } else {
          P0 = *(LAS v4u*)(pb + r * 16); P1 = *(LAS v4u*)(pb + 64 + r * 16);
          const v4f t = *(LAS v4f*)(pb + 128);
          al = t.x; Ls = t.y; mrec = t.z;
          st = *(volatile LAS v4u*)(L + L_CTL);
        }
        if (!first) {
#pragma unroll
          for (int blk = 0; blk < 8; ++blk) acc[blk] *= al;
        }
        asm volatile("" : "+v"(P0), "+v"(P1), "+v"(Ls), "+v"(acc[0]), "+v"(acc[1]), "+v"(acc[2]), "+v"(acc[3]), "+v"(acc[4]), "+v"(acc[5]), "+v"(acc[6]), "+v"(acc[7]));
        v4u V[8];
#define WAITV(need) _Pragma("unroll 1") for (unsigned sp = 0; rfl(st.x) < (need); ++sp) { if (sp > SPIN_CAP) return; if (sp) __builtin_amdgcn_s_sleep(1); st = *(volatile LAS v4u*)(L + L_CTL); } \
        asm volatile("" ::: "memory");
        WAITV(4 * sv + 2)
        read_v(L, va, 0, V);
#pragma unroll
        for (int blk = 0; blk < 8; ++blk) acc[blk] = mfma(V[blk], P0, acc[blk]);
        WAITV(4 * sv + 4)
#undef WAITV
        const unsigned tl = now32(), nend = rfl(st.y);
        read_v(L, va, 8192, V);
        hint_set(ws, lane == 0 && n + 1 == nend ? W_F1 + wg * 128 : OOB, tl, ta);
#pragma unroll
        for (int blk = 0; blk < 8; ++blk) acc[blk] = mfma(V[blk], P1, acc[blk]);
        if (pubn) {
          v2f o[8];
#pragma unroll
          for (int k = 0; k < 8; ++k) o[k] = sel2(mk0, v2f{acc[k][2], acc[k][3]}, v2f{acc[k][0], acc[k][1]});
#pragma unroll
          for (int k = 0; k < 4; ++k) o[k] = sel2(mk3, o[k + 4], o[k]);
#pragma unroll
          for (int k = 0; k < 2; ++k) o[k] = sel2(mk2, o[k + 2], o[k]);
          o[0] = sel2(mk1, o[1], o[0]);
          bool now = dyn && (slot >= NHOLD || rfl(st.z) >= GOAT);
          if (!now) {
            bool emit = true;
            if (!dyn) {
              LAS unsigned char* rp = L + L_REC + (8 + slot * 4) * REC_B;
              *(LAS v2f*)(rp + cw * REC_B + 16 + gid * 8) = o[0];
              if (lane == 0) *(LAS v2f*)(rp + cw * REC_B) = v2f{mrec, Ls};
              emit = last_arriver(L, C_CNT + slot - 1, 4, lane);
              if (emit) o[0] = merge_rec<4>(rp, lane, mrec, Ls);
            }
            if (emit) now = park_rec(L, ctl, slot, base + rec * 128, dstr, base + 8 * dstr + rec * 16, gi, lane, o[0], mrec, Ls);
            if (!now) { ad = OOB; am = OOB; }
          }
          gst(ws, ad, ubits(o[0].x), ubits(o[0].y), ta, tb);
          gst(ws, am, ubits(mrec), ubits(Ls), ta, tb);
          ST(n >= 20 ? 7 : 5);
          freshv = true;
        }
        lgkm0();
        ctl[C_FREE + (sv & 7)] = sv + 1;
        if (n + 4 < NS) n += 4;
        else { n = dyn ? n + 1 : NS; mode = 3; }
      }
      jb = 3; jend = 4;
    } else {
      const int k = w - 3;
      v4u Wf[8], Wr[8];
      const unsigned char* pw = (side ? a.woB : a.woA) + (size_t)((side ? g : 2 * (g & 15) + (g >> 4)) * 128 + (j & 3) * 32) * 1152;
      const __amdgpu_buffer_rsrc_t rw = mkrs(pw, 32 * 1152);
      const bool live = k < 4 || c < 8;
      const int wo = live ? 8 * r * 1152 + k * 256 + c * 16 : OOB;
      const int yreal = live ? W_Y + ((side ? 3 - (r & 1) : (r & 1)) * 36 + k * 8 + (c >> 2)) * YS + prod * 128 + (2 * (c & 3) + (r >> 1)) * 16 : OOB;
      const unsigned mh = bitmask(r, 1);
#define WO_LOAD() _Pragma("unroll") for (int i = 0; i < 8; ++i) Wr[i] = __builtin_amdgcn_raw_buffer_load_b128(rw, wo + i * 1152, 0, 2);
#define WO_PACK() _Pragma("unroll") for (int e = 0; e < 4; ++e) _Pragma("unroll") for (int d = 0; d < 4; ++d) { \
          Wf[2 * e][d] = __builtin_amdgcn_perm(Wr[2 * d + 1][e], Wr[2 * d][e], 0x05040100u); Wf[2 * e + 1][d] = __builtin_amdgcn_perm(Wr[2 * d + 1][e], Wr[2 * d][e], 0x07060302u); } \
        asm volatile("" : "+v"(Wf[0]), "+v"(Wf[1]), "+v"(Wf[2]), "+v"(Wf[3]), "+v"(Wf[4]), "+v"(Wf[5]), "+v"(Wf[6]), "+v"(Wf[7]));
      if (!WOAT) { WO_LOAD() WO_PACK() }
#pragma unroll 1
      for (int pass = DRY && !WOAT ? 0 : 1; pass < 2; ++pass) {
        if (!wait_ge<16>(ctl, C_WANT, pass)) return;
        if (TOUCH && k == 0) {
          const unsigned t0 = __builtin_nontemporal_load((const GAS unsigned*)(pw + (lane & 1) * (31 * 1152))), t1 = __builtin_amdgcn_raw_buffer_load_b32(ws, lane & 1 ? W_SAFE1 : W_SAFE2, 0, SC1);
          asm volatile("" ::"v"(t0), "v"(t1));
        }
        if (!wait_ge<2>(ctl, C_PHASE, pass * GOAT)) return;
        const unsigned held = pass ? rfl(ctl[C_HELD]) : 31u;
        asm volatile("" ::: "memory");
#pragma unroll 1
        for (int slot = k; slot < NHOLD; slot += 5) {
          if (!(held >> slot & 1)) continue;
          LAS unsigned char* h = L + L_HOLD + slot * HOLD_B;
          const v4u hd = *(LAS v4u*)h;
          const v2u ml = *(LAS v2u*)(h + 16), o = *(LAS v2u*)(h + 32 + lane * 8);
          gst(ws, pass ? hd.x + (lane >> 3) * hd.y + (lane & 7) * 16 : OOB, o.x, o.y, ta, tb);
          gst(ws, pass && lane == 0 ? hd.z : OOB, ml.x, ml.y, ta, tb);
        }
        if (w == 4) ST(14);
        if (WOAT) {
          if (!wait_ge<2>(ctl, C_PHASE, WOAT)) return;
          WO_LOAD() WO_PACK()
        }
        int y0 = pass ? yreal : OOB, y1 = y0 + (k < 4 ? 4 : 2) * YS;
        asm volatile("" : "+v"(y0), "+v"(y1));
        v4u oa;
#pragma unroll 1
        for (unsigned sp = 0;; ++sp) {
          const unsigned fl = ctl[C_OSEQ];
          oa = *(volatile LAS v4u*)(L + L_OP + ((c >> 2) & 1) * 64 + r * 16);
          if (rfl(fl) >= (unsigned)pass) break;
          if (sp > SPIN_CAP) return;
          __builtin_amdgcn_s_sleep(1);
        }
#pragma unroll
        for (int b = 0; b < 2; ++b) {
          v4f d[4];
#pragma unroll
          for (int f = 0; f < 4; ++f) d[f] = mfma(oa, Wf[(f >> 1) * 4 + 2 * b + (f & 1)], v4f{0.f, 0.f, 0.f, 0.f});
          gst(ws, b ? y1 : y0, ubits(sel(mh, d[2][0], d[0][0])), ubits(sel(mh, d[3][0], d[1][0])), ta, tb);
        }
      }
      if (w == 4 && side) ST(12);
      return;
    }
  }
#pragma unroll 1
  for (; jb < jend; ++jb) {
    const bool dry = jb < 2;
    unsigned sp = 0;
    if (jb & 1) {
      const int qt = w & 3, c = side * 2 + (prod & 1), rho = prod >> 1;
      if (rho >= 36 && !dry) return;
      int voff = dry ? OOB : W_Y + (c * 36 + rho) * YS + qt * 4096 + lane * 16;
      const int yi = rho < 32 ? (rho >> 2) & 1 : (rho >> 1) & 1, yq = rho < 32 ? rho & 3 : rho & 1;
      int oo = lane < 16 && !dry ? ((c & 1) * 1152 + (c >> 1) * 576 + (rho >> 3) * 128 + (yq * 4 + (lane >> 2)) * 8 + ((lane >> 1) & 1) * 4 + yi * 2 + (lane & 1)) * 2 : OOB;
      asm volatile("" : "+v"(voff), "+v"(oo));
      if (!wait_ge<4>(ctl, C_T1, dry ? 0 : 1)) return;
      if (!wait_ge<1>(ctl, C_T2, dry ? 0 : 1)) return;
      const unsigned until = rfl(ctl[C_T2]) - tstart;
      wait_until(tstart, dry ? 0u : until);
      v4u X[4];
#pragma unroll 1
      for (;; ++sp) {
#pragma unroll
        for (int i = 0; i < 4; ++i) X[i] = gld(ws, voff, i * (FMODE ? 1024 : 4096));
        unsigned bad = 0;
#pragma unroll
        for (int i = 0; i < 4; ++i) bad = gbad(X[i], ta, tb, bad);
        if (__all(bad == 0u) || dry) break;
        if (sp > SPIN_CAP) return;
        __builtin_amdgcn_s_sleep(4);
      }
      float s = rs32((fbits(X[0].x) + fbits(X[1].x)) + (fbits(X[2].x) + fbits(X[3].x)), (fbits(X[0].z) + fbits(X[1].z)) + (fbits(X[2].z) + fbits(X[3].z)));
      s = rs16(s, s);
      s += dpp<0x128>(s);
      *(volatile LAS unsigned*)(L + L_FP + (((lane & 7) * 2 + (lane >> 5)) * 4 + qt) * 4) = dry ? ~0u : ubits(s);
      if (qt != 0 && !dry) return;
#pragma unroll 1
      for (unsigned s2 = 0;; ++s2) {
        const v4u p = *(volatile LAS v4u*)(L + L_FP + (lane & 15) * 16);
        s = (fbits(p.x) + fbits(p.y)) + (fbits(p.z) + fbits(p.w));
        if (__all(p.y != ~0u && p.z != ~0u && p.w != ~0u) || dry || s2 > SPIN_CAP) break;
      }
      __builtin_amdgcn_raw_buffer_store_b16((unsigned short)pk_bf16(s, s), mkrs(a.out, 4608), oo, 0, 0);
      __builtin_amdgcn_raw_buffer_store_b64(v2u{ta, tb}, ws, c == 3 && rho == 0 && qt == 0 && lane == 0 && !dry ? W_X : OOB, 0, SC1);
      if (!dry && qt == 0) ST(c == 3 ? 13 : 6);
    } else {
      const int nr1 = side ? 56 : 8;
      const int ta_ = (g & 15) * 2 + (g >> 4);
      const int b0 = side ? W_P1 + g * P1_STR : W_P + ta_ * P_STR, b1 = W_P + (side ? 64 + g : 32 + ta_) * P_STR, d0 = side ? 8192 : 1024, dest = 2 * (j & 3);
      int voff = b0 + dest * d0 + lane * 16;
      int voff7 = b1 + dest * 1024 + lane * 16;
      int voff8 = lane >= 56 ? b1 + 8192 + (lane - 56) * 16 : b0 + 8 * d0 + (lane < nr1 ? lane : 0) * 16;
      if (dry) { voff = OOB; voff7 = OOB; voff8 = OOB; }
      int voffb = voff + d0;
      asm volatile("" : "+v"(voff), "+v"(voffb), "+v"(voff7), "+v"(voff8));
      if (!wait_ge<16>(ctl, C_WANT, dry ? 0 : 1)) return;
      if (!wait_ge<2>(ctl, C_PHASE, dry ? 0 : POLLAT)) return;
      const int hoff = side ? (lane < 8 ? W_F1 + (g * 8 + lane) * 128 : OOB) : (lane < 16 ? W_F1 + ((g & 15) * 8 + (lane >> 3) * 128 + (lane & 7)) * 128 : OOB);
      const unsigned until = hint_time<false>(ws, dry ? OOB : hoff, OOB, ta, tstart) + DT1;
      wait_until(tstart, dry ? 0u : until);
      v4u X[2][8], ML;
#pragma unroll 1
      for (;; ++sp) {
        const unsigned tr = now32();
        ML = gld(ws, voff8);
#pragma unroll
        for (int i = 0; i < 7; ++i) { X[0][i] = gld(ws, voff, side ? i * 1024 : 0); X[1][i] = gld(ws, voffb, side ? i * 1024 : 0); }
        X[0][7] = gld(ws, voff7); X[1][7] = gld(ws, voff7 + 1024);
        if (!dry) ctl[C_T1] = tr | 1u;
        __builtin_amdgcn_sched_barrier(0);
        unsigned bad = gbad(ML, ta, tb, 0u);
#pragma unroll
        for (int i = 0; i < 8; ++i) bad = gbad(X[1][i], ta, tb, gbad(X[0][i], ta, tb, bad));
        if (__all(bad == 0u) || dry) break;
        if (sp > SPIN_CAP) return;
        __builtin_amdgcn_s_sleep(4);
      }
      const bool seg1 = lane >= 56;
      const float mr = fbits(ML.x);
      const float M = max8(mr);
      const float M0 = max8to64(seg1 ? -INFINITY : M);
      const float f = (lane >= nr1 && !seg1) ? 0.f : ex2(mr - (seg1 ? M : M0));
      float fb[8];
#pragma unroll
      for (int b = 0; b < 8; ++b) fb[b] = fbits(__builtin_amdgcn_ds_bpermute((8 * b + (lane >> 3)) * 4, ubits(f)));
      float Ls = f * fbits(ML.z);
      Ls += dpp<0xB1>(Ls); Ls += dpp<0x4E>(Ls); Ls += dpp<0x141>(Ls);
      const float L1 = fbits(__builtin_amdgcn_readlane(ubits(Ls), 56));
      float L0 = seg1 ? 0.f : Ls;
      L0 += dpp<0x128>(L0); L0 = xsum(L0);
      float inv = __builtin_amdgcn_rcpf(lane >= 32 ? L1 : L0);
      asm volatile("" : "+v"(inv));
      float oe[2], oo[2];
#pragma unroll
      for (int h = 0; h < 2; ++h) {
        float o0 = 0.f, o1 = 0.f;
#pragma unroll
        for (int b = 0; b < 7; ++b) { o0 = fmaf(fb[b], fbits(X[h][b].x), o0); o1 = fmaf(fb[b], fbits(X[h][b].z), o1); }
        oe[h] = rs32(o0, fb[7] * fbits(X[h][7].x)); oo[h] = rs32(o1, fb[7] * fbits(X[h][7].z));
      }
      float e = rs16(oe[0], oe[1]), o = rs16(oo[0], oo[1]);
      e += dpp<0x128>(e); o += dpp<0x128>(o);
      *(volatile LAS unsigned*)(L + L_OP + (lane >> 4) * 32 + (lane & 7) * 4) = pk_bf16(e * inv, o * inv);
      ctl[C_OSEQ] = dry ? 0u : 1u;
    }
  }
}

extern "C" void kernel_launch(void* const* d_in, const int* in_sizes, int n_in, void* d_out, int out_size, void* d_ws, size_t ws_size,
                              hipStream_t stream) {
  Args a;
  a.xA = (const unsigned char*)d_in[0]; a.kA = (const unsigned char*)d_in[1]; a.vA = (const unsigned char*)d_in[2];
  a.wqA = (const unsigned char*)d_in[3]; a.woA = (const unsigned char*)d_in[4];
  a.xB = (const unsigned char*)d_in[5]; a.kB = (const unsigned char*)d_in[6]; a.vB = (const unsigned char*)d_in[7];
  a.wqB = (const unsigned char*)d_in[8]; a.woB = (const unsigned char*)d_in[9];
  a.out = (unsigned char*)d_out; a.ws = (unsigned char*)d_ws;
  fused_decode<<<256, NTHR, 0, stream>>>(a);
}
```

```cpp
#include <hip/hip_runtime.h>
#include <stdint.h>

typedef unsigned v4u __attribute__((ext_vector_type(4)));
typedef unsigned v2u __attribute__((ext_vector_type(2)));
typedef float v4f __attribute__((ext_vector_type(4)));
typedef float v2f __attribute__((ext_vector_type(2)));
typedef short v4s __attribute__((ext_vector_type(4)));
typedef __bf16 bf16x2 __attribute__((ext_vector_type(2)));
typedef __bf16 bf16x8 __attribute__((ext_vector_type(8)));
#define LAS __attribute__((address_space(3)))
#define GAS __attribute__((address_space(1)))
#define DI __device__ __forceinline__

constexpr int WLD = 22;
constexpr int DT1 = 50;
constexpr int DT2 = 100;
constexpr int NTHR = 12 * 64;
constexpr int NSTAT = 9;
constexpr int NS = 4 + 16 + NSTAT;
constexpr int POOLC = (16 - NSTAT) * 8;
constexpr int NT = 48, TP = POOLC - NT;
constexpr int WINE = (WLD + 7) / 8 * 2;
constexpr int NREP = 16;
DI unsigned segk(unsigned n) { return n < NS ? n + 4 : 2 * n - 1; }
DI unsigned segv(unsigned n) { return n + 1 < NS ? n + NS : 2 * n + 2; }
constexpr unsigned SPIN_CAP = 1u << 20;
constexpr int NHOLD = 16, HOLD_B = 544, REC_B = 528, PB_B = 144;
constexpr int L_CTL = 131072  , L_FP = L_CTL + 768  , L_Q = L_CTL + 1024, L_OP = L_Q + 1024, L_QP = L_OP + 128,
              L_REC = L_QP + 9 * 256, L_HOLD = L_REC + 24 * REC_B, L_PB = L_HOLD + NHOLD * HOLD_B, L_END = L_PB + (NS - 4) * PB_B;
static_assert(L_END <= 163840 && TP >= 0 && TP <= NT && NT + 2 <= 64 && POOLC <= 64 && WINE >= 2 && WINE <= 8, "");
enum { C_FULL = 0  , C_END = 1  , C_PHASE = 2  , C_QRDY = 3, C_WANT = 4  ,
       C_T1 = 5  , C_TAGA = 6, C_TAGB = 7, C_FREE = 8  , C_CNT = 16  , C_QCNT = 19, C_HELD = 21, C_OSEQ = 22,
       C_T2 = 24  , C_TICK = 64, C_DESC = 128 };
constexpr int W_X = 0, W_CTR = 4096, W_F1 = 16384  , W_F2 = W_F1 + 32768  , W_QA = W_F2 + 2 * NREP * 1024, W_QB = W_QA + 32768,
              W_P = W_QB + 32768, P_STR = 9216  , W_P1 = W_P + 96 * P_STR, P1_STR = 66560  ,
              W_Y = W_P1 + 32 * P1_STR, YS = 16384, W_ENDB = 256 + W_Y + 4 * 36 * YS  ;
constexpr int W_SAFE1 = W_P1 + 24 * P1_STR + 7424, W_SAFE2 = W_ENDB - 128;
constexpr float SCALE = 0.088388347648318440f, L2E = 1.4426950408889634f;
constexpr int SC1 = 16, OOB = 0x70000000;

struct Args {
  const unsigned char *xA, *kA, *vA, *wqA, *woA, *xB, *kB, *vB, *wqB, *woB;
  unsigned char *out, *ws;
};

DI float dot2(unsigned a, unsigned b, float c) { return __builtin_amdgcn_fdot2_f32_bf16(__builtin_bit_cast(bf16x2, a), __builtin_bit_cast(bf16x2, b), c, false); }
template <int CTRL> DI float dpp(float x) { return __builtin_bit_cast(float, __builtin_amdgcn_mov_dpp(__builtin_bit_cast(int, x), CTRL, 0xf, 0xf, true)); }
template <int LANE> DI unsigned bcast16(unsigned x) { return __builtin_amdgcn_mov_dpp(x, 0x150 + LANE, 0xf, 0xf, true); }
DI unsigned pk_bf16(float lo, float hi) {
  unsigned r = __builtin_bit_cast(unsigned, bf16x2{(__bf16)lo, (__bf16)hi});
  asm("" : "+v"(r));
  return r;
}
DI float bf_lo(unsigned u) { return __builtin_bit_cast(float, u << 16); }
DI float bf_hi(unsigned u) { return __builtin_bit_cast(float, u & 0xffff0000u); }
DI float fbits(unsigned u) { return __builtin_bit_cast(float, u); }
DI unsigned ubits(float f) { return __builtin_bit_cast(unsigned, f); }
DI unsigned rfl(unsigned x) { return __builtin_amdgcn_readfirstlane(x); }
DI float ex2(float x) { return __builtin_amdgcn_exp2f(x); }
DI v4f mfma(v4u a, v4u b, v4f c) { return __builtin_amdgcn_mfma_f32_16x16x32_bf16(__builtin_bit_cast(bf16x8, a), __builtin_bit_cast(bf16x8, b), c, 0, 0, 0); }
DI v2u trd(LAS unsigned char* p) { return __builtin_bit_cast(v2u, __builtin_amdgcn_ds_read_tr16_b64_v4i16((LAS v4s*)p)); }
DI float rs32(float x, float y) { auto s = __builtin_amdgcn_permlane32_swap(ubits(x), ubits(y), false, false); return fbits(s[0]) + fbits(s[1]); }
DI float rs16(float x, float y) { auto s = __builtin_amdgcn_permlane16_swap(ubits(x), ubits(y), false, false); return fbits(s[0]) + fbits(s[1]); }
DI float xsum(float x) { x = rs16(x, x); return rs32(x, x); }
DI float xmax(float x) {
  auto s = __builtin_amdgcn_permlane16_swap(ubits(x), ubits(x), false, false); x = fmaxf(fbits(s[0]), fbits(s[1]));
  auto t = __builtin_amdgcn_permlane32_swap(ubits(x), ubits(x), false, false); return fmaxf(fbits(t[0]), fbits(t[1]));
}
DI float max8(float x) {
  asm("s_nop 1\n\tv_max_f32_dpp %0, %0, %0 quad_perm:[1,0,3,2] row_mask:0xf bank_mask:0xf\n\ts_nop 1\n\tv_max_f32_dpp %0, %0, %0 quad_perm:[2,3,0,1] row_mask:0xf bank_mask:0xf\n\t"
      "s_nop 1\n\tv_max_f32_dpp %0, %0, %0 row_half_mirror row_mask:0xf bank_mask:0xf" : "+v"(x));
  return x;
}
DI float max8to64(float x) {
  float t;
  asm("s_nop 1\n\tv_max_f32_dpp %0, %0, %0 row_ror:8 row_mask:0xf bank_mask:0xf\n\tv_mov_b32 %1, %0\n\ts_nop 1\n\tv_permlane16_swap_b32 %0, %1\n\tv_max_f32 %0, %0, %1\n\t"
      "v_mov_b32 %1, %0\n\ts_nop 1\n\tv_permlane32_swap_b32 %0, %1\n\tv_max_f32 %0, %0, %1" : "+v"(x), "=&v"(t));
  return x;
}
DI float max4to64(float x) {
  asm("s_nop 1\n\tv_max_f32_dpp %0, %0, %0 row_ror:4 row_mask:0xf bank_mask:0xf" : "+v"(x));
  return max8to64(x);
}
DI void lgkm0() { asm volatile("s_waitcnt lgkmcnt(0)" ::: "memory"); }
template <int N> DI void waitvm() { asm volatile("s_waitcnt vmcnt(%0)" ::"n"(N) : "memory"); }
DI __amdgpu_buffer_rsrc_t mkrs(const void* p, unsigned bytes) { return __builtin_amdgcn_make_buffer_rsrc((void*)p, 0, bytes, 0x00020000); }
DI void dma8(__amdgpu_buffer_rsrc_t rs, unsigned voff, unsigned soff, unsigned lds) {
  unsigned keep, s2;
  asm volatile("s_mov_b32 %0, m0\n\ts_mov_b32 m0, %5\n\ts_add_u32 %1, %4, 0x1000\n\ts_nop 2\n\t"
               "buffer_load_dwordx4 %2, %3, %4 offen nt lds\n\t"
               "buffer_load_dwordx4 %2, %3, %4 offen offset:1024 nt lds\n\t"
               "buffer_load_dwordx4 %2, %3, %4 offen offset:2048 nt lds\n\t"
               "buffer_load_dwordx4 %2, %3, %4 offen offset:3072 nt lds\n\t"
               "s_add_u32 m0, m0, 0x1000\n\ts_nop 0\n\t"
               "buffer_load_dwordx4 %2, %3, %1 offen nt lds\n\t"
               "buffer_load_dwordx4 %2, %3, %1 offen offset:1024 nt lds\n\t"
               "buffer_load_dwordx4 %2, %3, %1 offen offset:2048 nt lds\n\t"
               "buffer_load_dwordx4 %2, %3, %1 offen offset:3072 nt lds\n\t"
               "s_mov_b32 m0, %0"
               : "=&s"(keep), "=&s"(s2) : "v"(voff), "s"(rs), "s"(soff), "s"(lds) : "memory", "scc");
}
DI v4u gld(__amdgpu_buffer_rsrc_t rs, int off, int soff = 0) { return __builtin_amdgcn_raw_buffer_load_b128(rs, off, soff, SC1); }
DI void gst(__amdgpu_buffer_rsrc_t rs, int off, unsigned a, unsigned b, unsigned ta, unsigned tb) { __builtin_amdgcn_raw_buffer_store_b128(v4u{a, ta, b, tb}, rs, off, 0, SC1); }
DI unsigned gbad(v4u g, unsigned ta, unsigned tb, unsigned acc) { return __builtin_amdgcn_bitop3_b32(g.w, tb, __builtin_amdgcn_bitop3_b32(g.y, ta, acc, 0xBE), 0xBE); }

typedef volatile LAS unsigned* ctl_t;
template <int SLP> DI bool wait_ge(ctl_t ctl, int idx, unsigned v) {
#pragma unroll 1
  for (unsigned s = 0; rfl(ctl[idx]) < v; ++s) {
    if (s > SPIN_CAP) return false;
    __builtin_amdgcn_s_sleep(SLP);
  }
  asm volatile("" ::: "memory");
  return true;
}
DI void lds_inc(LAS unsigned char* L, int idx, int lane) {
  lgkm0();
  if (lane == 0) __hip_atomic_fetch_add((LAS unsigned*)(L + L_CTL) + idx, 1u, __ATOMIC_RELAXED, __HIP_MEMORY_SCOPE_WORKGROUP);
}
DI bool last_arriver(LAS unsigned char* L, int idx, unsigned n, int lane) {
  lgkm0();
  unsigned old = 0;
  if (lane == 0) old = __hip_atomic_fetch_add((LAS unsigned*)(L + L_CTL) + idx, 1u, __ATOMIC_RELAXED, __HIP_MEMORY_SCOPE_WORKGROUP);
  old = rfl(old);
  asm volatile("" ::: "memory");
  return old == n - 1;
}
DI void read_k(LAS unsigned char* L, unsigned off, int imm, v4u (&K)[4]) {
#pragma unroll
  for (int kk = 0; kk < 4; ++kk) K[kk] = *(const LAS v4u*)(L + (off ^ (kk * 64)) + imm);
}
DI void read_v(LAS unsigned char* L, unsigned off, int imm, v4u (&V)[8]) {
#pragma unroll
  for (int blk = 0; blk < 8; ++blk) {
    LAS unsigned char* p = L + (off ^ ((blk >> 1) * 64)) + (blk & 1) * 32 + imm;
    const v2u x = trd(p), y = trd(p + 4096);
    V[blk] = v4u{x.x, x.y, y.x, y.y};
  }
  __builtin_amdgcn_sched_barrier(0);
}
DI v4f qk(const v4u (&K)[4], const v4u (&q)[4]) {
  v4f d = {0.f, 0.f, 0.f, 0.f};
#pragma unroll
  for (int kk = 0; kk < 4; ++kk) d = mfma(K[kk], q[kk], d);
  return d;
}
DI v4f round2(v4f d) {
  const unsigned x01 = pk_bf16(d[0], d[1]), x23 = pk_bf16(d[2], d[3]);
  const unsigned b01 = pk_bf16(bf_lo(x01) * SCALE, bf_hi(x01) * SCALE), b23 = pk_bf16(bf_lo(x23) * SCALE, bf_hi(x23) * SCALE);
  return v4f{bf_lo(b01), bf_hi(b01), bf_lo(b23), bf_hi(b23)};
}
DI float sel(unsigned mask, float a, float b) { return fbits(__builtin_amdgcn_bitop3_b32(mask, ubits(a), ubits(b), 0xCA)); }
DI v4f sel4(unsigned mask, v4f a, v4f b) { return v4f{sel(mask, a[0], b[0]), sel(mask, a[1], b[1]), sel(mask, a[2], b[2]), sel(mask, a[3], b[3])}; }
DI v2f sel2(unsigned mask, v2f a, v2f b) { return v2f{sel(mask, a[0], b[0]), sel(mask, a[1], b[1])}; }
DI unsigned bitmask(int x, int bit) { unsigned m = (unsigned)((x << (31 - bit)) >> 31); asm("" : "+v"(m)); return m; }
DI float max4(v4f d) { return fmaxf(fmaxf(d[0], d[1]), fmaxf(d[2], d[3])); }
DI float sum4(v4f d) { return (d[0] + d[1]) + (d[2] + d[3]); }
DI v4f expo(v4f s, float m2) { return v4f{ex2(fmaf(s[0], L2E, -m2)), ex2(fmaf(s[1], L2E, -m2)), ex2(fmaf(s[2], L2E, -m2)), ex2(fmaf(s[3], L2E, -m2))}; }

DI unsigned now32() { return (unsigned)__builtin_amdgcn_s_memrealtime(); }
DI void wait_until(unsigned t0, unsigned until) {
#pragma unroll 1
  for (unsigned sp = 0; sp < 64 && now32() - t0 < until; ++sp) { }
}
DI void hint_set(__amdgpu_buffer_rsrc_t ws, int off, unsigned t, unsigned tag) {
  asm volatile("buffer_atomic_swap_x2 %0, %1, %2, 0 offen" ::"v"(v2u{t, tag}), "v"(off), "s"(ws) : "memory");
}
template <bool TWO> DI unsigned hint_time(__amdgpu_buffer_rsrc_t ws, int off, int off2, unsigned tag, unsigned t0) {
  unsigned rel = 0;
#define HP1(A0, A1, o) asm volatile("v_mov_b32 v16" #A0 ", 0\n\tv_mov_b32 v16" #A1 ", 0\n\tbuffer_atomic_add_x2 v[16" #A0 ":16" #A1 "], %0, %1, 0 offen sc0" \
                                    ::"v"(o), "s"(ws) : "memory", "v16" #A0, "v16" #A1)
#define HC1(A0, A1, o, N) { unsigned t_, g_; asm volatile("s_waitcnt vmcnt(%2)\n\tv_mov_b32 %0, v16" #A0 "\n\tv_mov_b32 %1, v16" #A1 : "=v"(t_), "=v"(g_) : "n"(N) : "memory", "v16" #A0, "v16" #A1); \
                            const bool ok_ = g_ == tag; rel = ok_ ? max(rel, t_ - t0) : rel; o = ok_ ? OOB : o; }
  HP1(0, 1, off);
  if (TWO) HP1(2, 3, off2);
  __builtin_amdgcn_s_sleep(8);
#pragma unroll 1
  for (unsigned sp = 0; sp < 4096; ++sp) {
    HP1(4, 5, off);
    if (TWO) HP1(6, 7, off2);
    HC1(0, 1, off, TWO ? 3 : 1)
    if (TWO) HC1(2, 3, off2, 2)
    if (__all(off == OOB && off2 == OOB)) break;
    HP1(0, 1, off);
    if (TWO) HP1(2, 3, off2);
    HC1(4, 5, off, TWO ? 3 : 1)
    if (TWO) HC1(6, 7, off2, 2)
    if (__all(off == OOB && off2 == OOB)) break;
  }
  return rfl((unsigned)max8to64(max8((float)rel)));
}
#undef HP1
#undef HC1
DI bool park_rec(LAS unsigned char* L, ctl_t ctl, int slot, int adr, int dstr, int mladr, int gi, int lane, v2f o, float m, float l) {
  LAS unsigned char* h = L + L_HOLD + slot * HOLD_B;
  *(LAS v2f*)(h + 32 + gi * 8) = o;
  if (lane == 0) { *(LAS v4u*)h = v4u{(unsigned)adr, (unsigned)dstr, (unsigned)mladr, 0u}; *(LAS v2f*)(h + 16) = v2f{m, l}; }
  lgkm0();
  if (lane == 0) __hip_atomic_fetch_or((LAS unsigned*)(L + L_CTL) + C_HELD, 1u << slot, __ATOMIC_RELAXED, __HIP_MEMORY_SCOPE_WORKGROUP);
  lgkm0();
  return rfl(ctl[C_PHASE]) != 0;
}
template <int NR> DI v2f merge_rec(LAS unsigned char* rec, int lane, float& Mo, float& Lo) {
  v2f av[NR];
  const v2f ml = *(LAS v2f*)(rec + (lane & (NR - 1)) * REC_B);
#pragma unroll
  for (int w = 0; w < NR; ++w) av[w] = *(LAS v2f*)(rec + w * REC_B + 16 + lane * 8);
  const float M = max8(ml.x), f = ex2(ml.x - M);
  float Ls = f * ml.y; Ls += dpp<0xB1>(Ls); Ls += dpp<0x4E>(Ls);
  if (NR == 8) Ls += dpp<0x141>(Ls);
  float a0 = 0.f, a1 = 0.f;
#pragma unroll
  for (int w = 0; w < NR; ++w) { const float fw = fbits(__builtin_amdgcn_readlane(ubits(f), w)); a0 = fmaf(fw, av[w].x, a0); a1 = fmaf(fw, av[w].y, a1); }
  Mo = M; Lo = Ls;
  return v2f{a0, a1};
}

__global__ void __launch_bounds__(NTHR, 3) fused_decode(Args a) {
  __shared__ __attribute__((aligned(16))) unsigned char smem[L_END];
  LAS unsigned char* L = (LAS unsigned char*)smem;
  ctl_t ctl = (ctl_t)(L + L_CTL);
  asm volatile("" ::"s"(a.xA), "s"(a.kA), "s"(a.vA), "s"(a.wqA), "s"(a.woA), "s"(a.xB), "s"(a.kB), "s"(a.vB), "s"(a.wqB), "s"(a.woB), "s"(a.out), "s"(a.ws));
  const int wg = blockIdx.x, g = wg >> 3, j = wg & 7;
  const int lane = threadIdx.x & 63, w = rfl(threadIdx.x >> 6);

  if (w == 0) {
    __builtin_amdgcn_s_waitcnt(0);
    const unsigned lb = (unsigned)(uintptr_t)L;
    const unsigned voff = (lane * 16) ^ ((lane >> 4) << 6);
    const unsigned offA = g * (2u << 20) + (3u << 19) + j * 16384;
    const __amdgpu_buffer_rsrc_t rk = mkrs(a.kA, 64u << 20);
    dma8(rk, voff, offA, lb); dma8(rk, voff, offA + 8192, lb + 8192);
    asm volatile("s_barrier" ::: "memory");
    __builtin_amdgcn_s_setprio(3);
    int u = 4, pub = 0;
    unsigned sg = 1;
#define LWAIT(idx, v, out) _Pragma("unroll 1") for (unsigned sp = 0;; ++sp) { \
      out = rfl(ctl[idx]); if (out + 1u > (v)) break; \
      if (sp > SPIN_CAP) return; \
      if (sp == 6) { waitvm<0>(); pub = u; ctl[C_FULL] = pub; } \
      __builtin_amdgcn_s_sleep(1); }
#define HALF(rs, so, ld) do { waitvm<WLD>(); if (u - WINE > pub) { pub = u - WINE; ctl[C_FULL] = pub; } dma8(rs, voff, so, ld); u += 2; } while (0)
#define SEG(rs, so) do { unsigned fr_; LWAIT(C_FREE + (sg & 7), sg < 8 ? 0u : sg < 16 ? 2u : sg - 7, fr_) \
      const unsigned ld_ = lb + (sg & 7) * 16384; HALF(rs, so, ld_); HALF(rs, (so) + 8192, ld_ + 8192); ++sg; } while (0)
#pragma unroll 1
    for (int run = 0; run < 4; ++run) {
      const __amdgpu_buffer_rsrc_t rs = mkrs(run == 0 ? a.kA : run == 1 ? a.vA : run == 2 ? a.kB : a.vB, run < 2 ? 64u << 20 : 128u << 20);
      const int cnt = run < 2 ? 4 : NS - 2 - run;
#pragma unroll 1
      for (int i = run == 0; i < cnt; ++i) {
        const unsigned so = run < 2 ? offA + i * 131072 : (g + (i & 16) * 2) * (2u << 20) + ((i * 8 + wg) & 127) * 16384;
        if (sg + 4 == 2 * NS) ctl[C_WANT] = 1;
        SEG(rs, so);
      }
    }
    const __amdgpu_buffer_rsrc_t rkb = mkrs(a.kB, 128u << 20), rvb = mkrs(a.vB, 128u << 20);
    const unsigned b1 = (32 + g) * (2u << 20);
    unsigned sv = b1 + (((NS - 5) * 8 + wg) & 127) * 16384;
    unsigned k = 0, cid = 0, dsc = 0, want = 0;
    bool second = false;
#pragma unroll 1
    for (unsigned n = NS;; ++n) {
      unsigned t = 0;
      if (!second) { LWAIT(C_TICK + k, 0u, t) }
      const bool last = t >= (unsigned)NT;
      unsigned sk = 0;
      if (!last) {
        if (second) { ++cid; dsc |= 1u << 10; second = false; }
        else {
          second = t < (unsigned)TP;
          cid = second ? 2 * t : TP + t;
          dsc = (8 + t) | ((NS + k) & 3) << 8 | (second ? 0u : 1u << 10) | (4 + k) << 12;
          ++k;
        }
        ctl[C_DESC + n - NS] = dsc;
        sk = b1 + ((NSTAT * 8 + cid + g * 8) & 127) * 16384;
        if (cid >= 2 * TP) ctl[C_WANT] = want = max(want, k + 1);
        SEG(rkb, sk);
        if (cid < 2 * TP) ctl[C_WANT] = want = second || cid + 16 < 2 * TP ? k + 1 : k + 2;
      } else {
        ctl[C_END] = n; ctl[C_PHASE] = 1; ++sg;
      }
      SEG(rvb, sv);
      if (last) break;
      sv = sk;
    }
    ctl[C_PHASE] = 2;
#define DRAIN(k_) if (WINE >= k_) { waitvm<4 * k_>(); const int p_ = u - k_ + (k_ < 4 ? 4 : 0); if (p_ > pub) { pub = p_; ctl[C_FULL] = pub; } }
    DRAIN(8) DRAIN(6) DRAIN(4) DRAIN(2) DRAIN(0)
    return;
  }
  const __amdgpu_buffer_rsrc_t ws = mkrs(a.ws, W_ENDB);
  const unsigned tstart = now32();
  const int cb = (wg & ~15) + ((wg & 7) << 1) + ((wg >> 3) & 1);
  const int side = j >> 2, prod = g * 4 + (j & 3);

  if (w == 1) {
    const v2u ep = __builtin_amdgcn_raw_buffer_load_b64(ws, W_X, 0, SC1);
    if (cb >= 128 && (cb & 3) == 0 && lane == 0) __builtin_amdgcn_raw_buffer_store_b32(0u, ws, W_CTR + ((cb - 128) >> 2) * 256, 0, SC1);
    ctl[lane] = lane == C_END ? 0x7fffffffu : 0u;
    ctl[lane + 64] = 0xffffffffu; ctl[lane + 128] = 0xffffffffu; ctl[lane + 192] = 0xffffffffu;
    asm volatile("s_waitcnt lgkmcnt(0)\n\ts_barrier" ::: "memory");
    unsigned ta = rfl(ep.x) + 1u, tb = rfl(ep.y) + 1u;
    ta += ta == 0u; tb += tb == 0u;
    waitvm<0>();
    if (!wait_ge<1>(ctl, C_QCNT, 9)) return;
    if (lane < 16) {
      v4f s = {0.f, 0.f, 0.f, 0.f};
#pragma unroll
      for (int i = 0; i < 9; ++i) s += *(LAS v4f*)(L + L_QP + i * 256 + lane * 16);
      const int b = lane >> 3, quad = lane & 7;
      const int off = cb < 128 ? W_QA + ((b * 16 + (cb >> 3)) * 64 + (cb & 7) * 8 + quad) * 16 : W_QB + (((cb - 128) >> 2) * 64 + b * 32 + (cb & 3) * 8 + quad) * 16;
      gst(ws, off, pk_bf16(s.x, s.y), pk_bf16(s.z, s.w), ta, tb);
    }
    v4u qa, qb;
#pragma unroll 1
    for (unsigned sp = 0;; ++sp) {
      __builtin_amdgcn_s_sleep(8);
      qa = gld(ws, W_QA + g * 1024 + lane * 16);
      qb = gld(ws, W_QB + g * 1024 + lane * 16);
      if (__all(gbad(qb, ta, tb, gbad(qa, ta, tb, 0u)) == 0u)) break;
      if (sp > SPIN_CAP) return;
    }
    *(LAS v2u*)(L + L_Q + lane * 8) = v2u{qa.x, qa.z};
    *(LAS v2u*)(L + L_Q + 512 + lane * 8) = v2u{qb.x, qb.z};
    ctl[C_TAGA] = ta; ctl[C_TAGB] = tb;
    lgkm0();
    ctl[C_QRDY] = 1;
#pragma unroll 1
    for (int k = 0; k <= NT; ++k) {
      if (!wait_ge<2>(ctl, C_WANT, k + 1)) return;
      unsigned t = 0;
      if (lane == 0) t = __hip_atomic_fetch_add((GAS unsigned*)(a.ws + W_CTR + g * 256), 1u, __ATOMIC_RELAXED, __HIP_MEMORY_SCOPE_AGENT);
      t = rfl(t);
      ctl[C_TICK + k] = t;
      if (t >= (unsigned)NT) break;
    }
    if (!wait_ge<1>(ctl, C_T1, 1)) return;
    hint_set(ws, lane < NREP ? W_F2 + (side * NREP + lane) * 1024 + prod * 8 : OOB, ctl[C_T1], ta);
    if (prod < 72) {
      const int ho = W_F2 + (side * NREP + prod % NREP) * 1024 + lane * 8;
      ctl[C_T2] = (hint_time<true>(ws, ho, ho + 512, ta, tstart) + tstart + DT2) | 1u;
    }
    return;
  }
  if (w == 2) {
    asm volatile("s_barrier" ::: "memory");
    const int nr1 = side ? 56 : 8;
    const int ta_ = (g & 15) * 2 + (g >> 4);
    const int b0 = side ? W_P1 + g * P1_STR : W_P + ta_ * P_STR, b1 = W_P + (side ? 64 + g : 32 + ta_) * P_STR, d0 = side ? 8192 : 1024, dest = 2 * (j & 3);
    int voff = b0 + dest * d0 + lane * 16;
    int voffb = voff + d0;
    int voff7 = b1 + dest * 1024 + lane * 16;
    int voff8 = lane >= 56 ? b1 + 8192 + (lane - 56) * 16 : b0 + 8 * d0 + (lane < nr1 ? lane : 0) * 16;
    asm volatile("" : "+v"(voff), "+v"(voffb), "+v"(voff7), "+v"(voff8));
    if (!wait_ge<16>(ctl, C_WANT, 1)) return;
    if (!wait_ge<2>(ctl, C_PHASE, 2)) return;
    const unsigned ta = rfl(ctl[C_TAGA]), tb = rfl(ctl[C_TAGB]);
    const int hoff = side ? (lane < 8 ? W_F1 + (g * 8 + lane) * 128 : OOB) : (lane < 16 ? W_F1 + ((g & 15) * 8 + (lane >> 3) * 128 + (lane & 7)) * 128 : OOB);
    wait_until(tstart, hint_time<false>(ws, hoff, OOB, ta, tstart) + DT1);
    v4u X[2][8], ML;
#pragma unroll 1
    for (unsigned sp = 0;; ++sp) {
      const unsigned tr = now32();
      ML = gld(ws, voff8);
#pragma unroll
      for (int i = 0; i < 7; ++i) { X[0][i] = gld(ws, voff, side ? i * 1024 : 0); X[1][i] = gld(ws, voffb, side ? i * 1024 : 0); }
      X[0][7] = gld(ws, voff7); X[1][7] = gld(ws, voff7 + 1024);
      ctl[C_T1] = tr | 1u;
      __builtin_amdgcn_sched_barrier(0);
      const bool seg1 = lane >= 56;
      const float mr = fbits(ML.x);
      const float M = max8(mr);
      const float M0 = max8to64(seg1 ? -INFINITY : M);
      const float f = (lane >= nr1 && !seg1) ? 0.f : ex2(mr - (seg1 ? M : M0));
      float fb[8];
#pragma unroll
      for (int b = 0; b < 8; ++b) fb[b] = fbits(__builtin_amdgcn_ds_bpermute((8 * b + (lane >> 3)) * 4, ubits(f)));
      float Ls = f * fbits(ML.z);
      Ls += dpp<0xB1>(Ls); Ls += dpp<0x4E>(Ls); Ls += dpp<0x141>(Ls);
      const float L1 = fbits(__builtin_amdgcn_readlane(ubits(Ls), 56));
      float L0 = seg1 ? 0.f : Ls;
      L0 += dpp<0x128>(L0); L0 = xsum(L0);
      const float inv = __builtin_amdgcn_rcpf(lane >= 32 ? L1 : L0);
      __builtin_amdgcn_sched_barrier(0);
      unsigned bad = gbad(ML, ta, tb, 0u);
#pragma unroll
      for (int i = 0; i < 8; ++i) bad = gbad(X[1][i], ta, tb, gbad(X[0][i], ta, tb, bad));
      float oe[2], oo[2];
#pragma unroll
      for (int h = 0; h < 2; ++h) {
        float o0 = 0.f, o1 = 0.f;
#pragma unroll
        for (int b = 0; b < 7; ++b) { o0 = fmaf(fb[b], fbits(X[h][b].x), o0); o1 = fmaf(fb[b], fbits(X[h][b].z), o1); }
        oe[h] = rs32(o0, fb[7] * fbits(X[h][7].x)); oo[h] = rs32(o1, fb[7] * fbits(X[h][7].z));
      }
      float e = rs16(oe[0], oe[1]), o = rs16(oo[0], oo[1]);
      e += dpp<0x128>(e); o += dpp<0x128>(o);
      *(volatile LAS unsigned*)(L + L_OP + (lane >> 4) * 32 + (lane & 7) * 4) = pk_bf16(e * inv, o * inv);
      if (__all(bad == 0u)) break;
      if (sp > SPIN_CAP) return;
      __builtin_amdgcn_s_sleep(4);
    }
    ctl[C_OSEQ] = 1;
    return;
  }
  {
    const int qw = w - 3;
    const int r0 = qw * 64 + (lane >> 2) * 4;
    const unsigned char* xp = (cb < 128 ? a.xA : a.xB) + r0 * 2;
    const unsigned char* wp = (cb < 128 ? a.wqA : a.wqB) + (size_t)r0 * 8192 + (cb & 127) * 64 + (lane & 3) * 16;
    v4u Wq[4];
#pragma unroll
    for (int i = 0; i < 4; ++i) Wq[i] = __builtin_nontemporal_load((const GAS v4u*)(wp + i * 8192));
    const v2u X0 = *(const GAS v2u*)xp, X1 = *(const GAS v2u*)(xp + 1152);
    asm volatile("s_barrier" ::: "memory");
    float ac[2][8];
#pragma unroll
    for (int k = 0; k < 8; ++k) { ac[0][k] = 0.f; ac[1][k] = 0.f; }
#pragma unroll
    for (int s = 0; s < 2; ++s)
#pragma unroll
      for (int d = 0; d < 4; ++d) {
        const unsigned lo = __builtin_amdgcn_perm(Wq[2 * s + 1][d], Wq[2 * s][d], 0x05040100u), hi = __builtin_amdgcn_perm(Wq[2 * s + 1][d], Wq[2 * s][d], 0x07060302u);
        ac[0][2 * d] = dot2(lo, X0[s], ac[0][2 * d]); ac[0][2 * d + 1] = dot2(hi, X0[s], ac[0][2 * d + 1]);
        ac[1][2 * d] = dot2(lo, X1[s], ac[1][2 * d]); ac[1][2 * d + 1] = dot2(hi, X1[s], ac[1][2 * d + 1]);
      }
    float b8[8], c4[4];
#pragma unroll
    for (int k = 0; k < 8; ++k) b8[k] = rs32(ac[0][k], ac[1][k]);
#pragma unroll
    for (int k = 0; k < 4; ++k) { float t = rs16(b8[k], b8[k + 4]); t += dpp<0x128>(t); t += dpp<0x124>(t); c4[k] = t; }
    if ((lane & 12) == 0) *(LAS v4f*)(L + L_QP + qw * 256 + (lane >> 5) * 128 + (lane & 3) * 32 + ((lane >> 4) & 1) * 16) = v4f{c4[0], c4[1], c4[2], c4[3]};
    lds_inc(L, C_QCNT, lane);
  }
  const int r = lane >> 4, c = lane & 15;
  const unsigned kof = c * 256 + r * 16 + (c & 3) * 64;
  const unsigned vof = (4 * r + (c >> 2)) * 256 + (c >> 2) * 64 + (c & 3) * 8;
  if (w >= 4) {
    const unsigned aw = w - 4, off = aw * 8192, an = aw >> 1;
    v4u K0[4], K1[4], V[8];
    if (!wait_ge<1>(ctl, C_FULL, 2 * aw + 2)) return;
    read_k(L, off + kof, 0, K0); read_k(L, off + kof, 4096, K1);
    lds_inc(L, C_FREE + an, lane);
    if (!wait_ge<1>(ctl, C_FULL, 2 * aw + 18)) return;
    read_v(L, 65536 + off + vof, 0, V);
    lds_inc(L, C_FREE + 4 + an, lane);
    if (!wait_ge<1>(ctl, C_QRDY, 1)) return;
    const LAS v4u* qp = (const LAS v4u*)(L + L_Q) + r + (c >> 3) * 16;
    const v4u q[4] = {qp[0], qp[4], qp[8], qp[12]};
    v4f s0 = round2(qk(K0, q)), s1 = round2(qk(K1, q));
    const float M = xmax(fmaxf(max4(s0), max4(s1))) * L2E;
    s0 = expo(s0, M); s1 = expo(s1, M);
    const v4u P = {pk_bf16(s0[0], s0[1]), pk_bf16(s0[2], s0[3]), pk_bf16(s1[0], s1[1]), pk_bf16(s1[2], s1[3])};
    const float Ls = xsum(sum4(s0) + sum4(s1));
    v4f acc[8];
#pragma unroll
    for (int blk = 0; blk < 8; ++blk) acc[blk] = mfma(V[blk], P, v4f{0.f, 0.f, 0.f, 0.f});
#pragma unroll
    for (int k = 0; k < 4; ++k) acc[k] = sel4(bitmask(c, 2), acc[k + 4], acc[k]);
#pragma unroll
    for (int k = 0; k < 2; ++k) acc[k] = sel4(bitmask(c, 1), acc[k + 2], acc[k]);
    acc[0] = sel4(bitmask(c, 0), acc[1], acc[0]);
    LAS unsigned char* rec = L + L_REC + ((c >> 3) * 8 + aw) * REC_B;
    *(LAS v4f*)(rec + 16 + ((c & 7) * 16 + r * 4) * 4) = acc[0];
    if ((lane & 0x37) == 0) *(LAS v2f*)rec = v2f{M, Ls};
    if (last_arriver(L, C_CNT, 8, lane)) {
#pragma unroll 1
      for (int e = 0; e < 2; ++e) {
        float Mo, Lo;
        const v2f o = merge_rec<8>(L + L_REC + e * 8 * REC_B, lane, Mo, Lo);
        const int base = W_P + (g * 2 + e) * P_STR;
        if (park_rec(L, ctl, e, base + j * 128, 1024, base + 8192 + j * 16, lane, lane, o, Mo, Lo)) {
          const unsigned ta_ = ctl[C_TAGA], tb_ = ctl[C_TAGB];
          gst(ws, base + j * 128 + (lane >> 3) * 1024 + (lane & 7) * 16, ubits(o.x), ubits(o.y), ta_, tb_);
          if (lane == 0) gst(ws, base + 8192 + j * 16, ubits(Mo), ubits(Lo), ta_, tb_);
        }
      }
    }
  }
  if (w < 8) {
    const int k = w - 3;
    const unsigned char* pw = (side ? a.woB : a.woA) + (size_t)((side ? g : 2 * (g & 15) + (g >> 4)) * 128 + (j & 3) * 32) * 1152;
    const bool live = k < 4 || c < 8;
    if (!wait_ge<16>(ctl, C_WANT, 1)) return;
    const unsigned ta = rfl(ctl[C_TAGA]), tb = rfl(ctl[C_TAGB]);
    if (k == 0) {
      const unsigned t0 = __builtin_nontemporal_load((const GAS unsigned*)(pw + (lane & 1) * (31 * 1152))), t1 = __builtin_amdgcn_raw_buffer_load_b32(ws, lane & 1 ? W_SAFE1 : W_SAFE2, 0, SC1);
      asm volatile("" ::"v"(t0), "v"(t1));
    }
    if (!wait_ge<2>(ctl, C_PHASE, 1)) return;
    const unsigned held = rfl(ctl[C_HELD]);
    asm volatile("" ::: "memory");
#pragma unroll 1
    for (int slot = k; slot < NHOLD; slot += 5) {
      if (!(held >> slot & 1)) continue;
      LAS unsigned char* h = L + L_HOLD + slot * HOLD_B;
      const v4u hd = *(LAS v4u*)h;
      const v2u ml = *(LAS v2u*)(h + 16), o = *(LAS v2u*)(h + 32 + lane * 8);
      gst(ws, hd.x + (lane >> 3) * hd.y + (lane & 7) * 16, o.x, o.y, ta, tb);
      gst(ws, lane == 0 ? hd.z : OOB, ml.x, ml.y, ta, tb);
    }
    if (!wait_ge<2>(ctl, C_PHASE, 2)) return;
    v4u Wf[8], Wr[8];
    const __amdgpu_buffer_rsrc_t rw = mkrs(pw, 32 * 1152);
#pragma unroll
    for (int i = 0; i < 8; ++i) Wr[i] = __builtin_amdgcn_raw_buffer_load_b128(rw, (live ? 8 * r * 1152 + k * 256 + c * 16 : OOB) + i * 1152, 0, 2);
#pragma unroll
    for (int e = 0; e < 4; ++e)
#pragma unroll
      for (int d = 0; d < 4; ++d) {
        Wf[2 * e][d] = __builtin_amdgcn_perm(Wr[2 * d + 1][e], Wr[2 * d][e], 0x05040100u);
        Wf[2 * e + 1][d] = __builtin_amdgcn_perm(Wr[2 * d + 1][e], Wr[2 * d][e], 0x07060302u);
      }
    int y0 = live ? W_Y + ((side ? 3 - (r & 1) : (r & 1)) * 36 + k * 8 + (c >> 2)) * YS + prod * 128 + (2 * (c & 3) + (r >> 1)) * 16 : OOB, y1 = y0 + (k < 4 ? 4 : 2) * YS;
    const unsigned mh = bitmask(r, 1);
    asm volatile("" : "+v"(y0), "+v"(y1), "+v"(Wf[0]), "+v"(Wf[1]), "+v"(Wf[2]), "+v"(Wf[3]), "+v"(Wf[4]), "+v"(Wf[5]), "+v"(Wf[6]), "+v"(Wf[7]));
    v4u oa;
#pragma unroll 1
    for (unsigned sp = 0;; ++sp) {
      const unsigned fl = ctl[C_OSEQ];
      oa = *(volatile LAS v4u*)(L + L_OP + ((c >> 2) & 1) * 64 + r * 16);
      if (rfl(fl)) break;
      if (sp > SPIN_CAP) return;
      __builtin_amdgcn_s_sleep(1);
    }
#pragma unroll
    for (int i = 0; i < 2; ++i) {
      v4f d[4];
#pragma unroll
      for (int f = 0; f < 4; ++f) d[f] = mfma(oa, Wf[(f >> 1) * 4 + 2 * i + (f & 1)], v4f{0.f, 0.f, 0.f, 0.f});
      gst(ws, i ? y1 : y0, ubits(sel(mh, d[2][0], d[0][0])), ubits(sel(mh, d[3][0], d[1][0])), ta, tb);
    }
    return;
  }
  const unsigned ta = rfl(ctl[C_TAGA]), tb = rfl(ctl[C_TAGB]);
  const int cw = w - 8;
  {
    float m = -INFINITY, l = 0.f;
    v4f acc[8];
    bool fresh = true, freshv = true;
    const unsigned mk0 = bitmask(c, 0), mk1 = bitmask(c, 1), mk2 = bitmask(c, 2), mk3 = bitmask(c, 3);
    const int gid = (c >> 1) * 8 + 2 * r + (c & 1);
    int mode = 1;
#pragma unroll 1
    for (unsigned n = 4 + cw;;) {
      bool stop = false;
      unsigned dsc = cw << 8;
      const bool dyn = n >= NS;
      if (dyn) {
#pragma unroll 1
        for (unsigned sp = 0;; ++sp) {
          const unsigned dd = ctl[C_DESC + n - NS], en = ctl[C_END];
          dsc = rfl(dd);
          if (n >= rfl(en) || sp > SPIN_CAP) { stop = true; break; }
          if (dsc != 0xffffffffu) break;
          __builtin_amdgcn_s_sleep(2);
        }
        if (stop) break;
        if ((dsc >> 8 & 3) != (unsigned)cw) { ++n; continue; }
      }
      const bool pubn = dyn ? (dsc >> 10 & 1) : (n >= NS - 4 || (n & ~3u) == 16);
      const int base = n >= 20 ? W_P1 + g * P1_STR : W_P + (64 + g) * P_STR, dstr = n >= 20 ? 8192 : 1024, rec = dyn ? dsc & 255 : j, slot = dyn ? dsc >> 12 : 2 + (n >= 20);
      const int gi = dyn ? gid : lane;
      const unsigned sk = segk(n), sv = segv(n);
      int ad = base + rec * 128 + (gi >> 3) * dstr + (gi & 7) * 16, am = lane == 0 ? base + 8 * dstr + rec * 16 : OOB;
      unsigned ka = (sk & 7) * 16384 + kof, va = (sv & 7) * 16384 + vof;
      LAS unsigned char* pb = L + L_PB + (dyn ? 0 : n - 4) * PB_B;
      v4u P0, P1, st;
      float al, Ls, mrec;
      const bool first = freshv;
      if ((mode & 2) && freshv) {
#pragma unroll
        for (int blk = 0; blk < 8; ++blk) acc[blk] = v4f{0.f, 0.f, 0.f, 0.f};
        freshv = false;
      }
      if (mode & 1) {
        const LAS v4u* qp = (const LAS v4u*)(L + L_Q) + r + (n < 20 ? 32 : 48);
        v4u q[4] = {qp[0], qp[4], qp[8], qp[12]};
        if (fresh) { m = -INFINITY; l = 0.f; }
        fresh = pubn;
        asm volatile("" : "+v"(ad), "+v"(am), "+v"(ka), "+v"(va), "+v"(q[0]), "+v"(q[1]), "+v"(q[2]), "+v"(q[3]));
        if (!wait_ge<1>(ctl, C_FULL, 4 * sk + 4)) return;
        v4f s[4];
        v4u K[4][4];
#pragma unroll
        for (int uu = 0; uu < 4; ++uu) read_k(L, ka, uu * 4096, K[uu]);
        __builtin_amdgcn_sched_barrier(0);
#pragma unroll
        for (int uu = 0; uu < 4; ++uu) s[uu] = qk(K[uu], q);
        __builtin_amdgcn_sched_barrier(0);
        lgkm0();
        ctl[C_FREE + (sk & 7)] = sk + 1;
        v4f sc = round2(sel4(mk3, sel4(mk2, s[3], s[2]), sel4(mk2, s[1], s[0])));
        const float mn = fmaxf(max4to64(max4(sc)) * L2E, m);
        st = *(volatile LAS v4u*)(L + L_CTL);
        sc = expo(sc, mn);
        const unsigned px = pk_bf16(sc[0], sc[1]), py = pk_bf16(sc[2], sc[3]);
        P0 = v4u{bcast16<0>(px), bcast16<0>(py), bcast16<4>(px), bcast16<4>(py)}; P1 = v4u{bcast16<8>(px), bcast16<8>(py), bcast16<12>(px), bcast16<12>(py)};
        al = ex2(m - mn);
        mrec = m = mn;
        l = fmaf(l, al, sum4(sc));
        Ls = l + dpp<0x124>(l);
        Ls += dpp<0x128>(Ls);
        Ls = xsum(Ls);
        if (mode == 1) {
          *(LAS v4u*)(pb + r * 16) = P0; *(LAS v4u*)(pb + 64 + r * 16) = P1; *(LAS v4f*)(pb + 128) = v4f{al, Ls, mn, 0.f};
          n += 4;
          if (n >= NS) { n = 4 + cw; mode = 2; }
          continue;
        }
      } else {
        P0 = *(LAS v4u*)(pb + r * 16); P1 = *(LAS v4u*)(pb + 64 + r * 16);
        const v4f t = *(LAS v4f*)(pb + 128);
        al = t.x; Ls = t.y; mrec = t.z;
        st = *(volatile LAS v4u*)(L + L_CTL);
      }
      if (!first) {
#pragma unroll
        for (int blk = 0; blk < 8; ++blk) acc[blk] *= al;
      }
      asm volatile("" : "+v"(P0), "+v"(P1), "+v"(Ls), "+v"(acc[0]), "+v"(acc[1]), "+v"(acc[2]), "+v"(acc[3]), "+v"(acc[4]), "+v"(acc[5]), "+v"(acc[6]), "+v"(acc[7]));
      v4u V[8];
#define WAITV(need) _Pragma("unroll 1") for (unsigned sp = 0; rfl(st.x) < (need); ++sp) { if (sp > SPIN_CAP) return; if (sp) __builtin_amdgcn_s_sleep(1); st = *(volatile LAS v4u*)(L + L_CTL); } \
      asm volatile("" ::: "memory");
      WAITV(4 * sv + 2)
      read_v(L, va, 0, V);
#pragma unroll
      for (int blk = 0; blk < 8; ++blk) acc[blk] = mfma(V[blk], P0, acc[blk]);
      WAITV(4 * sv + 4)
      const unsigned tl = now32(), nend = rfl(st.y);
      read_v(L, va, 8192, V);
      hint_set(ws, lane == 0 && n + 1 == nend ? W_F1 + wg * 128 : OOB, tl, ta);
#pragma unroll
      for (int blk = 0; blk < 8; ++blk) acc[blk] = mfma(V[blk], P1, acc[blk]);
      if (pubn) {
        v2f o[8];
#pragma unroll
        for (int k = 0; k < 8; ++k) o[k] = sel2(mk0, v2f{acc[k][2], acc[k][3]}, v2f{acc[k][0], acc[k][1]});
#pragma unroll
        for (int k = 0; k < 4; ++k) o[k] = sel2(mk3, o[k + 4], o[k]);
#pragma unroll
        for (int k = 0; k < 2; ++k) o[k] = sel2(mk2, o[k + 2], o[k]);
        o[0] = sel2(mk1, o[1], o[0]);
        bool now = dyn && (slot >= NHOLD || rfl(st.z) != 0);
        if (!now) {
          bool emit = true;
          if (!dyn) {
            LAS unsigned char* rp = L + L_REC + (8 + slot * 4) * REC_B;
            *(LAS v2f*)(rp + cw * REC_B + 16 + gid * 8) = o[0];
            if (lane == 0) *(LAS v2f*)(rp + cw * REC_B) = v2f{mrec, Ls};
            emit = last_arriver(L, C_CNT + slot - 1, 4, lane);
            if (emit) o[0] = merge_rec<4>(rp, lane, mrec, Ls);
          }
          if (emit) now = park_rec(L, ctl, slot, base + rec * 128, dstr, base + 8 * dstr + rec * 16, gi, lane, o[0], mrec, Ls);
          if (!now) { ad = OOB; am = OOB; }
        }
        gst(ws, ad, ubits(o[0].x), ubits(o[0].y), ta, tb);
        gst(ws, am, ubits(mrec), ubits(Ls), ta, tb);
        freshv = true;
      }
      lgkm0();
      ctl[C_FREE + (sv & 7)] = sv + 1;
      if (n + 4 < NS) n += 4;
      else { n = dyn ? n + 1 : NS; mode = 3; }
    }
  }
  if (prod >= 72) return;
  const int cmb = side * 2 + (prod & 1), rho = prod >> 1;
  int voff = W_Y + (cmb * 36 + rho) * YS + cw * 4096 + lane * 16;
  const int yi = rho < 32 ? (rho >> 2) & 1 : (rho >> 1) & 1, yq = rho < 32 ? rho & 3 : rho & 1;
  int oo = lane < 16 ? ((cmb & 1) * 1152 + (cmb >> 1) * 576 + (rho >> 3) * 128 + (yq * 4 + (lane >> 2)) * 8 + ((lane >> 1) & 1) * 4 + yi * 2 + (lane & 1)) * 2 : OOB;
  asm volatile("" : "+v"(voff), "+v"(oo));
  if (!wait_ge<4>(ctl, C_T1, 1)) return;
  if (!wait_ge<1>(ctl, C_T2, 1)) return;
  wait_until(tstart, rfl(ctl[C_T2]) - tstart);
  v4u X[4];
#pragma unroll 1
  for (unsigned sp = 0;; ++sp) {
#pragma unroll
    for (int i = 0; i < 4; ++i) X[i] = gld(ws, voff, i * 1024);
    unsigned bad = 0;
#pragma unroll
    for (int i = 0; i < 4; ++i) bad = gbad(X[i], ta, tb, bad);
    if (__all(bad == 0u)) break;
    if (sp > SPIN_CAP) return;
    __builtin_amdgcn_s_sleep(4);
  }
  float s = rs32((fbits(X[0].x) + fbits(X[1].x)) + (fbits(X[2].x) + fbits(X[3].x)), (fbits(X[0].z) + fbits(X[1].z)) + (fbits(X[2].z) + fbits(X[3].z)));
  s = rs16(s, s);
  s += dpp<0x128>(s);
  *(volatile LAS unsigned*)(L + L_FP + (((lane & 7) * 2 + (lane >> 5)) * 4 + cw) * 4) = min(ubits(s), 0xfffffffeu);
  if (cw != 0) return;
#pragma unroll 1
  for (unsigned sp = 0;; ++sp) {
    const v4u p = *(volatile LAS v4u*)(L + L_FP + (lane & 15) * 16);
    s = (fbits(p.x) + fbits(p.y)) + (fbits(p.z) + fbits(p.w));
    if (__all(p.y != ~0u && p.z != ~0u && p.w != ~0u)) break;
    if (sp > SPIN_CAP) return;
  }
  __builtin_amdgcn_raw_buffer_store_b16((unsigned short)pk_bf16(s, s), mkrs(a.out, 4608), oo, 0, 0);
  __builtin_amdgcn_raw_buffer_store_b64(v2u{ta, tb}, ws, cmb == 3 && rho == 0 && lane == 0 ? W_X : OOB, 0, SC1);
}

extern "C" void kernel_launch(void* const* d_in, const int* in_sizes, int n_in, void* d_out, int out_size, void* d_ws, size_t ws_size,
                              hipStream_t stream) {
  Args a;
  a.xA = (const unsigned char*)d_in[0]; a.kA = (const unsigned char*)d_in[1]; a.vA = (const unsigned char*)d_in[2];
  a.wqA = (const unsigned char*)d_in[3]; a.woA = (const unsigned char*)d_in[4];
  a.xB = (const unsigned char*)d_in[5]; a.kB = (const unsigned char*)d_in[6]; a.vB = (const unsigned char*)d_in[7];
  a.wqB = (const unsigned char*)d_in[8]; a.woB = (const unsigned char*)d_in[9];
  a.out = (unsigned char*)d_out; a.ws = (unsigned char*)d_ws + (200u  __H0__ );
  fused_decode<<<256, NTHR, 0, stream>>>(a);
}
```

```cpp
#include <hip/hip_runtime.h>
#include <stdint.h>

typedef unsigned v4u __attribute__((ext_vector_type(4)));
typedef unsigned v2u __attribute__((ext_vector_type(2)));
typedef float v4f __attribute__((ext_vector_type(4)));
typedef float v2f __attribute__((ext_vector_type(2)));
typedef short v4s __attribute__((ext_vector_type(4)));
typedef __bf16 bf16x2 __attribute__((ext_vector_type(2)));
typedef __bf16 bf16x8 __attribute__((ext_vector_type(8)));
#define LAS __attribute__((address_space(3)))
#define GAS __attribute__((address_space(1)))
#define DI __device__ __forceinline__

constexpr int WLD = 22;
constexpr int DT1 = 50;
constexpr int DT2 = 100;
constexpr int KAH = 3;
constexpr int NTHR = 12 * 64;
constexpr int NSTAT = 8;
constexpr int NS = 4 + 16 + NSTAT;
constexpr int POOLC = (16 - NSTAT) * 8;
constexpr int NT = 48, TP = POOLC - NT;
constexpr int WINE = (WLD + 7) / 8 * 2;
constexpr int NREP = 16;
constexpr int NKMAX = 52;
constexpr unsigned SPIN_CAP = 1u << 20;
constexpr int NHOLD = 16, HOLD_B = 544, REC_B = 528, PB_B = 144;
constexpr int L_CTL = 131072  , L_FP = L_CTL + 768  , L_Q = L_CTL + 1024, L_OP = L_Q + 1024, L_QP = L_OP + 128,
              L_REC = L_QP + 9 * 256, L_HOLD = L_REC + 24 * REC_B, L_PB = L_HOLD + NHOLD * HOLD_B, L_END = L_PB + (NKMAX - 4) * PB_B;
static_assert(L_END <= 163840 && TP >= 0 && TP <= NT && NT + 2 <= 64 && POOLC <= 64 && WINE >= 2 && WINE <= 8, "");
enum { C_FULL = 0  , C_END = 1  , C_PHASE = 2  , C_NK = 3, C_WANT = 4  ,
       C_T1 = 5  , C_TAGA = 6, C_TAGB = 7, C_FREE = 8  , C_CNT = 16  , C_QCNT = 19, C_QRDY = 20, C_HELD = 21, C_LATE = 23, C_OSEQ = 22,
       C_T2 = 24  , C_TICK = 64, C_DESC = 128 };
constexpr int W_X = 0, W_CTR = 4096, W_F1 = 16384  , W_F2 = W_F1 + 32768  , W_QA = W_F2 + 2 * NREP * 1024, W_QB = W_QA + 32768,
              W_P = W_QB + 32768, P_STR = 9216  , W_P1 = W_P + 96 * P_STR, P1_STR = 66560  ,
              W_Y = W_P1 + 32 * P1_STR, YS = 16384, W_ENDB = 256 + W_Y + 4 * 36 * YS  ;
constexpr int W_SAFE1 = W_P1 + 24 * P1_STR + 7424, W_SAFE2 = W_ENDB - 128;
constexpr float SCALE = 0.088388347648318440f, L2E = 1.4426950408889634f;
constexpr int SC1 = 16, OOB = 0x70000000;

struct Args {
  const unsigned char *xA, *kA, *vA, *wqA, *woA, *xB, *kB, *vB, *wqB, *woB;
  unsigned char *out, *ws;
};

DI float dot2(unsigned a, unsigned b, float c) { return __builtin_amdgcn_fdot2_f32_bf16(__builtin_bit_cast(bf16x2, a), __builtin_bit_cast(bf16x2, b), c, false); }
template <int CTRL> DI float dpp(float x) { return __builtin_bit_cast(float, __builtin_amdgcn_mov_dpp(__builtin_bit_cast(int, x), CTRL, 0xf, 0xf, true)); }
template <int LANE> DI unsigned bcast16(unsigned x) { return __builtin_amdgcn_mov_dpp(x, 0x150 + LANE, 0xf, 0xf, true); }
DI unsigned pk_bf16(float lo, float hi) {
  unsigned r = __builtin_bit_cast(unsigned, bf16x2{(__bf16)lo, (__bf16)hi});
  asm("" : "+v"(r));
  return r;
}
DI float bf_lo(unsigned u) { return __builtin_bit_cast(float, u << 16); }
DI float bf_hi(unsigned u) { return __builtin_bit_cast(float, u & 0xffff0000u); }
DI float fbits(unsigned u) { return __builtin_bit_cast(float, u); }
DI unsigned ubits(float f) { return __builtin_bit_cast(unsigned, f); }
DI unsigned rfl(unsigned x) { return __builtin_amdgcn_readfirstlane(x); }
DI float ex2(float x) { return __builtin_amdgcn_exp2f(x); }
DI v4f mfma(v4u a, v4u b, v4f c) { return __builtin_amdgcn_mfma_f32_16x16x32_bf16(__builtin_bit_cast(bf16x8, a), __builtin_bit_cast(bf16x8, b), c, 0, 0, 0); }
DI v2u trd(LAS unsigned char* p) { return __builtin_bit_cast(v2u, __builtin_amdgcn_ds_read_tr16_b64_v4i16((LAS v4s*)p)); }
DI float rs32(float x, float y) { auto s = __builtin_amdgcn_permlane32_swap(ubits(x), ubits(y), false, false); return fbits(s[0]) + fbits(s[1]); }
DI float rs16(float x, float y) { auto s = __builtin_amdgcn_permlane16_swap(ubits(x), ubits(y), false, false); return fbits(s[0]) + fbits(s[1]); }
DI float xsum(float x) { x = rs16(x, x); return rs32(x, x); }
DI float xmax(float x) {
  auto s = __builtin_amdgcn_permlane16_swap(ubits(x), ubits(x), false, false); x = fmaxf(fbits(s[0]), fbits(s[1]));
  auto t = __builtin_amdgcn_permlane32_swap(ubits(x), ubits(x), false, false); return fmaxf(fbits(t[0]), fbits(t[1]));
}
DI float max8(float x) {
  asm("s_nop 1\n\tv_max_f32_dpp %0, %0, %0 quad_perm:[1,0,3,2] row_mask:0xf bank_mask:0xf\n\ts_nop 1\n\tv_max_f32_dpp %0, %0, %0 quad_perm:[2,3,0,1] row_mask:0xf bank_mask:0xf\n\t"
      "s_nop 1\n\tv_max_f32_dpp %0, %0, %0 row_half_mirror row_mask:0xf bank_mask:0xf" : "+v"(x));
  return x;
}
DI float max8to64(float x) {
  float t;
  asm("s_nop 1\n\tv_max_f32_dpp %0, %0, %0 row_ror:8 row_mask:0xf bank_mask:0xf\n\tv_mov_b32 %1, %0\n\ts_nop 1\n\tv_permlane16_swap_b32 %0, %1\n\tv_max_f32 %0, %0, %1\n\t"
      "v_mov_b32 %1, %0\n\ts_nop 1\n\tv_permlane32_swap_b32 %0, %1\n\tv_max_f32 %0, %0, %1" : "+v"(x), "=&v"(t));
  return x;
}
DI float max4to64(float x) {
  asm("s_nop 1\n\tv_max_f32_dpp %0, %0, %0 row_ror:4 row_mask:0xf bank_mask:0xf" : "+v"(x));
  return max8to64(x);
}
DI void lgkm0() { asm volatile("s_waitcnt lgkmcnt(0)" ::: "memory"); }
template <int N> DI void waitvm() { asm volatile("s_waitcnt vmcnt(%0)" ::"n"(N) : "memory"); }
DI __amdgpu_buffer_rsrc_t mkrs(const void* p, unsigned bytes) { return __builtin_amdgcn_make_buffer_rsrc((void*)p, 0, bytes, 0x00020000); }
DI void dma8(__amdgpu_buffer_rsrc_t rs, unsigned voff, unsigned soff, unsigned lds) {
  unsigned keep, s2;
  asm volatile("s_mov_b32 %0, m0\n\ts_mov_b32 m0, %5\n\ts_add_u32 %1, %4, 0x1000\n\ts_nop 2\n\t"
               "buffer_load_dwordx4 %2, %3, %4 offen nt lds\n\t"
               "buffer_load_dwordx4 %2, %3, %4 offen offset:1024 nt lds\n\t"
               "buffer_load_dwordx4 %2, %3, %4 offen offset:2048 nt lds\n\t"
               "buffer_load_dwordx4 %2, %3, %4 offen offset:3072 nt lds\n\t"
               "s_add_u32 m0, m0, 0x1000\n\ts_nop 0\n\t"
               "buffer_load_dwordx4 %2, %3, %1 offen nt lds\n\t"
               "buffer_load_dwordx4 %2, %3, %1 offen offset:1024 nt lds\n\t"
               "buffer_load_dwordx4 %2, %3, %1 offen offset:2048 nt lds\n\t"
               "buffer_load_dwordx4 %2, %3, %1 offen offset:3072 nt lds\n\t"
               "s_mov_b32 m0, %0"
               : "=&s"(keep), "=&s"(s2) : "v"(voff), "s"(rs), "s"(soff), "s"(lds) : "memory", "scc");
}
DI v4u gld(__amdgpu_buffer_rsrc_t rs, int off, int soff = 0) { return __builtin_amdgcn_raw_buffer_load_b128(rs, off, soff, SC1); }
DI void gst(__amdgpu_buffer_rsrc_t rs, int off, unsigned a, unsigned b, unsigned ta, unsigned tb) { __builtin_amdgcn_raw_buffer_store_b128(v4u{a, ta, b, tb}, rs, off, 0, SC1); }
DI unsigned gbad(v4u g, unsigned ta, unsigned tb, unsigned acc) { return __builtin_amdgcn_bitop3_b32(g.w, tb, __builtin_amdgcn_bitop3_b32(g.y, ta, acc, 0xBE), 0xBE); }

typedef volatile LAS unsigned* ctl_t;
template <int SLP> DI bool wait_ge(ctl_t ctl, int idx, unsigned v) {
#pragma unroll 1
  for (unsigned s = 0; rfl(ctl[idx]) < v; ++s) {
    if (s > SPIN_CAP) return false;
    __builtin_amdgcn_s_sleep(SLP);
  }
  asm volatile("" ::: "memory");
  return true;
}
DI void lds_inc(LAS unsigned char* L, int idx, int lane) {
  lgkm0();
  if (lane == 0) __hip_atomic_fetch_add((LAS unsigned*)(L + L_CTL) + idx, 1u, __ATOMIC_RELAXED, __HIP_MEMORY_SCOPE_WORKGROUP);
}
DI bool last_arriver(LAS unsigned char* L, int idx, unsigned n, int lane) {
  lgkm0();
  unsigned old = 0;
  if (lane == 0) old = __hip_atomic_fetch_add((LAS unsigned*)(L + L_CTL) + idx, 1u, __ATOMIC_RELAXED, __HIP_MEMORY_SCOPE_WORKGROUP);
  old = rfl(old);
  asm volatile("" ::: "memory");
  return old == n - 1;
}
DI void read_k(LAS unsigned char* L, unsigned off, int imm, v4u (&K)[4]) {
#pragma unroll
  for (int kk = 0; kk < 4; ++kk) K[kk] = *(const LAS v4u*)(L + (off ^ (kk * 64)) + imm);
}
DI void read_v(LAS unsigned char* L, unsigned off, int imm, v4u (&V)[8]) {
#pragma unroll
  for (int blk = 0; blk < 8; ++blk) {
    LAS unsigned char* p = L + (off ^ ((blk >> 1) * 64)) + (blk & 1) * 32 + imm;
    const v2u x = trd(p), y = trd(p + 4096);
    V[blk] = v4u{x.x, x.y, y.x, y.y};
  }
  __builtin_amdgcn_sched_barrier(0);
}
DI v4f qk(const v4u (&K)[4], const v4u (&q)[4]) {
  v4f d = {0.f, 0.f, 0.f, 0.f};
#pragma unroll
  for (int kk = 0; kk < 4; ++kk) d = mfma(K[kk], q[kk], d);
  return d;
}
DI v4f round2(v4f d) {
  const unsigned x01 = pk_bf16(d[0], d[1]), x23 = pk_bf16(d[2], d[3]);
  const unsigned b01 = pk_bf16(bf_lo(x01) * SCALE, bf_hi(x01) * SCALE), b23 = pk_bf16(bf_lo(x23) * SCALE, bf_hi(x23) * SCALE);
  return v4f{bf_lo(b01), bf_hi(b01), bf_lo(b23), bf_hi(b23)};
}
DI float sel(unsigned mask, float a, float b) { return fbits(__builtin_amdgcn_bitop3_b32(mask, ubits(a), ubits(b), 0xCA)); }
DI v4f sel4(unsigned mask, v4f a, v4f b) { return v4f{sel(mask, a[0], b[0]), sel(mask, a[1], b[1]), sel(mask, a[2], b[2]), sel(mask, a[3], b[3])}; }
DI v2f sel2(unsigned mask, v2f a, v2f b) { return v2f{sel(mask, a[0], b[0]), sel(mask, a[1], b[1])}; }
DI unsigned bitmask(int x, int bit) { unsigned m = (unsigned)((x << (31 - bit)) >> 31); asm("" : "+v"(m)); return m; }
DI float max4(v4f d) { return fmaxf(fmaxf(d[0], d[1]), fmaxf(d[2], d[3])); }
DI float sum4(v4f d) { return (d[0] + d[1]) + (d[2] + d[3]); }
DI v4f expo(v4f s, float m2) { return v4f{ex2(fmaf(s[0], L2E, -m2)), ex2(fmaf(s[1], L2E, -m2)), ex2(fmaf(s[2], L2E, -m2)), ex2(fmaf(s[3], L2E, -m2))}; }

DI unsigned now32() { return (unsigned)__builtin_amdgcn_s_memrealtime(); }
DI void wait_until(unsigned t0, unsigned until) {
#pragma unroll 1
  for (unsigned sp = 0; sp < 64 && now32() - t0 < until; ++sp) { }
}
DI void hint_set(__amdgpu_buffer_rsrc_t ws, int off, unsigned t, unsigned tag) {
  asm volatile("buffer_atomic_swap_x2 %0, %1, %2, 0 offen" ::"v"(v2u{t, tag}), "v"(off), "s"(ws) : "memory");
}
template <bool TWO> DI unsigned hint_time(__amdgpu_buffer_rsrc_t ws, int off, int off2, unsigned tag, unsigned t0) {
  unsigned rel = 0;
#define HP1(A0, A1, o) asm volatile("v_mov_b32 v16" #A0 ", 0\n\tv_mov_b32 v16" #A1 ", 0\n\tbuffer_atomic_add_x2 v[16" #A0 ":16" #A1 "], %0, %1, 0 offen sc0" \
                                    ::"v"(o), "s"(ws) : "memory", "v16" #A0, "v16" #A1)
#define HC1(A0, A1, o, N) { unsigned t_, g_; asm volatile("s_waitcnt vmcnt(%2)\n\tv_mov_b32 %0, v16" #A0 "\n\tv_mov_b32 %1, v16" #A1 : "=v"(t_), "=v"(g_) : "n"(N) : "memory", "v16" #A0, "v16" #A1); \
                            const bool ok_ = g_ == tag; rel = ok_ ? max(rel, t_ - t0) : rel; o = ok_ ? OOB : o; }
  HP1(0, 1, off);
  if (TWO) HP1(2, 3, off2);
  __builtin_amdgcn_s_sleep(8);
#pragma unroll 1
  for (unsigned sp = 0; sp < 4096; ++sp) {
    HP1(4, 5, off);
    if (TWO) HP1(6, 7, off2);
    HC1(0, 1, off, TWO ? 3 : 1)
    if (TWO) HC1(2, 3, off2, 2)
    if (__all(off == OOB && off2 == OOB)) break;
    HP1(0, 1, off);
    if (TWO) HP1(2, 3, off2);
    HC1(4, 5, off, TWO ? 3 : 1)
    if (TWO) HC1(6, 7, off2, 2)
    if (__all(off == OOB && off2 == OOB)) break;
  }
  return rfl((unsigned)max8to64(max8((float)rel)));
}
#undef HP1
#undef HC1
DI bool park_rec(LAS unsigned char* L, ctl_t ctl, int slot, int adr, int dstr, int mladr, int gi, int lane, v2f o, float m, float l) {
  LAS unsigned char* h = L + L_HOLD + slot * HOLD_B;
  *(LAS v2f*)(h + 32 + gi * 8) = o;
  if (lane == 0) { *(LAS v4u*)h = v4u{(unsigned)adr, (unsigned)dstr, (unsigned)mladr, 0u}; *(LAS v2f*)(h + 16) = v2f{m, l}; }
  lgkm0();
  if (lane == 0) __hip_atomic_fetch_or((LAS unsigned*)(L + L_CTL) + C_HELD, 1u << slot, __ATOMIC_RELAXED, __HIP_MEMORY_SCOPE_WORKGROUP);
  lgkm0();
  return rfl(ctl[C_PHASE]) != 0;
}
template <int NR> DI v2f merge_rec(LAS unsigned char* rec, int lane, float& Mo, float& Lo) {
  v2f av[NR];
  const v2f ml = *(LAS v2f*)(rec + (lane & (NR - 1)) * REC_B);
#pragma unroll
  for (int w = 0; w < NR; ++w) av[w] = *(LAS v2f*)(rec + w * REC_B + 16 + lane * 8);
  const float M = max8(ml.x), f = ex2(ml.x - M);
  float Ls = f * ml.y; Ls += dpp<0xB1>(Ls); Ls += dpp<0x4E>(Ls);
  if (NR == 8) Ls += dpp<0x141>(Ls);
  float a0 = 0.f, a1 = 0.f;
#pragma unroll
  for (int w = 0; w < NR; ++w) { const float fw = fbits(__builtin_amdgcn_readlane(ubits(f), w)); a0 = fmaf(fw, av[w].x, a0); a1 = fmaf(fw, av[w].y, a1); }
  Mo = M; Lo = Ls;
  return v2f{a0, a1};
}

__global__ void __launch_bounds__(NTHR, 3) fused_decode(Args a) {
  __shared__ __attribute__((aligned(16))) unsigned char smem[L_END];
  LAS unsigned char* L = (LAS unsigned char*)smem;
  ctl_t ctl = (ctl_t)(L + L_CTL);
  asm volatile("" ::"s"(a.xA), "s"(a.kA), "s"(a.vA), "s"(a.wqA), "s"(a.woA), "s"(a.xB), "s"(a.kB), "s"(a.vB), "s"(a.wqB), "s"(a.woB), "s"(a.out), "s"(a.ws));
  const int wg = blockIdx.x, g = wg >> 3, j = wg & 7;
  const int lane = threadIdx.x & 63, w = rfl(threadIdx.x >> 6);

  if (w == 0) {
    __builtin_amdgcn_s_waitcnt(0);
    const unsigned lb = (unsigned)(uintptr_t)L;
    const unsigned voff = (lane * 16) ^ ((lane >> 4) << 6);
    const unsigned offA = g * (2u << 20) + (3u << 19) + j * 16384;
    const __amdgpu_buffer_rsrc_t rk = mkrs(a.kA, 64u << 20);
    dma8(rk, voff, offA, lb); dma8(rk, voff, offA + 8192, lb + 8192);
    asm volatile("s_barrier" ::: "memory");
    __builtin_amdgcn_s_setprio(3);
    int u = 4, pub = 0;
    unsigned sg = 1;
#define LWAIT(idx, v, out) _Pragma("unroll 1") for (unsigned sp = 0;; ++sp) { \
      out = rfl(ctl[idx]); if (out + 1u > (v)) break; \
      if (sp > SPIN_CAP) return; \
      if (sp == 6) { waitvm<0>(); pub = u; ctl[C_FULL] = pub; } \
      __builtin_amdgcn_s_sleep(1); }
#define HALF(rs, so, ld) do { waitvm<WLD>(); if (u - WINE > pub) { pub = u - WINE; ctl[C_FULL] = pub; } dma8(rs, voff, so, ld); u += 2; } while (0)
#define SEG(rs, so) do { unsigned fr_; LWAIT(C_FREE + (sg & 7), sg < 8 ? 0u : sg < 16 ? 2u : sg - 7, fr_) \
      const unsigned ld_ = lb + (sg & 7) * 16384; HALF(rs, so, ld_); HALF(rs, (so) + 8192, ld_ + 8192); ++sg; } while (0)
    const unsigned b1 = (32 + g) * (2u << 20);
    unsigned k = 0, dsc = 0, nk = NS;
    bool second = false;
#define POOLOFF(d) (b1 + ((NSTAT * 8 + ((d) >> 20) + g * 8) & 127) * 16384)
#define NEXT_ITEM(t, n) do { \
      if (second) { dsc += (1u << 20) + (1u << 10); second = false; } \
      else { second = t < (unsigned)TP; dsc = (8 + t) | ((NS + k) & 3) << 8 | (second ? 0u : 1u << 10) | (4 + k) << 12 | (second ? 2 * t : TP + t) << 20; ++k; } \
      ctl[C_DESC + (n) - NS] = dsc; } while (0)
    const __amdgpu_buffer_rsrc_t rkb = mkrs(a.kB, 128u << 20), rvb = mkrs(a.vB, 128u << 20);
#pragma unroll 1
    for (int run = 0; run < 4; ++run) {
      const __amdgpu_buffer_rsrc_t rs = mkrs(run == 0 ? a.kA : run == 1 ? a.vA : run == 2 ? a.kB : a.vB, run < 2 ? 64u << 20 : 128u << 20);
      const int cnt = run < 2 ? 4 : NS - 2 - run;
#pragma unroll 1
      for (int i = run == 0; i < cnt; ++i) {
        const unsigned so = run < 2 ? offA + i * 131072 : (g + (i & 16) * 2) * (2u << 20) + ((i * 8 + wg) & 127) * 16384;
        if (sg == NS + 4 - KAH) ctl[C_WANT] = 1;
        SEG(rs, so);
      }
      if (run == 2) {
#pragma unroll 1
        for (;;) {
          unsigned t = 0;
          if (!second) { LWAIT(C_TICK + k, 0u, t) if (t >= (unsigned)NT) break; }
          NEXT_ITEM(t, nk);
          const bool more = second && nk + 4 <= NKMAX;
          if (more) ctl[C_WANT] = k + 1;
          SEG(rkb, POOLOFF(dsc));
          ++nk;
          if (!second && (t >= (unsigned)TP || nk + 2 > NKMAX)) break;
        }
        ctl[C_NK] = nk;
      }
    }
    ctl[C_LATE] = 1;
    unsigned sv = b1 + (((NS - 5) * 8 + wg) & 127) * 16384;
#pragma unroll 1
    for (unsigned n = NS;; ++n) {
      unsigned t = 0, sk = 0;
      if (n + 2 >= nk) ctl[C_WANT] = k + 1;
      if (n < nk) sk = POOLOFF(rfl(ctl[C_DESC + n - NS]));
      else {
        if (!second) { LWAIT(C_TICK + k, 0u, t) }
        if (t < (unsigned)NT) {
          NEXT_ITEM(t, n);
          sk = POOLOFF(dsc);
          ctl[C_WANT] = k + 1;
          SEG(rkb, sk);
        } else {
          ctl[C_END] = n; ctl[C_PHASE] = 1; ++sg;
        }
      }
      SEG(rvb, sv);
      if (t >= (unsigned)NT) break;
      sv = sk;
    }
    ctl[C_PHASE] = 2;
#define DRAIN(k_) if (WINE >= k_) { waitvm<4 * k_>(); const int p_ = u - k_ + (k_ < 4 ? 4 : 0); if (p_ > pub) { pub = p_; ctl[C_FULL] = pub; } }
    DRAIN(8) DRAIN(6) DRAIN(4) DRAIN(2) DRAIN(0)
    return;
  }
  const __amdgpu_buffer_rsrc_t ws = mkrs(a.ws, W_ENDB);
  const unsigned tstart = now32();
  const int cb = (wg & ~15) + ((wg & 7) << 1) + ((wg >> 3) & 1);
  const int side = j >> 2, prod = g * 4 + (j & 3);

  if (w == 1) {
    const v2u ep = __builtin_amdgcn_raw_buffer_load_b64(ws, W_X, 0, SC1);
    if (cb >= 128 && (cb & 3) == 0 && lane == 0) __builtin_amdgcn_raw_buffer_store_b32(0u, ws, W_CTR + ((cb - 128) >> 2) * 256, 0, SC1);
    ctl[lane] = lane == C_END || lane == C_NK ? 0x7fffffffu : 0u;
    ctl[lane + 64] = 0xffffffffu; ctl[lane + 128] = 0xffffffffu; ctl[lane + 192] = 0xffffffffu;
    asm volatile("s_waitcnt lgkmcnt(0)\n\ts_barrier" ::: "memory");
    unsigned ta = rfl(ep.x) + 1u, tb = rfl(ep.y) + 1u;
    ta += ta == 0u; tb += tb == 0u;
    waitvm<0>();
    if (!wait_ge<1>(ctl, C_QCNT, 9)) return;
    if (lane < 16) {
      v4f s = {0.f, 0.f, 0.f, 0.f};
#pragma unroll
      for (int i = 0; i < 9; ++i) s += *(LAS v4f*)(L + L_QP + i * 256 + lane * 16);
      const int b = lane >> 3, quad = lane & 7;
      const int off = cb < 128 ? W_QA + ((b * 16 + (cb >> 3)) * 64 + (cb & 7) * 8 + quad) * 16 : W_QB + (((cb - 128) >> 2) * 64 + b * 32 + (cb & 3) * 8 + quad) * 16;
      gst(ws, off, pk_bf16(s.x, s.y), pk_bf16(s.z, s.w), ta, tb);
    }
    v4u qa, qb;
#pragma unroll 1
    for (unsigned sp = 0;; ++sp) {
      __builtin_amdgcn_s_sleep(8);
      qa = gld(ws, W_QA + g * 1024 + lane * 16);
      qb = gld(ws, W_QB + g * 1024 + lane * 16);
      if (__all(gbad(qb, ta, tb, gbad(qa, ta, tb, 0u)) == 0u)) break;
      if (sp > SPIN_CAP) return;
    }
    *(LAS v2u*)(L + L_Q + lane * 8) = v2u{qa.x, qa.z};
    *(LAS v2u*)(L + L_Q + 512 + lane * 8) = v2u{qb.x, qb.z};
    ctl[C_TAGA] = ta; ctl[C_TAGB] = tb;
    lgkm0();
    ctl[C_QRDY] = 1;
#pragma unroll 1
    for (int k = 0; k <= NT; ++k) {
      if (!wait_ge<2>(ctl, C_WANT, k + 1)) return;
      unsigned t = 0;
      if (lane == 0) t = __hip_atomic_fetch_add((GAS unsigned*)(a.ws + W_CTR + g * 256), 1u, __ATOMIC_RELAXED, __HIP_MEMORY_SCOPE_AGENT);
      t = rfl(t);
      ctl[C_TICK + k] = t;
      if (t >= (unsigned)NT) break;
    }
    if (!wait_ge<1>(ctl, C_T1, 1)) return;
    hint_set(ws, lane < NREP ? W_F2 + (side * NREP + lane) * 1024 + prod * 8 : OOB, ctl[C_T1], ta);
    if (prod < 72) {
      const int ho = W_F2 + (side * NREP + prod % NREP) * 1024 + lane * 8;
      ctl[C_T2] = (hint_time<true>(ws, ho, ho + 512, ta, tstart) + tstart + DT2) | 1u;
    }
    return;
  }
  if (w == 2) {
    asm volatile("s_barrier" ::: "memory");
    const int nr1 = side ? 56 : 8;
    const int ta_ = (g & 15) * 2 + (g >> 4);
    const int b0 = side ? W_P1 + g * P1_STR : W_P + ta_ * P_STR, b1 = W_P + (side ? 64 + g : 32 + ta_) * P_STR, d0 = side ? 8192 : 1024, dest = 2 * (j & 3);
    int voff = b0 + dest * d0 + lane * 16;
    int voffb = voff + d0;
    int voff7 = b1 + dest * 1024 + lane * 16;
    int voff8 = lane >= 56 ? b1 + 8192 + (lane - 56) * 16 : b0 + 8 * d0 + (lane < nr1 ? lane : 0) * 16;
    asm volatile("" : "+v"(voff), "+v"(voffb), "+v"(voff7), "+v"(voff8));
    if (!wait_ge<16>(ctl, C_LATE, 1)) return;
    if (!wait_ge<2>(ctl, C_PHASE, 2)) return;
    const unsigned ta = rfl(ctl[C_TAGA]), tb = rfl(ctl[C_TAGB]);
    const int hoff = side ? (lane < 8 ? W_F1 + (g * 8 + lane) * 128 : OOB) : (lane < 16 ? W_F1 + ((g & 15) * 8 + (lane >> 3) * 128 + (lane & 7)) * 128 : OOB);
    wait_until(tstart, hint_time<false>(ws, hoff, OOB, ta, tstart) + DT1);
    v4u X[2][8], ML;
#pragma unroll 1
    for (unsigned sp = 0;; ++sp) {
      const unsigned tr = now32();
      ML = gld(ws, voff8);
#pragma unroll
      for (int i = 0; i < 7; ++i) { X[0][i] = gld(ws, voff, side ? i * 1024 : 0); X[1][i] = gld(ws, voffb, side ? i * 1024 : 0); }
      X[0][7] = gld(ws, voff7); X[1][7] = gld(ws, voff7 + 1024);
      ctl[C_T1] = tr | 1u;
      __builtin_amdgcn_sched_barrier(0);
      const bool seg1 = lane >= 56;
      const float mr = fbits(ML.x);
      const float M = max8(mr);
      const float M0 = max8to64(seg1 ? -INFINITY : M);
      const float f = (lane >= nr1 && !seg1) ? 0.f : ex2(mr - (seg1 ? M : M0));
      float fb[8];
#pragma unroll
      for (int b = 0; b < 8; ++b) fb[b] = fbits(__builtin_amdgcn_ds_bpermute((8 * b + (lane >> 3)) * 4, ubits(f)));
      float Ls = f * fbits(ML.z);
      Ls += dpp<0xB1>(Ls); Ls += dpp<0x4E>(Ls); Ls += dpp<0x141>(Ls);
      const float L1 = fbits(__builtin_amdgcn_readlane(ubits(Ls), 56));
      float L0 = seg1 ? 0.f : Ls;
      L0 += dpp<0x128>(L0); L0 = xsum(L0);
      const float inv = __builtin_amdgcn_rcpf(lane >= 32 ? L1 : L0);
      __builtin_amdgcn_sched_barrier(0);
      unsigned bad = gbad(ML, ta, tb, 0u);
#pragma unroll
      for (int i = 0; i < 8; ++i) bad = gbad(X[1][i], ta, tb, gbad(X[0][i], ta, tb, bad));
      float oe[2], oo[2];
#pragma unroll
      for (int h = 0; h < 2; ++h) {
        float o0 = 0.f, o1 = 0.f;
#pragma unroll
        for (int b = 0; b < 7; ++b) { o0 = fmaf(fb[b], fbits(X[h][b].x), o0); o1 = fmaf(fb[b], fbits(X[h][b].z), o1); }
        oe[h] = rs32(o0, fb[7] * fbits(X[h][7].x)); oo[h] = rs32(o1, fb[7] * fbits(X[h][7].z));
      }
      float e = rs16(oe[0], oe[1]), o = rs16(oo[0], oo[1]);
      e += dpp<0x128>(e); o += dpp<0x128>(o);
      *(volatile LAS unsigned*)(L + L_OP + (lane >> 4) * 32 + (lane & 7) * 4) = pk_bf16(e * inv, o * inv);
      if (__all(bad == 0u)) break;
      if (sp > SPIN_CAP) return;
      __builtin_amdgcn_s_sleep(4);
    }
    ctl[C_OSEQ] = 1;
    return;
  }
  {
    const int qw = w - 3;
    const int r0 = qw * 64 + (lane >> 2) * 4;
    const unsigned char* xp = (cb < 128 ? a.xA : a.xB) + r0 * 2;
    const unsigned char* wp = (cb < 128 ? a.wqA : a.wqB) + (size_t)r0 * 8192 + (cb & 127) * 64 + (lane & 3) * 16;
    v4u Wq[4];
#pragma unroll
    for (int i = 0; i < 4; ++i) Wq[i] = __builtin_nontemporal_load((const GAS v4u*)(wp + i * 8192));
    const v2u X0 = *(const GAS v2u*)xp, X1 = *(const GAS v2u*)(xp + 1152);
    asm volatile("s_barrier" ::: "memory");
    float ac[2][8];
#pragma unroll
    for (int k = 0; k < 8; ++k) { ac[0][k] = 0.f; ac[1][k] = 0.f; }
#pragma unroll
    for (int s = 0; s < 2; ++s)
#pragma unroll
      for (int d = 0; d < 4; ++d) {
        const unsigned lo = __builtin_amdgcn_perm(Wq[2 * s + 1][d], Wq[2 * s][d], 0x05040100u), hi = __builtin_amdgcn_perm(Wq[2 * s + 1][d], Wq[2 * s][d], 0x07060302u);
        ac[0][2 * d] = dot2(lo, X0[s], ac[0][2 * d]); ac[0][2 * d + 1] = dot2(hi, X0[s], ac[0][2 * d + 1]);
        ac[1][2 * d] = dot2(lo, X1[s], ac[1][2 * d]); ac[1][2 * d + 1] = dot2(hi, X1[s], ac[1][2 * d + 1]);
      }
    float b8[8], c4[4];
#pragma unroll
    for (int k = 0; k < 8; ++k) b8[k] = rs32(ac[0][k], ac[1][k]);
#pragma unroll
    for (int k = 0; k < 4; ++k) { float t = rs16(b8[k], b8[k + 4]); t += dpp<0x128>(t); t += dpp<0x124>(t); c4[k] = t; }
    if ((lane & 12) == 0) *(LAS v4f*)(L + L_QP + qw * 256 + (lane >> 5) * 128 + (lane & 3) * 32 + ((lane >> 4) & 1) * 16) = v4f{c4[0], c4[1], c4[2], c4[3]};
    lds_inc(L, C_QCNT, lane);
  }
  const int r = lane >> 4, c = lane & 15;
  const unsigned kof = c * 256 + r * 16 + (c & 3) * 64;
  const unsigned vof = (4 * r + (c >> 2)) * 256 + (c >> 2) * 64 + (c & 3) * 8;
  if (w >= 4) {
    const unsigned aw = w - 4, off = aw * 8192, an = aw >> 1;
    v4u K0[4], K1[4], V[8];
    if (!wait_ge<1>(ctl, C_FULL, 2 * aw + 2)) return;
    read_k(L, off + kof, 0, K0); read_k(L, off + kof, 4096, K1);
    lds_inc(L, C_FREE + an, lane);
    if (!wait_ge<1>(ctl, C_FULL, 2 * aw + 18)) return;
    read_v(L, 65536 + off + vof, 0, V);
    lds_inc(L, C_FREE + 4 + an, lane);
    if (!wait_ge<1>(ctl, C_QRDY, 1)) return;
    const LAS v4u* qp = (const LAS v4u*)(L + L_Q) + r + (c >> 3) * 16;
    const v4u q[4] = {qp[0], qp[4], qp[8], qp[12]};
    v4f s0 = round2(qk(K0, q)), s1 = round2(qk(K1, q));
    const float M = xmax(fmaxf(max4(s0), max4(s1))) * L2E;
    s0 = expo(s0, M); s1 = expo(s1, M);
    const v4u P = {pk_bf16(s0[0], s0[1]), pk_bf16(s0[2], s0[3]), pk_bf16(s1[0], s1[1]), pk_bf16(s1[2], s1[3])};
    const float Ls = xsum(sum4(s0) + sum4(s1));
    v4f acc[8];
#pragma unroll
    for (int blk = 0; blk < 8; ++blk) acc[blk] = mfma(V[blk], P, v4f{0.f, 0.f, 0.f, 0.f});
#pragma unroll
    for (int k = 0; k < 4; ++k) acc[k] = sel4(bitmask(c, 2), acc[k + 4], acc[k]);
#pragma unroll
    for (int k = 0; k < 2; ++k) acc[k] = sel4(bitmask(c, 1), acc[k + 2], acc[k]);
    acc[0] = sel4(bitmask(c, 0), acc[1], acc[0]);
    LAS unsigned char* rec = L + L_REC + ((c >> 3) * 8 + aw) * REC_B;
    *(LAS v4f*)(rec + 16 + ((c & 7) * 16 + r * 4) * 4) = acc[0];
    if ((lane & 0x37) == 0) *(LAS v2f*)rec = v2f{M, Ls};
    if (last_arriver(L, C_CNT, 8, lane)) {
#pragma unroll 1
      for (int e = 0; e < 2; ++e) {
        float Mo, Lo;
        const v2f o = merge_rec<8>(L + L_REC + e * 8 * REC_B, lane, Mo, Lo);
        const int base = W_P + (g * 2 + e) * P_STR;
        if (park_rec(L, ctl, e, base + j * 128, 1024, base + 8192 + j * 16, lane, lane, o, Mo, Lo)) {
          const unsigned ta_ = ctl[C_TAGA], tb_ = ctl[C_TAGB];
          gst(ws, base + j * 128 + (lane >> 3) * 1024 + (lane & 7) * 16, ubits(o.x), ubits(o.y), ta_, tb_);
          if (lane == 0) gst(ws, base + 8192 + j * 16, ubits(Mo), ubits(Lo), ta_, tb_);
        }
      }
    }
  }
  if (w < 8) {
    const int k = w - 3;
    const unsigned char* pw = (side ? a.woB : a.woA) + (size_t)((side ? g : 2 * (g & 15) + (g >> 4)) * 128 + (j & 3) * 32) * 1152;
    const bool live = k < 4 || c < 8;
    if (!wait_ge<16>(ctl, C_LATE, 1)) return;
    const unsigned ta = rfl(ctl[C_TAGA]), tb = rfl(ctl[C_TAGB]);
    if (k == 0) {
      const unsigned t0 = __builtin_nontemporal_load((const GAS unsigned*)(pw + (lane & 1) * (31 * 1152))), t1 = __builtin_amdgcn_raw_buffer_load_b32(ws, lane & 1 ? W_SAFE1 : W_SAFE2, 0, SC1);
      asm volatile("" ::"v"(t0), "v"(t1));
    }
    if (!wait_ge<2>(ctl, C_PHASE, 1)) return;
    const unsigned held = rfl(ctl[C_HELD]);
    asm volatile("" ::: "memory");
#pragma unroll 1
    for (int slot = k; slot < NHOLD; slot += 5) {
      if (!(held >> slot & 1)) continue;
      LAS unsigned char* h = L + L_HOLD + slot * HOLD_B;
      const v4u hd = *(LAS v4u*)h;
      const v2u ml = *(LAS v2u*)(h + 16), o = *(LAS v2u*)(h + 32 + lane * 8);
      gst(ws, hd.x + (lane >> 3) * hd.y + (lane & 7) * 16, o.x, o.y, ta, tb);
      gst(ws, lane == 0 ? hd.z : OOB, ml.x, ml.y, ta, tb);
    }
    if (!wait_ge<2>(ctl, C_PHASE, 2)) return;
    v4u Wf[8], Wr[8];
    const __amdgpu_buffer_rsrc_t rw = mkrs(pw, 32 * 1152);
#pragma unroll
    for (int i = 0; i < 8; ++i) Wr[i] = __builtin_amdgcn_raw_buffer_load_b128(rw, (live ? 8 * r * 1152 + k * 256 + c * 16 : OOB) + i * 1152, 0, 2);
#pragma unroll
    for (int e = 0; e < 4; ++e)
#pragma unroll
      for (int d = 0; d < 4; ++d) {
        Wf[2 * e][d] = __builtin_amdgcn_perm(Wr[2 * d + 1][e], Wr[2 * d][e], 0x05040100u);
        Wf[2 * e + 1][d] = __builtin_amdgcn_perm(Wr[2 * d + 1][e], Wr[2 * d][e], 0x07060302u);
      }
    int y0 = live ? W_Y + ((side ? 3 - (r & 1) : (r & 1)) * 36 + k * 8 + (c >> 2)) * YS + prod * 128 + (2 * (c & 3) + (r >> 1)) * 16 : OOB, y1 = y0 + (k < 4 ? 4 : 2) * YS;
    const unsigned mh = bitmask(r, 1);
    asm volatile("" : "+v"(y0), "+v"(y1), "+v"(Wf[0]), "+v"(Wf[1]), "+v"(Wf[2]), "+v"(Wf[3]), "+v"(Wf[4]), "+v"(Wf[5]), "+v"(Wf[6]), "+v"(Wf[7]));
    v4u oa;
#pragma unroll 1
    for (unsigned sp = 0;; ++sp) {
      const unsigned fl = ctl[C_OSEQ];
      oa = *(volatile LAS v4u*)(L + L_OP + ((c >> 2) & 1) * 64 + r * 16);
      if (rfl(fl)) break;
      if (sp > SPIN_CAP) return;
      __builtin_amdgcn_s_sleep(1);
    }
#pragma unroll
    for (int i = 0; i < 2; ++i) {
      v4f d[4];
#pragma unroll
      for (int f = 0; f < 4; ++f) d[f] = mfma(oa, Wf[(f >> 1) * 4 + 2 * i + (f & 1)], v4f{0.f, 0.f, 0.f, 0.f});
      gst(ws, i ? y1 : y0, ubits(sel(mh, d[2][0], d[0][0])), ubits(sel(mh, d[3][0], d[1][0])), ta, tb);
    }
    return;
  }
  const unsigned ta = rfl(ctl[C_TAGA]), tb = rfl(ctl[C_TAGB]);
  const int cw = w - 8;
  {
    float m = -INFINITY, l = 0.f;
    v4f acc[8];
    bool fresh = true, freshv = true;
    const unsigned mk0 = bitmask(c, 0), mk1 = bitmask(c, 1), mk2 = bitmask(c, 2), mk3 = bitmask(c, 3);
    const int gid = (c >> 1) * 8 + 2 * r + (c & 1);
    int mode = 1;
    unsigned nk = 0x7fffffffu;
#pragma unroll 1
    for (unsigned n = 4 + cw;; n = n + 4 < NS ? n + 4 : n < NS ? NS : n + 1) {
      bool stop = false;
      unsigned dsc = cw << 8;
#pragma unroll 1
      for (unsigned sp = 0; n >= NS; ++sp) {
        const unsigned dd = ctl[C_DESC + n - NS], en = ctl[mode == 1 ? C_NK : C_END];
        dsc = rfl(dd);
        if (n >= rfl(en) || sp > SPIN_CAP) {
          if (mode != 1) { stop = true; break; }
          nk = n; n = 4 + cw; mode = 2;
        } else if (dsc == 0xffffffffu) __builtin_amdgcn_s_sleep(2);
        else if ((dsc >> 8 & 3) == (unsigned)cw) break;
        else ++n;
      }
      if (stop) break;
      if (n >= nk) mode = 3;
      const bool dyn = n >= NS;
      const bool pubn = dyn ? (dsc >> 10 & 1) : (n >= NS - 4 || (n & ~3u) == 16);
      const int base = n >= 20 ? W_P1 + g * P1_STR : W_P + (64 + g) * P_STR, dstr = n >= 20 ? 8192 : 1024, rec = dyn ? dsc & 255 : j, slot = dyn ? dsc >> 12 & 255 : 2 + (n >= 20);
      const int gi = dyn ? gid : lane;
      const unsigned sk = n < nk ? n + 4 : 2 * n - 1, sv = n + 1 < nk ? n + nk : 2 * n + 2;
      int ad = base + rec * 128 + (gi >> 3) * dstr + (gi & 7) * 16, am = lane == 0 ? base + 8 * dstr + rec * 16 : OOB;
      unsigned ka = (sk & 7) * 16384 + kof, va = (sv & 7) * 16384 + vof;
      LAS unsigned char* pb = L + L_PB + (mode == 3 ? 0 : n - 4) * PB_B;
      v4u P0, P1, st;
      float al, Ls, mrec;
      const bool first = freshv;
      if ((mode & 2) && freshv) {
#pragma unroll
        for (int blk = 0; blk < 8; ++blk) acc[blk] = v4f{0.f, 0.f, 0.f, 0.f};
        freshv = false;
      }
      if (mode & 1) {
        const LAS v4u* qp = (const LAS v4u*)(L + L_Q) + r + (n < 20 ? 32 : 48);
        v4u q[4] = {qp[0], qp[4], qp[8], qp[12]};
        if (fresh) { m = -INFINITY; l = 0.f; }
        fresh = pubn;
        asm volatile("" : "+v"(ad), "+v"(am), "+v"(ka), "+v"(va), "+v"(q[0]), "+v"(q[1]), "+v"(q[2]), "+v"(q[3]));
        if (!wait_ge<1>(ctl, C_FULL, 4 * sk + 4)) return;
        v4f s[4];
        v4u K[4][4];
#pragma unroll
        for (int uu = 0; uu < 4; ++uu) read_k(L, ka, uu * 4096, K[uu]);
        __builtin_amdgcn_sched_barrier(0);
#pragma unroll
        for (int uu = 0; uu < 4; ++uu) s[uu] = qk(K[uu], q);
        __builtin_amdgcn_sched_barrier(0);
        lgkm0();
        ctl[C_FREE + (sk & 7)] = sk + 1;
        v4f sc = round2(sel4(mk3, sel4(mk2, s[3], s[2]), sel4(mk2, s[1], s[0])));
        const float mn = fmaxf(max4to64(max4(sc)) * L2E, m);
        st = *(volatile LAS v4u*)(L + L_CTL);
        sc = expo(sc, mn);
        const unsigned px = pk_bf16(sc[0], sc[1]), py = pk_bf16(sc[2], sc[3]);
        P0 = v4u{bcast16<0>(px), bcast16<0>(py), bcast16<4>(px), bcast16<4>(py)}; P1 = v4u{bcast16<8>(px), bcast16<8>(py), bcast16<12>(px), bcast16<12>(py)};
        al = ex2(m - mn);
        mrec = m = mn;
        l = fmaf(l, al, sum4(sc));
        Ls = l + dpp<0x124>(l);
        Ls += dpp<0x128>(Ls);
        Ls = xsum(Ls);
        if (mode == 1) {
          *(LAS v4u*)(pb + r * 16) = P0; *(LAS v4u*)(pb + 64 + r * 16) = P1; *(LAS v4f*)(pb + 128) = v4f{al, Ls, mn, 0.f};
        }
      } else {
        P0 = *(LAS v4u*)(pb + r * 16); P1 = *(LAS v4u*)(pb + 64 + r * 16);
        const v4f t = *(LAS v4f*)(pb + 128);
        al = t.x; Ls = t.y; mrec = t.z;
        st = *(volatile LAS v4u*)(L + L_CTL);
      }
      if (mode == 1) continue;
      if (!first) {
#pragma unroll
        for (int blk = 0; blk < 8; ++blk) acc[blk] *= al;
      }
      asm volatile("" : "+v"(P0), "+v"(P1), "+v"(Ls), "+v"(acc[0]), "+v"(acc[1]), "+v"(acc[2]), "+v"(acc[3]), "+v"(acc[4]), "+v"(acc[5]), "+v"(acc[6]), "+v"(acc[7]));
      v4u V[8];
#define WAITV(need) _Pragma("unroll 1") for (unsigned sp = 0; rfl(st.x) < (need); ++sp) { if (sp > SPIN_CAP) return; if (sp) __builtin_amdgcn_s_sleep(1); st = *(volatile LAS v4u*)(L + L_CTL); } \
      asm volatile("" ::: "memory");
      WAITV(4 * sv + 2)
      read_v(L, va, 0, V);
#pragma unroll
      for (int blk = 0; blk < 8; ++blk) acc[blk] = mfma(V[blk], P0, acc[blk]);
      WAITV(4 * sv + 4)
      const unsigned tl = now32(), nend = rfl(st.y);
      read_v(L, va, 8192, V);
      hint_set(ws, lane == 0 && n + 1 == nend ? W_F1 + wg * 128 : OOB, tl, ta);
#pragma unroll
      for (int blk = 0; blk < 8; ++blk) acc[blk] = mfma(V[blk], P1, acc[blk]);
      if (pubn) {
        v2f o[8];
#pragma unroll
        for (int k = 0; k < 8; ++k) o[k] = sel2(mk0, v2f{acc[k][2], acc[k][3]}, v2f{acc[k][0], acc[k][1]});
#pragma unroll
        for (int k = 0; k < 4; ++k) o[k] = sel2(mk3, o[k + 4], o[k]);
#pragma unroll
        for (int k = 0; k < 2; ++k) o[k] = sel2(mk2, o[k + 2], o[k]);
        o[0] = sel2(mk1, o[1], o[0]);
        bool now = dyn && (slot >= NHOLD || rfl(st.z) != 0);
        if (!now) {
          bool emit = true;
          if (!dyn) {
            LAS unsigned char* rp = L + L_REC + (8 + slot * 4) * REC_B;
            *(LAS v2f*)(rp + cw * REC_B + 16 + gid * 8) = o[0];
            if (lane == 0) *(LAS v2f*)(rp + cw * REC_B) = v2f{mrec, Ls};
            emit = last_arriver(L, C_CNT + slot - 1, 4, lane);
            if (emit) o[0] = merge_rec<4>(rp, lane, mrec, Ls);
          }
          if (emit) now = park_rec(L, ctl, slot, base + rec * 128, dstr, base + 8 * dstr + rec * 16, gi, lane, o[0], mrec, Ls);
          if (!now) { ad = OOB; am = OOB; }
        }
        gst(ws, ad, ubits(o[0].x), ubits(o[0].y), ta, tb);
        gst(ws, am, ubits(mrec), ubits(Ls), ta, tb);
        freshv = true;
      }
      lgkm0();
      ctl[C_FREE + (sv & 7)] = sv + 1;
    }
  }
  if (prod >= 72) return;
  const int cmb = side * 2 + (prod & 1), rho = prod >> 1;
  int voff = W_Y + (cmb * 36 + rho) * YS + cw * 4096 + lane * 16;
  const int yi = rho < 32 ? (rho >> 2) & 1 : (rho >> 1) & 1, yq = rho < 32 ? rho & 3 : rho & 1;
  int oo = lane < 16 ? ((cmb & 1) * 1152 + (cmb >> 1) * 576 + (rho >> 3) * 128 + (yq * 4 + (lane >> 2)) * 8 + ((lane >> 1) & 1) * 4 + yi * 2 + (lane & 1)) * 2 : OOB;
  asm volatile("" : "+v"(voff), "+v"(oo));
  if (!wait_ge<4>(ctl, C_T1, 1)) return;
  if (!wait_ge<1>(ctl, C_T2, 1)) return;
  wait_until(tstart, rfl(ctl[C_T2]) - tstart);
  v4u X[4];
#pragma unroll 1
  for (unsigned sp = 0;; ++sp) {
#pragma unroll
    for (int i = 0; i < 4; ++i) X[i] = gld(ws, voff, i * 1024);
    unsigned bad = 0;
#pragma unroll
    for (int i = 0; i < 4; ++i) bad = gbad(X[i], ta, tb, bad);
    if (__all(bad == 0u)) break;
    if (sp > SPIN_CAP) return;
    __builtin_amdgcn_s_sleep(4);
  }
  float s = rs32((fbits(X[0].x) + fbits(X[1].x)) + (fbits(X[2].x) + fbits(X[3].x)), (fbits(X[0].z) + fbits(X[1].z)) + (fbits(X[2].z) + fbits(X[3].z)));
  s = rs16(s, s);
  s += dpp<0x128>(s);
  *(volatile LAS unsigned*)(L + L_FP + (((lane & 7) * 2 + (lane >> 5)) * 4 + cw) * 4) = min(ubits(s), 0xfffffffeu);
  if (cw != 0) return;
#pragma unroll 1
  for (unsigned sp = 0;; ++sp) {
    const v4u p = *(volatile LAS v4u*)(L + L_FP + (lane & 15) * 16);
    s = (fbits(p.x) + fbits(p.y)) + (fbits(p.z) + fbits(p.w));
    if (__all(p.y != ~0u && p.z != ~0u && p.w != ~0u)) break;
    if (sp > SPIN_CAP) return;
  }
  __builtin_amdgcn_raw_buffer_store_b16((unsigned short)pk_bf16(s, s), mkrs(a.out, 4608), oo, 0, 0);
  __builtin_amdgcn_raw_buffer_store_b64(v2u{ta, tb}, ws, cmb == 3 && rho == 0 && lane == 0 ? W_X : OOB, 0, SC1);
}

extern "C" void kernel_launch(void* const* d_in, const int* in_sizes, int n_in, void* d_out, int out_size, void* d_ws, size_t ws_size,
                              hipStream_t stream) {
  Args a;
  a.xA = (const unsigned char*)d_in[0]; a.kA = (const unsigned char*)d_in[1]; a.vA = (const unsigned char*)d_in[2];
  a.wqA = (const unsigned char*)d_in[3]; a.woA = (const unsigned char*)d_in[4];
  a.xB = (const unsigned char*)d_in[5]; a.kB = (const unsigned char*)d_in[6]; a.vB = (const unsigned char*)d_in[7];
  a.wqB = (const unsigned char*)d_in[8]; a.woB = (const unsigned char*)d_in[9];
  a.out = (unsigned char*)d_out; a.ws = (unsigned char*)d_ws;
  fused_decode<<<256, NTHR, 0, stream>>>(a);
}
```

```cpp
#include <hip/hip_runtime.h>
#include <stdint.h>

typedef unsigned v4u __attribute__((ext_vector_type(4)));
typedef unsigned v2u __attribute__((ext_vector_type(2)));
typedef float v4f __attribute__((ext_vector_type(4)));
typedef float v2f __attribute__((ext_vector_type(2)));
typedef short v4s __attribute__((ext_vector_type(4)));
typedef __bf16 bf16x2 __attribute__((ext_vector_type(2)));
typedef __bf16 bf16x8 __attribute__((ext_vector_type(8)));
#define LAS __attribute__((address_space(3)))
#define GAS __attribute__((address_space(1)))
#define DI __device__ __forceinline__

constexpr int WLD = 22;
constexpr int DT1 = 50;
constexpr int DT2 = 100;
constexpr int KAH = 3;
constexpr int NTHR = 12 * 64;
constexpr int NSTAT = 9;
constexpr int NS = 4 + 16 + NSTAT;
constexpr int POOLC = (16 - NSTAT) * 8;
constexpr int NT = 48, TP = POOLC - NT;
constexpr int WINE = (WLD + 7) / 8 * 2;
constexpr int NREP = 16;
constexpr int NKMAX = 52;
constexpr unsigned SPIN_CAP = 1u << 20;
constexpr int NHOLD = 16, HOLD_B = 544, REC_B = 528, PB_B = 144;
constexpr int L_CTL = 131072  , L_FP = L_CTL + 768  , L_Q = L_CTL + 1024, L_OP = L_Q + 1024, L_QP = L_OP + 128,
              L_REC = L_QP + 9 * 256, L_HOLD = L_REC + 24 * REC_B, L_PB = L_HOLD + NHOLD * HOLD_B, L_END = L_PB + (NKMAX - 4) * PB_B;
static_assert(L_END <= 163840 && TP >= 0 && TP <= NT && NT + 2 <= 64 && POOLC <= 64 && WINE >= 2 && WINE <= 8, "");
enum { C_FULL = 0  , C_END = 1  , C_PHASE = 2  , C_NK = 3, C_WANT = 4  ,
       C_T1 = 5  , C_TAGA = 6, C_TAGB = 7, C_FREE = 8  , C_CNT = 16  , C_QCNT = 19, C_QRDY = 20, C_HELD = 21, C_LATE = 23, C_OSEQ = 22,
       C_T2 = 24  , C_TICK = 64, C_DESC = 128 };
constexpr int W_X = 0, W_CTR = 4096, W_F1 = 16384  , W_F2 = W_F1 + 32768  , W_QA = W_F2 + 2 * NREP * 1024, W_QB = W_QA + 32768,
              W_P = W_QB + 32768, P_STR = 9216  , W_P1 = W_P + 96 * P_STR, P1_STR = 66560  ,
              W_Y = W_P1 + 32 * P1_STR, YS = 16384, W_ENDB = 256 + W_Y + 4 * 36 * YS  ;
constexpr int W_SAFE1 = W_P1 + 24 * P1_STR + 7424, W_SAFE2 = W_ENDB - 128;
constexpr float SCALE = 0.088388347648318440f, L2E = 1.4426950408889634f;
constexpr int SC1 = 16, OOB = 0x70000000;

struct Args {
  const unsigned char *xA, *kA, *vA, *wqA, *woA, *xB, *kB, *vB, *wqB, *woB;
  unsigned char *out, *ws;
};

DI float dot2(unsigned a, unsigned b, float c) { return __builtin_amdgcn_fdot2_f32_bf16(__builtin_bit_cast(bf16x2, a), __builtin_bit_cast(bf16x2, b), c, false); }
template <int CTRL> DI float dpp(float x) { return __builtin_bit_cast(float, __builtin_amdgcn_mov_dpp(__builtin_bit_cast(int, x), CTRL, 0xf, 0xf, true)); }
template <int LANE> DI unsigned bcast16(unsigned x) { return __builtin_amdgcn_mov_dpp(x, 0x150 + LANE, 0xf, 0xf, true); }
DI unsigned pk_bf16(float lo, float hi) {
  unsigned r = __builtin_bit_cast(unsigned, bf16x2{(__bf16)lo, (__bf16)hi});
  asm("" : "+v"(r));
  return r;
}
DI float bf_lo(unsigned u) { return __builtin_bit_cast(float, u << 16); }
DI float bf_hi(unsigned u) { return __builtin_bit_cast(float, u & 0xffff0000u); }
DI float fbits(unsigned u) { return __builtin_bit_cast(float, u); }
DI unsigned ubits(float f) { return __builtin_bit_cast(unsigned, f); }
DI unsigned rfl(unsigned x) { return __builtin_amdgcn_readfirstlane(x); }
DI float ex2(float x) { return __builtin_amdgcn_exp2f(x); }
DI v4f mfma(v4u a, v4u b, v4f c) { return __builtin_amdgcn_mfma_f32_16x16x32_bf16(__builtin_bit_cast(bf16x8, a), __builtin_bit_cast(bf16x8, b), c, 0, 0, 0); }
DI v2u trd(LAS unsigned char* p) { return __builtin_bit_cast(v2u, __builtin_amdgcn_ds_read_tr16_b64_v4i16((LAS v4s*)p)); }
DI float rs32(float x, float y) { auto s = __builtin_amdgcn_permlane32_swap(ubits(x), ubits(y), false, false); return fbits(s[0]) + fbits(s[1]); }
DI float rs16(float x, float y) { auto s = __builtin_amdgcn_permlane16_swap(ubits(x), ubits(y), false, false); return fbits(s[0]) + fbits(s[1]); }
DI float xsum(float x) { x = rs16(x, x); return rs32(x, x); }
DI float xmax(float x) {
  auto s = __builtin_amdgcn_permlane16_swap(ubits(x), ubits(x), false, false); x = fmaxf(fbits(s[0]), fbits(s[1]));
  auto t = __builtin_amdgcn_permlane32_swap(ubits(x), ubits(x), false, false); return fmaxf(fbits(t[0]), fbits(t[1]));
}
DI float max8(float x) {
  asm("s_nop 1\n\tv_max_f32_dpp %0, %0, %0 quad_perm:[1,0,3,2] row_mask:0xf bank_mask:0xf\n\ts_nop 1\n\tv_max_f32_dpp %0, %0, %0 quad_perm:[2,3,0,1] row_mask:0xf bank_mask:0xf\n\t"
      "s_nop 1\n\tv_max_f32_dpp %0, %0, %0 row_half_mirror row_mask:0xf bank_mask:0xf" : "+v"(x));
  return x;
}
DI float max8to64(float x) {
  float t;
  asm("s_nop 1\n\tv_max_f32_dpp %0, %0, %0 row_ror:8 row_mask:0xf bank_mask:0xf\n\tv_mov_b32 %1, %0\n\ts_nop 1\n\tv_permlane16_swap_b32 %0, %1\n\tv_max_f32 %0, %0, %1\n\t"
      "v_mov_b32 %1, %0\n\ts_nop 1\n\tv_permlane32_swap_b32 %0, %1\n\tv_max_f32 %0, %0, %1" : "+v"(x), "=&v"(t));
  return x;
}
DI float max4to64(float x) {
  asm("s_nop 1\n\tv_max_f32_dpp %0, %0, %0 row_ror:4 row_mask:0xf bank_mask:0xf" : "+v"(x));
  return max8to64(x);
}
DI void lgkm0() { asm volatile("s_waitcnt lgkmcnt(0)" ::: "memory"); }
template <int N> DI void waitvm() { asm volatile("s_waitcnt vmcnt(%0)" ::"n"(N) : "memory"); }
DI __amdgpu_buffer_rsrc_t mkrs(const void* p, unsigned bytes) { return __builtin_amdgcn_make_buffer_rsrc((void*)p, 0, bytes, 0x00020000); }
DI void dma8(__amdgpu_buffer_rsrc_t rs, unsigned voff, unsigned soff, unsigned lds) {
  unsigned keep, s2;
  asm volatile("s_mov_b32 %0, m0\n\ts_mov_b32 m0, %5\n\ts_add_u32 %1, %4, 0x1000\n\ts_nop 2\n\t"
               "buffer_load_dwordx4 %2, %3, %4 offen nt lds\n\t"
               "buffer_load_dwordx4 %2, %3, %4 offen offset:1024 nt lds\n\t"
               "buffer_load_dwordx4 %2, %3, %4 offen offset:2048 nt lds\n\t"
               "buffer_load_dwordx4 %2, %3, %4 offen offset:3072 nt lds\n\t"
               "s_add_u32 m0, m0, 0x1000\n\ts_nop 0\n\t"
               "buffer_load_dwordx4 %2, %3, %1 offen nt lds\n\t"
               "buffer_load_dwordx4 %2, %3, %1 offen offset:1024 nt lds\n\t"
               "buffer_load_dwordx4 %2, %3, %1 offen offset:2048 nt lds\n\t"
               "buffer_load_dwordx4 %2, %3, %1 offen offset:3072 nt lds\n\t"
               "s_mov_b32 m0, %0"
               : "=&s"(keep), "=&s"(s2) : "v"(voff), "s"(rs), "s"(soff), "s"(lds) : "memory", "scc");
}
DI v4u gld(__amdgpu_buffer_rsrc_t rs, int off, int soff = 0) { return __builtin_amdgcn_raw_buffer_load_b128(rs, off, soff, SC1); }
DI void gst(__amdgpu_buffer_rsrc_t rs, int off, unsigned a, unsigned b, unsigned ta, unsigned tb) { __builtin_amdgcn_raw_buffer_store_b128(v4u{a, ta, b, tb}, rs, off, 0, SC1); }
DI unsigned gbad(v4u g, unsigned ta, unsigned tb, unsigned acc) { return __builtin_amdgcn_bitop3_b32(g.w, tb, __builtin_amdgcn_bitop3_b32(g.y, ta, acc, 0xBE), 0xBE); }

typedef volatile LAS unsigned* ctl_t;
template <int SLP> DI bool wait_ge(ctl_t ctl, int idx, unsigned v) {
#pragma unroll 1
  for (unsigned s = 0; rfl(ctl[idx]) < v; ++s) {
    if (s > SPIN_CAP) return false;
    __builtin_amdgcn_s_sleep(SLP);
  }
  asm volatile("" ::: "memory");
  return true;
}
DI void lds_inc(LAS unsigned char* L, int idx, int lane) {
  lgkm0();
  if (lane == 0) __hip_atomic_fetch_add((LAS unsigned*)(L + L_CTL) + idx, 1u, __ATOMIC_RELAXED, __HIP_MEMORY_SCOPE_WORKGROUP);
}
DI bool last_arriver(LAS unsigned char* L, int idx, unsigned n, int lane) {
  lgkm0();
  unsigned old = 0;
  if (lane == 0) old = __hip_atomic_fetch_add((LAS unsigned*)(L + L_CTL) + idx, 1u, __ATOMIC_RELAXED, __HIP_MEMORY_SCOPE_WORKGROUP);
  old = rfl(old);
  asm volatile("" ::: "memory");
  return old == n - 1;
}
DI void read_k(LAS unsigned char* L, unsigned off, int imm, v4u (&K)[4]) {
#pragma unroll
  for (int kk = 0; kk < 4; ++kk) K[kk] = *(const LAS v4u*)(L + (off ^ (kk * 64)) + imm);
}
DI void read_v(LAS unsigned char* L, unsigned off, int imm, v4u (&V)[8]) {
#pragma unroll
  for (int blk = 0; blk < 8; ++blk) {
    LAS unsigned char* p = L + (off ^ ((blk >> 1) * 64)) + (blk & 1) * 32 + imm;
    const v2u x = trd(p), y = trd(p + 4096);
    V[blk] = v4u{x.x, x.y, y.x, y.y};
  }
  __builtin_amdgcn_sched_barrier(0);
}
DI v4f qk(const v4u (&K)[4], const v4u (&q)[4]) {
  v4f d = {0.f, 0.f, 0.f, 0.f};
#pragma unroll
  for (int kk = 0; kk < 4; ++kk) d = mfma(K[kk], q[kk], d);
  return d;
}
DI v4f round2(v4f d) {
  const unsigned x01 = pk_bf16(d[0], d[1]), x23 = pk_bf16(d[2], d[3]);
  const unsigned b01 = pk_bf16(bf_lo(x01) * SCALE, bf_hi(x01) * SCALE), b23 = pk_bf16(bf_lo(x23) * SCALE, bf_hi(x23) * SCALE);
  return v4f{bf_lo(b01), bf_hi(b01), bf_lo(b23), bf_hi(b23)};
}
DI float sel(unsigned mask, float a, float b) { return fbits(__builtin_amdgcn_bitop3_b32(mask, ubits(a), ubits(b), 0xCA)); }
DI v4f sel4(unsigned mask, v4f a, v4f b) { return v4f{sel(mask, a[0], b[0]), sel(mask, a[1], b[1]), sel(mask, a[2], b[2]), sel(mask, a[3], b[3])}; }
DI v2f sel2(unsigned mask, v2f a, v2f b) { return v2f{sel(mask, a[0], b[0]), sel(mask, a[1], b[1])}; }
DI unsigned bitmask(int x, int bit) { unsigned m = (unsigned)((x << (31 - bit)) >> 31); asm("" : "+v"(m)); return m; }
DI float max4(v4f d) { return fmaxf(fmaxf(d[0], d[1]), fmaxf(d[2], d[3])); }
DI float sum4(v4f d) { return (d[0] + d[1]) + (d[2] + d[3]); }
DI v4f expo(v4f s, float m2) { return v4f{ex2(fmaf(s[0], L2E, -m2)), ex2(fmaf(s[1], L2E, -m2)), ex2(fmaf(s[2], L2E, -m2)), ex2(fmaf(s[3], L2E, -m2))}; }

DI unsigned now32() { return (unsigned)__builtin_amdgcn_s_memrealtime(); }
DI void wait_until(unsigned t0, unsigned until) {
#pragma unroll 1
  for (unsigned sp = 0; sp < 64 && now32() - t0 < until; ++sp) { }
}
DI void hint_set(__amdgpu_buffer_rsrc_t ws, int off, unsigned t, unsigned tag) {
  asm volatile("buffer_atomic_swap_x2 %0, %1, %2, 0 offen" ::"v"(v2u{t, tag}), "v"(off), "s"(ws) : "memory");
}
template <bool TWO> DI unsigned hint_time(__amdgpu_buffer_rsrc_t ws, int off, int off2, unsigned tag, unsigned t0) {
  unsigned rel = 0;
#define HP1(A0, A1, o) asm volatile("v_mov_b32 v16" #A0 ", 0\n\tv_mov_b32 v16" #A1 ", 0\n\tbuffer_atomic_add_x2 v[16" #A0 ":16" #A1 "], %0, %1, 0 offen sc0" \
                                    ::"v"(o), "s"(ws) : "memory", "v16" #A0, "v16" #A1)
#define HC1(A0, A1, o, N) { unsigned t_, g_; asm volatile("s_waitcnt vmcnt(%2)\n\tv_mov_b32 %0, v16" #A0 "\n\tv_mov_b32 %1, v16" #A1 : "=v"(t_), "=v"(g_) : "n"(N) : "memory", "v16" #A0, "v16" #A1); \
                            const bool ok_ = g_ == tag; rel = ok_ ? max(rel, t_ - t0) : rel; o = ok_ ? OOB : o; }
  HP1(0, 1, off);
  if (TWO) HP1(2, 3, off2);
  __builtin_amdgcn_s_sleep(8);
#pragma unroll 1
  for (unsigned sp = 0; sp < 4096; ++sp) {
    HP1(4, 5, off);
    if (TWO) HP1(6, 7, off2);
    HC1(0, 1, off, TWO ? 3 : 1)
    if (TWO) HC1(2, 3, off2, 2)
    if (__all(off == OOB && off2 == OOB)) break;
    HP1(0, 1, off);
    if (TWO) HP1(2, 3, off2);
    HC1(4, 5, off, TWO ? 3 : 1)
    if (TWO) HC1(6, 7, off2, 2)
    if (__all(off == OOB && off2 == OOB)) break;
  }
  return rfl((unsigned)max8to64(max8((float)rel)));
}
#undef HP1
#undef HC1
DI bool park_rec(LAS unsigned char* L, ctl_t ctl, int slot, int adr, int dstr, int mladr, int gi, int lane, v2f o, float m, float l) {
  LAS unsigned char* h = L + L_HOLD + slot * HOLD_B;
  *(LAS v2f*)(h + 32 + gi * 8) = o;
  if (lane == 0) { *(LAS v4u*)h = v4u{(unsigned)adr, (unsigned)dstr, (unsigned)mladr, 0u}; *(LAS v2f*)(h + 16) = v2f{m, l}; }
  lgkm0();
  if (lane == 0) __hip_atomic_fetch_or((LAS unsigned*)(L + L_CTL) + C_HELD, 1u << slot, __ATOMIC_RELAXED, __HIP_MEMORY_SCOPE_WORKGROUP);
  lgkm0();
  return rfl(ctl[C_PHASE]) != 0;
}
template <int NR> DI v2f merge_rec(LAS unsigned char* rec, int lane, float& Mo, float& Lo) {
  v2f av[NR];
  const v2f ml = *(LAS v2f*)(rec + (lane & (NR - 1)) * REC_B);
#pragma unroll
  for (int w = 0; w < NR; ++w) av[w] = *(LAS v2f*)(rec + w * REC_B + 16 + lane * 8);
  const float M = max8(ml.x), f = ex2(ml.x - M);
  float Ls = f * ml.y; Ls += dpp<0xB1>(Ls); Ls += dpp<0x4E>(Ls);
  if (NR == 8) Ls += dpp<0x141>(Ls);
  float a0 = 0.f, a1 = 0.f;
#pragma unroll
  for (int w = 0; w < NR; ++w) { const float fw = fbits(__builtin_amdgcn_readlane(ubits(f), w)); a0 = fmaf(fw, av[w].x, a0); a1 = fmaf(fw, av[w].y, a1); }
  Mo = M; Lo = Ls;
  return v2f{a0, a1};
}

__global__ void __launch_bounds__(NTHR, 3) fused_decode(Args a) {
  __shared__ __attribute__((aligned(16))) unsigned char smem[L_END];
  LAS unsigned char* L = (LAS unsigned char*)smem;
  ctl_t ctl = (ctl_t)(L + L_CTL);
  asm volatile("" ::"s"(a.xA), "s"(a.kA), "s"(a.vA), "s"(a.wqA), "s"(a.woA), "s"(a.xB), "s"(a.kB), "s"(a.vB), "s"(a.wqB), "s"(a.woB), "s"(a.out), "s"(a.ws));
  const int wg = blockIdx.x, g = wg >> 3, j = wg & 7;
  const int lane = threadIdx.x & 63, w = rfl(threadIdx.x >> 6);

  if (w == 0) {
    __builtin_amdgcn_s_waitcnt(0);
    const unsigned lb = (unsigned)(uintptr_t)L;
    const unsigned voff = (lane * 16) ^ ((lane >> 4) << 6);
    const unsigned offA = g * (2u << 20) + (3u << 19) + j * 16384;
    const __amdgpu_buffer_rsrc_t rk = mkrs(a.kA, 64u << 20);
    dma8(rk, voff, offA, lb); dma8(rk, voff, offA + 8192, lb + 8192);
    asm volatile("s_barrier" ::: "memory");
    __builtin_amdgcn_s_setprio(3);
    int u = 4, pub = 0;
    unsigned sg = 1;
#define LWAIT(idx, v, out) _Pragma("unroll 1") for (unsigned sp = 0;; ++sp) { \
      out = rfl(ctl[idx]); if (out + 1u > (v)) break; \
      if (sp > SPIN_CAP) return; \
      if (sp == 6) { waitvm<0>(); pub = u; ctl[C_FULL] = pub; } \
      __builtin_amdgcn_s_sleep(1); }
#define HALF(rs, so, ld) do { waitvm<WLD>(); if (u - WINE > pub) { pub = u - WINE; ctl[C_FULL] = pub; } dma8(rs, voff, so, ld); u += 2; } while (0)
#define SEG(rs, so) do { unsigned fr_; LWAIT(C_FREE + (sg & 7), sg < 8 ? 0u : sg < 16 ? 2u : sg - 7, fr_) \
      const unsigned ld_ = lb + (sg & 7) * 16384; HALF(rs, so, ld_); HALF(rs, (so) + 8192, ld_ + 8192); ++sg; } while (0)
    const unsigned b1 = (32 + g) * (2u << 20);
    unsigned k = 0, dsc = 0, nk = NS;
    bool second = false;
#define POOLOFF(d) (b1 + ((NSTAT * 8 + ((d) >> 20) + g * 8) & 127) * 16384)
#define NEXT_ITEM(t, n) do { \
      if (second) { dsc += (1u << 20) + (1u << 10); second = false; } \
      else { second = t < (unsigned)TP; dsc = (8 + t) | ((NS + k) & 3) << 8 | (second ? 0u : 1u << 10) | (4 + k) << 12 | (second ? 2 * t : TP + t) << 20; ++k; } \
      ctl[C_DESC + (n) - NS] = dsc; } while (0)
    const __amdgpu_buffer_rsrc_t rkb = mkrs(a.kB, 128u << 20), rvb = mkrs(a.vB, 128u << 20);
#pragma unroll 1
    for (int run = 0; run < 4; ++run) {
      const __amdgpu_buffer_rsrc_t rs = mkrs(run == 0 ? a.kA : run == 1 ? a.vA : run == 2 ? a.kB : a.vB, run < 2 ? 64u << 20 : 128u << 20);
      const int cnt = run < 2 ? 4 : NS - 2 - run;
#pragma unroll 1
      for (int i = run == 0; i < cnt; ++i) {
        const unsigned so = run < 2 ? offA + i * 131072 : (g + (i & 16) * 2) * (2u << 20) + ((i * 8 + wg) & 127) * 16384;
        if (sg == NS + 4 - KAH) ctl[C_WANT] = 1;
        SEG(rs, so);
      }
      if (run == 2) {
#pragma unroll 1
        for (;;) {
          unsigned t = 0;
          if (!second) { LWAIT(C_TICK + k, 0u, t) if (t >= (unsigned)NT) break; }
          NEXT_ITEM(t, nk);
          const bool more = second && nk + 4 <= NKMAX;
          if (more) ctl[C_WANT] = k + 1;
          SEG(rkb, POOLOFF(dsc));
          ++nk;
          if (!second && (t >= (unsigned)TP || nk + 2 > NKMAX)) break;
        }
        ctl[C_NK] = nk;
      }
    }
    ctl[C_LATE] = 1;
    unsigned sv = b1 + (((NS - 5) * 8 + wg) & 127) * 16384;
#pragma unroll 1
    for (unsigned n = NS;; ++n) {
      unsigned t = 0, sk = 0;
      if (n + 2 >= nk) ctl[C_WANT] = k + 1;
      if (n < nk) sk = POOLOFF(rfl(ctl[C_DESC + n - NS]));
      else {
        if (!second) { LWAIT(C_TICK + k, 0u, t) }
        if (t < (unsigned)NT) {
          NEXT_ITEM(t, n);
          sk = POOLOFF(dsc);
          ctl[C_WANT] = k + 1;
          SEG(rkb, sk);
        } else {
          ctl[C_END] = n; ctl[C_PHASE] = 1; ++sg;
        }
      }
      SEG(rvb, sv);
      if (t >= (unsigned)NT) break;
      sv = sk;
    }
    ctl[C_PHASE] = 2;
#define DRAIN(k_) if (WINE >= k_) { waitvm<4 * k_>(); const int p_ = u - k_ + (k_ < 4 ? 4 : 0); if (p_ > pub) { pub = p_; ctl[C_FULL] = pub; } }
    DRAIN(8) DRAIN(6) DRAIN(4) DRAIN(2) DRAIN(0)
    return;
  }
  const __amdgpu_buffer_rsrc_t ws = mkrs(a.ws, W_ENDB);
  const unsigned tstart = now32();
  const int cb = (wg & ~15) + ((wg & 7) << 1) + ((wg >> 3) & 1);
  const int side = j >> 2, prod = g * 4 + (j & 3);

  if (w == 1) {
    const v2u ep = __builtin_amdgcn_raw_buffer_load_b64(ws, W_X, 0, SC1);
    if (cb >= 128 && (cb & 3) == 0 && lane == 0) __builtin_amdgcn_raw_buffer_store_b32(0u, ws, W_CTR + ((cb - 128) >> 2) * 256, 0, SC1);
    ctl[lane] = lane == C_END || lane == C_NK ? 0x7fffffffu : 0u;
    ctl[lane + 64] = 0xffffffffu; ctl[lane + 128] = 0xffffffffu; ctl[lane + 192] = 0xffffffffu;
    asm volatile("s_waitcnt lgkmcnt(0)\n\ts_barrier" ::: "memory");
    unsigned ta = rfl(ep.x) + 1u, tb = rfl(ep.y) + 1u;
    ta += ta == 0u; tb += tb == 0u;
    waitvm<0>();
    if (!wait_ge<1>(ctl, C_QCNT, 9)) return;
    if (lane < 16) {
      v4f s = {0.f, 0.f, 0.f, 0.f};
#pragma unroll
      for (int i = 0; i < 9; ++i) s += *(LAS v4f*)(L + L_QP + i * 256 + lane * 16);
      const int b = lane >> 3, quad = lane & 7;
      const int off = cb < 128 ? W_QA + ((b * 16 + (cb >> 3)) * 64 + (cb & 7) * 8 + quad) * 16 : W_QB + (((cb - 128) >> 2) * 64 + b * 32 + (cb & 3) * 8 + quad) * 16;
      gst(ws, off, pk_bf16(s.x, s.y), pk_bf16(s.z, s.w), ta, tb);
    }
    v4u qa, qb;
#pragma unroll 1
    for (unsigned sp = 0;; ++sp) {
      __builtin_amdgcn_s_sleep(8);
      qa = gld(ws, W_QA + g * 1024 + lane * 16);
      qb = gld(ws, W_QB + g * 1024 + lane * 16);
      if (__all(gbad(qb, ta, tb, gbad(qa, ta, tb, 0u)) == 0u)) break;
      if (sp > SPIN_CAP) return;
    }
    *(LAS v2u*)(L + L_Q + lane * 8) = v2u{qa.x, qa.z};
    *(LAS v2u*)(L + L_Q + 512 + lane * 8) = v2u{qb.x, qb.z};
    ctl[C_TAGA] = ta; ctl[C_TAGB] = tb;
    lgkm0();
    ctl[C_QRDY] = 1;
#pragma unroll 1
    for (int k = 0; k <= NT; ++k) {
      if (!wait_ge<2>(ctl, C_WANT, k + 1)) return;
      unsigned t = 0;
      if (lane == 0) t = __hip_atomic_fetch_add((GAS unsigned*)(a.ws + W_CTR + g * 256), 1u, __ATOMIC_RELAXED, __HIP_MEMORY_SCOPE_AGENT);
      t = rfl(t);
      ctl[C_TICK + k] = t;
      if (t >= (unsigned)NT) break;
    }
    if (!wait_ge<1>(ctl, C_T1, 1)) return;
    hint_set(ws, lane < NREP ? W_F2 + (side * NREP + lane) * 1024 + prod * 8 : OOB, ctl[C_T1], ta);
    if (prod < 72) {
      const int ho = W_F2 + (side * NREP + prod % NREP) * 1024 + lane * 8;
      ctl[C_T2] = (hint_time<true>(ws, ho, ho + 512, ta, tstart) + tstart + DT2) | 1u;
    }
    return;
  }
  if (w == 2) {
    {
      const size_t pa = (size_t)g * (2u << 20) + (3u << 19), pb = (size_t)g * (2u << 20), pc = pb + (64u << 20);
      const unsigned char* pw = (side ? a.woB : a.woA) + (size_t)((side ? g : 2 * (g & 15) + (g >> 4)) * 128 + (j & 3) * 32) * 1152;
      unsigned d0, d1, d2, d3, d4, d5, d6, d7;
      asm volatile("s_load_dword %0, %8, 0\n\ts_load_dword %1, %9, 0\n\ts_load_dword %2, %10, 0\n\ts_load_dword %3, %11, 0\n\ts_load_dword %4, %12, 0\n\t"
                   "s_load_dword %5, %13, 0\n\ts_load_dword %6, %14, 0\n\ts_load_dword %7, %15, 0\n\ts_barrier\n\ts_waitcnt lgkmcnt(0)"
                   : "=&s"(d0), "=&s"(d1), "=&s"(d2), "=&s"(d3), "=&s"(d4), "=&s"(d5), "=&s"(d6), "=&s"(d7)
                   : "s"(a.vA + pa), "s"(a.kB + pb), "s"(a.kB + pc), "s"(a.vB + pb), "s"(a.vB + pc), "s"(pw), "s"(a.ws + W_SAFE1), "s"(a.ws + W_SAFE2) : "memory");
    }
    const int nr1 = side ? 56 : 8;
    const int ta_ = (g & 15) * 2 + (g >> 4);
    const int b0 = side ? W_P1 + g * P1_STR : W_P + ta_ * P_STR, b1 = W_P + (side ? 64 + g : 32 + ta_) * P_STR, d0 = side ? 8192 : 1024, dest = 2 * (j & 3);
    int voff = b0 + dest * d0 + lane * 16;
    int voffb = voff + d0;
    int voff7 = b1 + dest * 1024 + lane * 16;
    int voff8 = lane >= 56 ? b1 + 8192 + (lane - 56) * 16 : b0 + 8 * d0 + (lane < nr1 ? lane : 0) * 16;
    asm volatile("" : "+v"(voff), "+v"(voffb), "+v"(voff7), "+v"(voff8));
    if (!wait_ge<16>(ctl, C_LATE, 1)) return;
    if (!wait_ge<2>(ctl, C_PHASE, 2)) return;
    const unsigned ta = rfl(ctl[C_TAGA]), tb = rfl(ctl[C_TAGB]);
    const int hoff = side ? (lane < 8 ? W_F1 + (g * 8 + lane) * 128 : OOB) : (lane < 16 ? W_F1 + ((g & 15) * 8 + (lane >> 3) * 128 + (lane & 7)) * 128 : OOB);
    wait_until(tstart, hint_time<false>(ws, hoff, OOB, ta, tstart) + DT1);
    v4u X[2][8], ML;
#pragma unroll 1
    for (unsigned sp = 0;; ++sp) {
      const unsigned tr = now32();
      ML = gld(ws, voff8);
#pragma unroll
      for (int i = 0; i < 7; ++i) { X[0][i] = gld(ws, voff, side ? i * 1024 : 0); X[1][i] = gld(ws, voffb, side ? i * 1024 : 0); }
      X[0][7] = gld(ws, voff7); X[1][7] = gld(ws, voff7 + 1024);
      ctl[C_T1] = tr | 1u;
      __builtin_amdgcn_sched_barrier(0);
      const bool seg1 = lane >= 56;
      const float mr = fbits(ML.x);
      const float M = max8(mr);
      const float M0 = max8to64(seg1 ? -INFINITY : M);
      const float f = (lane >= nr1 && !seg1) ? 0.f : ex2(mr - (seg1 ? M : M0));
      float fb[8];
#pragma unroll
      for (int b = 0; b < 8; ++b) fb[b] = fbits(__builtin_amdgcn_ds_bpermute((8 * b + (lane >> 3)) * 4, ubits(f)));
      float Ls = f * fbits(ML.z);
      Ls += dpp<0xB1>(Ls); Ls += dpp<0x4E>(Ls); Ls += dpp<0x141>(Ls);
      const float L1 = fbits(__builtin_amdgcn_readlane(ubits(Ls), 56));
      float L0 = seg1 ? 0.f : Ls;
      L0 += dpp<0x128>(L0); L0 = xsum(L0);
      const float inv = __builtin_amdgcn_rcpf(lane >= 32 ? L1 : L0);
      __builtin_amdgcn_sched_barrier(0);
      unsigned bad = gbad(ML, ta, tb, 0u);
#pragma unroll
      for (int i = 0; i < 8; ++i) bad = gbad(X[1][i], ta, tb, gbad(X[0][i], ta, tb, bad));
      float oe[2], oo[2];
#pragma unroll
      for (int h = 0; h < 2; ++h) {
        float o0 = 0.f, o1 = 0.f;
#pragma unroll
        for (int b = 0; b < 7; ++b) { o0 = fmaf(fb[b], fbits(X[h][b].x), o0); o1 = fmaf(fb[b], fbits(X[h][b].z), o1); }
        oe[h] = rs32(o0, fb[7] * fbits(X[h][7].x)); oo[h] = rs32(o1, fb[7] * fbits(X[h][7].z));
      }
      float e = rs16(oe[0], oe[1]), o = rs16(oo[0], oo[1]);
      e += dpp<0x128>(e); o += dpp<0x128>(o);
      *(volatile LAS unsigned*)(L + L_OP + (lane >> 4) * 32 + (lane & 7) * 4) = pk_bf16(e * inv, o * inv);
      if (__all(bad == 0u)) break;
      if (sp > SPIN_CAP) return;
      __builtin_amdgcn_s_sleep(4);
    }
    ctl[C_OSEQ] = 1;
    return;
  }
  {
    const int qw = w - 3;
    const int r0 = qw * 64 + (lane >> 2) * 4;
    const unsigned char* xp = (cb < 128 ? a.xA : a.xB) + r0 * 2;
    const unsigned char* wp = (cb < 128 ? a.wqA : a.wqB) + (size_t)r0 * 8192 + (cb & 127) * 64 + (lane & 3) * 16;
    v4u Wq[4];
#pragma unroll
    for (int i = 0; i < 4; ++i) Wq[i] = __builtin_nontemporal_load((const GAS v4u*)(wp + i * 8192));
    const v2u X0 = *(const GAS v2u*)xp, X1 = *(const GAS v2u*)(xp + 1152);
    asm volatile("s_barrier" ::: "memory");
    float ac[2][8];
#pragma unroll
    for (int k = 0; k < 8; ++k) { ac[0][k] = 0.f; ac[1][k] = 0.f; }
#pragma unroll
    for (int s = 0; s < 2; ++s)
#pragma unroll
      for (int d = 0; d < 4; ++d) {
        const unsigned lo = __builtin_amdgcn_perm(Wq[2 * s + 1][d], Wq[2 * s][d], 0x05040100u), hi = __builtin_amdgcn_perm(Wq[2 * s + 1][d], Wq[2 * s][d], 0x07060302u);
        ac[0][2 * d] = dot2(lo, X0[s], ac[0][2 * d]); ac[0][2 * d + 1] = dot2(hi, X0[s], ac[0][2 * d + 1]);
        ac[1][2 * d] = dot2(lo, X1[s], ac[1][2 * d]); ac[1][2 * d + 1] = dot2(hi, X1[s], ac[1][2 * d + 1]);
      }
    float b8[8], c4[4];
#pragma unroll
    for (int k = 0; k < 8; ++k) b8[k] = rs32(ac[0][k], ac[1][k]);
#pragma unroll
    for (int k = 0; k < 4; ++k) { float t = rs16(b8[k], b8[k + 4]); t += dpp<0x128>(t); t += dpp<0x124>(t); c4[k] = t; }
    if ((lane & 12) == 0) *(LAS v4f*)(L + L_QP + qw * 256 + (lane >> 5) * 128 + (lane & 3) * 32 + ((lane >> 4) & 1) * 16) = v4f{c4[0], c4[1], c4[2], c4[3]};
    lds_inc(L, C_QCNT, lane);
  }
  const int r = lane >> 4, c = lane & 15;
  const unsigned kof = c * 256 + r * 16 + (c & 3) * 64;
  const unsigned vof = (4 * r + (c >> 2)) * 256 + (c >> 2) * 64 + (c & 3) * 8;
  if (w >= 4) {
    const unsigned aw = w - 4, off = aw * 8192, an = aw >> 1;
    v4u K0[4], K1[4], V[8];
    if (!wait_ge<1>(ctl, C_FULL, 2 * aw + 2)) return;
    read_k(L, off + kof, 0, K0); read_k(L, off + kof, 4096, K1);
    lds_inc(L, C_FREE + an, lane);
    if (!wait_ge<1>(ctl, C_FULL, 2 * aw + 18)) return;
    read_v(L, 65536 + off + vof, 0, V);
    lds_inc(L, C_FREE + 4 + an, lane);
    if (!wait_ge<1>(ctl, C_QRDY, 1)) return;
    const LAS v4u* qp = (const LAS v4u*)(L + L_Q) + r + (c >> 3) * 16;
    const v4u q[4] = {qp[0], qp[4], qp[8], qp[12]};
    v4f s0 = round2(qk(K0, q)), s1 = round2(qk(K1, q));
    const float M = xmax(fmaxf(max4(s0), max4(s1))) * L2E;
    s0 = expo(s0, M); s1 = expo(s1, M);
    const v4u P = {pk_bf16(s0[0], s0[1]), pk_bf16(s0[2], s0[3]), pk_bf16(s1[0], s1[1]), pk_bf16(s1[2], s1[3])};
    const float Ls = xsum(sum4(s0) + sum4(s1));
    v4f acc[8];
#pragma unroll
    for (int blk = 0; blk < 8; ++blk) acc[blk] = mfma(V[blk], P, v4f{0.f, 0.f, 0.f, 0.f});
#pragma unroll
    for (int k = 0; k < 4; ++k) acc[k] = sel4(bitmask(c, 2), acc[k + 4], acc[k]);
#pragma unroll
    for (int k = 0; k < 2; ++k) acc[k] = sel4(bitmask(c, 1), acc[k + 2], acc[k]);
    acc[0] = sel4(bitmask(c, 0), acc[1], acc[0]);
    LAS unsigned char* rec = L + L_REC + ((c >> 3) * 8 + aw) * REC_B;
    *(LAS v4f*)(rec + 16 + ((c & 7) * 16 + r * 4) * 4) = acc[0];
    if ((lane & 0x37) == 0) *(LAS v2f*)rec = v2f{M, Ls};
    if (last_arriver(L, C_CNT, 8, lane)) {
#pragma unroll 1
      for (int e = 0; e < 2; ++e) {
        float Mo, Lo;
        const v2f o = merge_rec<8>(L + L_REC + e * 8 * REC_B, lane, Mo, Lo);
        const int base = W_P + (g * 2 + e) * P_STR;
        if (park_rec(L, ctl, e, base + j * 128, 1024, base + 8192 + j * 16, lane, lane, o, Mo, Lo)) {
          const unsigned ta_ = ctl[C_TAGA], tb_ = ctl[C_TAGB];
          gst(ws, base + j * 128 + (lane >> 3) * 1024 + (lane & 7) * 16, ubits(o.x), ubits(o.y), ta_, tb_);
          if (lane == 0) gst(ws, base + 8192 + j * 16, ubits(Mo), ubits(Lo), ta_, tb_);
        }
      }
    }
  }
  if (w < 8) {
    const int k = w - 3;
    const unsigned char* pw = (side ? a.woB : a.woA) + (size_t)((side ? g : 2 * (g & 15) + (g >> 4)) * 128 + (j & 3) * 32) * 1152;
    const bool live = k < 4 || c < 8;
    if (!wait_ge<16>(ctl, C_LATE, 1)) return;
    const unsigned ta = rfl(ctl[C_TAGA]), tb = rfl(ctl[C_TAGB]);
    if (k == 0) {
      const unsigned t0 = __builtin_nontemporal_load((const GAS unsigned*)(pw + (lane & 1) * (31 * 1152))), t1 = __builtin_amdgcn_raw_buffer_load_b32(ws, lane & 1 ? W_SAFE1 : W_SAFE2, 0, SC1);
      asm volatile("" ::"v"(t0), "v"(t1));
    }
    if (!wait_ge<2>(ctl, C_PHASE, 1)) return;
    const unsigned held = rfl(ctl[C_HELD]);
    asm volatile("" ::: "memory");
#pragma unroll 1
    for (int slot = k; slot < NHOLD; slot += 5) {
      if (!(held >> slot & 1)) continue;
      LAS unsigned char* h = L + L_HOLD + slot * HOLD_B;
      const v4u hd = *(LAS v4u*)h;
      const v2u ml = *(LAS v2u*)(h + 16), o = *(LAS v2u*)(h + 32 + lane * 8);
      gst(ws, hd.x + (lane >> 3) * hd.y + (lane & 7) * 16, o.x, o.y, ta, tb);
      gst(ws, lane == 0 ? hd.z : OOB, ml.x, ml.y, ta, tb);
    }
    if (!wait_ge<2>(ctl, C_PHASE, 2)) return;
    v4u Wf[8], Wr[8];
    const __amdgpu_buffer_rsrc_t rw = mkrs(pw, 32 * 1152);
#pragma unroll
    for (int i = 0; i < 8; ++i) Wr[i] = __builtin_amdgcn_raw_buffer_load_b128(rw, (live ? 8 * r * 1152 + k * 256 + c * 16 : OOB) + i * 1152, 0, 2);
#pragma unroll
    for (int e = 0; e < 4; ++e)
#pragma unroll
      for (int d = 0; d < 4; ++d) {
        Wf[2 * e][d] = __builtin_amdgcn_perm(Wr[2 * d + 1][e], Wr[2 * d][e], 0x05040100u);
        Wf[2 * e + 1][d] = __builtin_amdgcn_perm(Wr[2 * d + 1][e], Wr[2 * d][e], 0x07060302u);
      }
    int y0 = live ? W_Y + ((side ? 3 - (r & 1) : (r & 1)) * 36 + k * 8 + (c >> 2)) * YS + prod * 128 + (2 * (c & 3) + (r >> 1)) * 16 : OOB, y1 = y0 + (k < 4 ? 4 : 2) * YS;
    const unsigned mh = bitmask(r, 1);
    asm volatile("" : "+v"(y0), "+v"(y1), "+v"(Wf[0]), "+v"(Wf[1]), "+v"(Wf[2]), "+v"(Wf[3]), "+v"(Wf[4]), "+v"(Wf[5]), "+v"(Wf[6]), "+v"(Wf[7]));
    v4u oa;
#pragma unroll 1
    for (unsigned sp = 0;; ++sp) {
      const unsigned fl = ctl[C_OSEQ];
      oa = *(volatile LAS v4u*)(L + L_OP + ((c >> 2) & 1) * 64 + r * 16);
      if (rfl(fl)) break;
      if (sp > SPIN_CAP) return;
      __builtin_amdgcn_s_sleep(1);
    }
#pragma unroll
    for (int i = 0; i < 2; ++i) {
      v4f d[4];
#pragma unroll
      for (int f = 0; f < 4; ++f) d[f] = mfma(oa, Wf[(f >> 1) * 4 + 2 * i + (f & 1)], v4f{0.f, 0.f, 0.f, 0.f});
      gst(ws, i ? y1 : y0, ubits(sel(mh, d[2][0], d[0][0])), ubits(sel(mh, d[3][0], d[1][0])), ta, tb);
    }
    return;
  }
  const unsigned ta = rfl(ctl[C_TAGA]), tb = rfl(ctl[C_TAGB]);
  const int cw = w - 8;
  {
    float m = -INFINITY, l = 0.f;
    v4f acc[8];
    bool fresh = true, freshv = true;
    const unsigned mk0 = bitmask(c, 0), mk1 = bitmask(c, 1), mk2 = bitmask(c, 2), mk3 = bitmask(c, 3);
    const int gid = (c >> 1) * 8 + 2 * r + (c & 1);
    int mode = 1;
    unsigned nk = 0x7fffffffu;
#pragma unroll 1
    for (unsigned n = 4 + cw;; n = n + 4 < NS ? n + 4 : n < NS ? NS : n + 1) {
      bool stop = false;
      unsigned dsc = cw << 8;
#pragma unroll 1
      for (unsigned sp = 0; n >= NS; ++sp) {
        const unsigned dd = ctl[C_DESC + n - NS], en = ctl[mode == 1 ? C_NK : C_END];
        dsc = rfl(dd);
        if (n >= rfl(en) || sp > SPIN_CAP) {
          if (mode != 1) { stop = true; break; }
          nk = n; n = 4 + cw; mode = 2;
        } else if (dsc == 0xffffffffu) __builtin_amdgcn_s_sleep(2);
        else if ((dsc >> 8 & 3) == (unsigned)cw) break;
        else ++n;
      }
      if (stop) break;
      if (n >= nk) mode = 3;
      const bool dyn = n >= NS;
      const bool pubn = dyn ? (dsc >> 10 & 1) : (n >= NS - 4 || (n & ~3u) == 16);
      const int base = n >= 20 ? W_P1 + g * P1_STR : W_P + (64 + g) * P_STR, dstr = n >= 20 ? 8192 : 1024, rec = dyn ? dsc & 255 : j, slot = dyn ? dsc >> 12 & 255 : 2 + (n >= 20);
      const int gi = dyn ? gid : lane;
      const unsigned sk = n < nk ? n + 4 : 2 * n - 1, sv = n + 1 < nk ? n + nk : 2 * n + 2;
      int ad = base + rec * 128 + (gi >> 3) * dstr + (gi & 7) * 16, am = lane == 0 ? base + 8 * dstr + rec * 16 : OOB;
      unsigned ka = (sk & 7) * 16384 + kof, va = (sv & 7) * 16384 + vof;
      LAS unsigned char* pb = L + L_PB + (mode == 3 ? 0 : n - 4) * PB_B;
      v4u P0, P1, st;
      float al, Ls, mrec;
      const bool first = freshv;
      if ((mode & 2) && freshv) {
#pragma unroll
        for (int blk = 0; blk < 8; ++blk) acc[blk] = v4f{0.f, 0.f, 0.f, 0.f};
        freshv = false;
      }
      if (mode & 1) {
        const LAS v4u* qp = (const LAS v4u*)(L + L_Q) + r + (n < 20 ? 32 : 48);
        v4u q[4] = {qp[0], qp[4], qp[8], qp[12]};
        if (fresh) { m = -INFINITY; l = 0.f; }
        fresh = pubn;
        asm volatile("" : "+v"(ad), "+v"(am), "+v"(ka), "+v"(va), "+v"(q[0]), "+v"(q[1]), "+v"(q[2]), "+v"(q[3]));
        if (!wait_ge<1>(ctl, C_FULL, 4 * sk + 4)) return;
        v4f s[4];
        v4u K[4][4];
#pragma unroll
        for (int uu = 0; uu < 4; ++uu) read_k(L, ka, uu * 4096, K[uu]);
        __builtin_amdgcn_sched_barrier(0);
#pragma unroll
        for (int uu = 0; uu < 4; ++uu) s[uu] = qk(K[uu], q);
        __builtin_amdgcn_sched_barrier(0);
        lgkm0();
        ctl[C_FREE + (sk & 7)] = sk + 1;
        v4f sc = round2(sel4(mk3, sel4(mk2, s[3], s[2]), sel4(mk2, s[1], s[0])));
        const float mn = fmaxf(max4to64(max4(sc)) * L2E, m);
        st = *(volatile LAS v4u*)(L + L_CTL);
        sc = expo(sc, mn);
        const unsigned px = pk_bf16(sc[0], sc[1]), py = pk_bf16(sc[2], sc[3]);
        P0 = v4u{bcast16<0>(px), bcast16<0>(py), bcast16<4>(px), bcast16<4>(py)}; P1 = v4u{bcast16<8>(px), bcast16<8>(py), bcast16<12>(px), bcast16<12>(py)};
        al = ex2(m - mn);
        mrec = m = mn;
        l = fmaf(l, al, sum4(sc));
        Ls = l + dpp<0x124>(l);
        Ls += dpp<0x128>(Ls);
        Ls = xsum(Ls);
        if (mode == 1) {
          *(LAS v4u*)(pb + r * 16) = P0; *(LAS v4u*)(pb + 64 + r * 16) = P1; *(LAS v4f*)(pb + 128) = v4f{al, Ls, mn, 0.f};
        }
      } else {
        P0 = *(LAS v4u*)(pb + r * 16); P1 = *(LAS v4u*)(pb + 64 + r * 16);
        const v4f t = *(LAS v4f*)(pb + 128);
        al = t.x; Ls = t.y; mrec = t.z;
        st = *(volatile LAS v4u*)(L + L_CTL);
      }
      if (mode == 1) continue;
      if (!first) {
#pragma unroll
        for (int blk = 0; blk < 8; ++blk) acc[blk] *= al;
      }
      asm volatile("" : "+v"(P0), "+v"(P1), "+v"(Ls), "+v"(acc[0]), "+v"(acc[1]), "+v"(acc[2]), "+v"(acc[3]), "+v"(acc[4]), "+v"(acc[5]), "+v"(acc[6]), "+v"(acc[7]));
      v4u V[8];
#define WAITV(need) _Pragma("unroll 1") for (unsigned sp = 0; rfl(st.x) < (need); ++sp) { if (sp > SPIN_CAP) return; if (sp) __builtin_amdgcn_s_sleep(1); st = *(volatile LAS v4u*)(L + L_CTL); } \
      asm volatile("" ::: "memory");
      WAITV(4 * sv + 2)
      read_v(L, va, 0, V);
#pragma unroll
      for (int blk = 0; blk < 8; ++blk) acc[blk] = mfma(V[blk], P0, acc[blk]);
      WAITV(4 * sv + 4)
      const unsigned tl = now32(), nend = rfl(st.y);
      read_v(L, va, 8192, V);
      hint_set(ws, lane == 0 && n + 1 == nend ? W_F1 + wg * 128 : OOB, tl, ta);
#pragma unroll
      for (int blk = 0; blk < 8; ++blk) acc[blk] = mfma(V[blk], P1, acc[blk]);
      if (pubn) {
        v2f o[8];
#pragma unroll
        for (int k = 0; k < 8; ++k) o[k] = sel2(mk0, v2f{acc[k][2], acc[k][3]}, v2f{acc[k][0], acc[k][1]});
#pragma unroll
        for (int k = 0; k < 4; ++k) o[k] = sel2(mk3, o[k + 4], o[k]);
#pragma unroll
        for (int k = 0; k < 2; ++k) o[k] = sel2(mk2, o[k + 2], o[k]);
        o[0] = sel2(mk1, o[1], o[0]);
        bool now = dyn && (slot >= NHOLD || rfl(st.z) != 0);
        if (!now) {
          bool emit = true;
          if (!dyn) {
            LAS unsigned char* rp = L + L_REC + (8 + slot * 4) * REC_B;
            *(LAS v2f*)(rp + cw * REC_B + 16 + gid * 8) = o[0];
            if (lane == 0) *(LAS v2f*)(rp + cw * REC_B) = v2f{mrec, Ls};
            emit = last_arriver(L, C_CNT + slot - 1, 4, lane);
            if (emit) o[0] = merge_rec<4>(rp, lane, mrec, Ls);
          }
          if (emit) now = park_rec(L, ctl, slot, base + rec * 128, dstr, base + 8 * dstr + rec * 16, gi, lane, o[0], mrec, Ls);
          if (!now) { ad = OOB; am = OOB; }
        }
        gst(ws, ad, ubits(o[0].x), ubits(o[0].y), ta, tb);
        gst(ws, am, ubits(mrec), ubits(Ls), ta, tb);
        freshv = true;
      }
      lgkm0();
      ctl[C_FREE + (sv & 7)] = sv + 1;
    }
  }
  if (prod >= 72) return;
  const int cmb = side * 2 + (prod & 1), rho = prod >> 1;
  int voff = W_Y + (cmb * 36 + rho) * YS + cw * 4096 + lane * 16;
  const int yi = rho < 32 ? (rho >> 2) & 1 : (rho >> 1) & 1, yq = rho < 32 ? rho & 3 : rho & 1;
  int oo = lane < 16 ? ((cmb & 1) * 1152 + (cmb >> 1) * 576 + (rho >> 3) * 128 + (yq * 4 + (lane >> 2)) * 8 + ((lane >> 1) & 1) * 4 + yi * 2 + (lane & 1)) * 2 : OOB;
  asm volatile("" : "+v"(voff), "+v"(oo));
  if (!wait_ge<4>(ctl, C_T1, 1)) return;
  if (!wait_ge<1>(ctl, C_T2, 1)) return;
  wait_until(tstart, rfl(ctl[C_T2]) - tstart);
  v4u X[4];
#pragma unroll 1
  for (unsigned sp = 0;; ++sp) {
#pragma unroll
    for (int i = 0; i < 4; ++i) X[i] = gld(ws, voff, i * 1024);
    unsigned bad = 0;
#pragma unroll
    for (int i = 0; i < 4; ++i) bad = gbad(X[i], ta, tb, bad);
    if (__all(bad == 0u)) break;
    if (sp > SPIN_CAP) return;
    __builtin_amdgcn_s_sleep(4);
  }
  float s = rs32((fbits(X[0].x) + fbits(X[1].x)) + (fbits(X[2].x) + fbits(X[3].x)), (fbits(X[0].z) + fbits(X[1].z)) + (fbits(X[2].z) + fbits(X[3].z)));
  s = rs16(s, s);
  s += dpp<0x128>(s);
  *(volatile LAS unsigned*)(L + L_FP + (((lane & 7) * 2 + (lane >> 5)) * 4 + cw) * 4) = min(ubits(s), 0xfffffffeu);
  if (cw != 0) return;
#pragma unroll 1
  for (unsigned sp = 0;; ++sp) {
    const v4u p = *(volatile LAS v4u*)(L + L_FP + (lane & 15) * 16);
    s = (fbits(p.x) + fbits(p.y)) + (fbits(p.z) + fbits(p.w));
    if (__all(p.y != ~0u && p.z != ~0u && p.w != ~0u)) break;
    if (sp > SPIN_CAP) return;
  }
  __builtin_amdgcn_raw_buffer_store_b16((unsigned short)pk_bf16(s, s), mkrs(a.out, 4608), oo, 0, 0);
  __builtin_amdgcn_raw_buffer_store_b64(v2u{ta, tb}, ws, cmb == 3 && rho == 0 && lane == 0 ? W_X : OOB, 0, SC1);
}

extern "C" void kernel_launch(void* const* d_in, const int* in_sizes, int n_in, void* d_out, int out_size, void* d_ws, size_t ws_size,
                              hipStream_t stream) {
  Args a;
  a.xA = (const unsigned char*)d_in[0]; a.kA = (const unsigned char*)d_in[1]; a.vA = (const unsigned char*)d_in[2];
  a.wqA = (const unsigned char*)d_in[3]; a.woA = (const unsigned char*)d_in[4];
  a.xB = (const unsigned char*)d_in[5]; a.kB = (const unsigned char*)d_in[6]; a.vB = (const unsigned char*)d_in[7];
  a.wqB = (const unsigned char*)d_in[8]; a.woB = (const unsigned char*)d_in[9];
  a.out = (unsigned char*)d_out; a.ws = (unsigned char*)d_ws;
  fused_decode<<<256, NTHR, 0, stream>>>(a);
}
```

```cpp
#include <hip/hip_runtime.h>
#include <stdint.h>

typedef unsigned v4u __attribute__((ext_vector_type(4)));
typedef unsigned v2u __attribute__((ext_vector_type(2)));
typedef float v4f __attribute__((ext_vector_type(4)));
typedef float v2f __attribute__((ext_vector_type(2)));
typedef short v4s __attribute__((ext_vector_type(4)));
typedef __bf16 bf16x2 __attribute__((ext_vector_type(2)));
typedef __bf16 bf16x8 __attribute__((ext_vector_type(8)));
#define LAS __attribute__((address_space(3)))
#define GAS __attribute__((address_space(1)))
#define DI __device__ __forceinline__

constexpr int WLD = 22;
constexpr int DT1 = 50;
constexpr int DT2 = 100;
constexpr int KAH = 3;
constexpr int NTHR = 12 * 64;
constexpr int NSTAT = 9;
constexpr int NS = 4 + 16 + NSTAT;
constexpr int POOLC = (16 - NSTAT) * 8;
constexpr int NT = 48, TP = POOLC - NT;
constexpr int WINE = (WLD + 7) / 8 * 2;
constexpr int NREP = 16;
constexpr int NKMAX = 52;
constexpr unsigned SPIN_CAP = 1u << 20;
constexpr int NHOLD = 16, HOLD_B = 544, REC_B = 528, PB_B = 144;
constexpr int L_CTL = 131072  , L_FP = L_CTL + 768  , L_Q = L_CTL + 1024, L_OP = L_Q + 1024, L_QP = L_OP + 128,
              L_REC = L_QP + 9 * 256, L_HOLD = L_REC + 24 * REC_B, L_PB = L_HOLD + NHOLD * HOLD_B, L_END = L_PB + (NKMAX - 4) * PB_B;
static_assert(L_END <= 163840 && TP >= 0 && TP <= NT && NT + 2 <= 64 && POOLC <= 64 && WINE >= 2 && WINE <= 8, "");
enum { C_FULL = 0  , C_END = 1  , C_PHASE = 2  , C_NK = 3, C_WANT = 4  ,
       C_T1 = 5  , C_TAGA = 6, C_TAGB = 7, C_FREE = 8  , C_CNT = 16  , C_QCNT = 19, C_QRDY = 20, C_HELD = 21, C_LATE = 23, C_OSEQ = 22,
       C_T2 = 24  , C_TICK = 64, C_DESC = 128 };
constexpr int W_X = 0, W_CTR = 4096, W_F1 = 16384  , W_F2 = W_F1 + 32768  , W_QA = W_F2 + 2 * NREP * 1024, W_QB = W_QA + 32768,
              W_P = W_QB + 32768, P_STR = 9216  , W_P1 = W_P + 96 * P_STR, P1_STR = 66560  ,
              W_Y = W_P1 + 32 * P1_STR, YS = 16384, W_ENDB = 256 + W_Y + 4 * 36 * YS  ;
constexpr int W_SAFE1 = W_P1 + 24 * P1_STR + 7424, W_SAFE2 = W_ENDB - 128;
constexpr float SCALE = 0.088388347648318440f, L2E = 1.4426950408889634f;
constexpr int SC1 = 16, OOB = 0x70000000;

struct Args {
  const unsigned char *xA, *kA, *vA, *wqA, *woA, *xB, *kB, *vB, *wqB, *woB;
  unsigned char *out, *ws;
};

DI float dot2(unsigned a, unsigned b, float c) { return __builtin_amdgcn_fdot2_f32_bf16(__builtin_bit_cast(bf16x2, a), __builtin_bit_cast(bf16x2, b), c, false); }
template <int CTRL> DI float dpp(float x) { return __builtin_bit_cast(float, __builtin_amdgcn_mov_dpp(__builtin_bit_cast(int, x), CTRL, 0xf, 0xf, true)); }
template <int LANE> DI unsigned bcast16(unsigned x) { return __builtin_amdgcn_mov_dpp(x, 0x150 + LANE, 0xf, 0xf, true); }
DI unsigned pk_bf16(float lo, float hi) {
  unsigned r = __builtin_bit_cast(unsigned, bf16x2{(__bf16)lo, (__bf16)hi});
  asm("" : "+v"(r));
  return r;
}
DI float bf_lo(unsigned u) { return __builtin_bit_cast(float, u << 16); }
DI float bf_hi(unsigned u) { return __builtin_bit_cast(float, u & 0xffff0000u); }
DI float fbits(unsigned u) { return __builtin_bit_cast(float, u); }
DI unsigned ubits(float f) { return __builtin_bit_cast(unsigned, f); }
DI unsigned rfl(unsigned x) { return __builtin_amdgcn_readfirstlane(x); }
DI float ex2(float x) { return __builtin_amdgcn_exp2f(x); }
DI v4f mfma(v4u a, v4u b, v4f c) { return __builtin_amdgcn_mfma_f32_16x16x32_bf16(__builtin_bit_cast(bf16x8, a), __builtin_bit_cast(bf16x8, b), c, 0, 0, 0); }
DI v2u trd(LAS unsigned char* p) { return __builtin_bit_cast(v2u, __builtin_amdgcn_ds_read_tr16_b64_v4i16((LAS v4s*)p)); }
DI float rs32(float x, float y) { auto s = __builtin_amdgcn_permlane32_swap(ubits(x), ubits(y), false, false); return fbits(s[0]) + fbits(s[1]); }
DI float rs16(float x, float y) { auto s = __builtin_amdgcn_permlane16_swap(ubits(x), ubits(y), false, false); return fbits(s[0]) + fbits(s[1]); }
DI float xsum(float x) { x = rs16(x, x); return rs32(x, x); }
DI float xmax(float x) {
  auto s = __builtin_amdgcn_permlane16_swap(ubits(x), ubits(x), false, false); x = fmaxf(fbits(s[0]), fbits(s[1]));
  auto t = __builtin_amdgcn_permlane32_swap(ubits(x), ubits(x), false, false); return fmaxf(fbits(t[0]), fbits(t[1]));
}
DI float max8(float x) {
  asm("s_nop 1\n\tv_max_f32_dpp %0, %0, %0 quad_perm:[1,0,3,2] row_mask:0xf bank_mask:0xf\n\ts_nop 1\n\tv_max_f32_dpp %0, %0, %0 quad_perm:[2,3,0,1] row_mask:0xf bank_mask:0xf\n\t"
      "s_nop 1\n\tv_max_f32_dpp %0, %0, %0 row_half_mirror row_mask:0xf bank_mask:0xf" : "+v"(x));
  return x;
}
DI float max8to64(float x) {
  float t;
  asm("s_nop 1\n\tv_max_f32_dpp %0, %0, %0 row_ror:8 row_mask:0xf bank_mask:0xf\n\tv_mov_b32 %1, %0\n\ts_nop 1\n\tv_permlane16_swap_b32 %0, %1\n\tv_max_f32 %0, %0, %1\n\t"
      "v_mov_b32 %1, %0\n\ts_nop 1\n\tv_permlane32_swap_b32 %0, %1\n\tv_max_f32 %0, %0, %1" : "+v"(x), "=&v"(t));
  return x;
}
DI float max4to64(float x) {
  asm("s_nop 1\n\tv_max_f32_dpp %0, %0, %0 row_ror:4 row_mask:0xf bank_mask:0xf" : "+v"(x));
  return max8to64(x);
}
DI void lgkm0() { asm volatile("s_waitcnt lgkmcnt(0)" ::: "memory"); }
template <int N> DI void waitvm() { asm volatile("s_waitcnt vmcnt(%0)" ::"n"(N) : "memory"); }
DI __amdgpu_buffer_rsrc_t mkrs(const void* p, unsigned bytes) { return __builtin_amdgcn_make_buffer_rsrc((void*)p, 0, bytes, 0x00020000); }
DI void dma8(__amdgpu_buffer_rsrc_t rs, unsigned voff, unsigned soff, unsigned lds) {
  unsigned keep, s2;
  asm volatile("s_mov_b32 %0, m0\n\ts_mov_b32 m0, %5\n\ts_add_u32 %1, %4, 0x1000\n\ts_nop 2\n\t"
               "buffer_load_dwordx4 %2, %3, %4 offen nt lds\n\t"
               "buffer_load_dwordx4 %2, %3, %4 offen offset:1024 nt lds\n\t"
               "buffer_load_dwordx4 %2, %3, %4 offen offset:2048 nt lds\n\t"
               "buffer_load_dwordx4 %2, %3, %4 offen offset:3072 nt lds\n\t"
               "s_add_u32 m0, m0, 0x1000\n\ts_nop 0\n\t"
               "buffer_load_dwordx4 %2, %3, %1 offen nt lds\n\t"
               "buffer_load_dwordx4 %2, %3, %1 offen offset:1024 nt lds\n\t"
               "buffer_load_dwordx4 %2, %3, %1 offen offset:2048 nt lds\n\t"
               "buffer_load_dwordx4 %2, %3, %1 offen offset:3072 nt lds\n\t"
               "s_mov_b32 m0, %0"
               : "=&s"(keep), "=&s"(s2) : "v"(voff), "s"(rs), "s"(soff), "s"(lds) : "memory", "scc");
}
DI v4u gld(__amdgpu_buffer_rsrc_t rs, int off, int soff = 0) { return __builtin_amdgcn_raw_buffer_load_b128(rs, off, soff, SC1); }
DI void gst(__amdgpu_buffer_rsrc_t rs, int off, unsigned a, unsigned b, unsigned ta, unsigned tb) { __builtin_amdgcn_raw_buffer_store_b128(v4u{a, ta, b, tb}, rs, off, 0, SC1); }
DI unsigned gbad(v4u g, unsigned ta, unsigned tb, unsigned acc) { return __builtin_amdgcn_bitop3_b32(g.w, tb, __builtin_amdgcn_bitop3_b32(g.y, ta, acc, 0xBE), 0xBE); }

typedef volatile LAS unsigned* ctl_t;
template <int SLP> DI bool wait_ge(ctl_t ctl, int idx, unsigned v) {
#pragma unroll 1
  for (unsigned s = 0; rfl(ctl[idx]) < v; ++s) {
    if (s > SPIN_CAP) return false;
    __builtin_amdgcn_s_sleep(SLP);
  }
  asm volatile("" ::: "memory");
  return true;
}
DI void lds_inc(LAS unsigned char* L, int idx, int lane) {
  lgkm0();
  if (lane == 0) __hip_atomic_fetch_add((LAS unsigned*)(L + L_CTL) + idx, 1u, __ATOMIC_RELAXED, __HIP_MEMORY_SCOPE_WORKGROUP);
}
DI bool last_arriver(LAS unsigned char* L, int idx, unsigned n, int lane) {
  lgkm0();
  unsigned old = 0;
  if (lane == 0) old = __hip_atomic_fetch_add((LAS unsigned*)(L + L_CTL) + idx, 1u, __ATOMIC_RELAXED, __HIP_MEMORY_SCOPE_WORKGROUP);
  old = rfl(old);
  asm volatile("" ::: "memory");
  return old == n - 1;
}
DI void read_k(LAS unsigned char* L, unsigned off, int imm, v4u (&K)[4]) {
#pragma unroll
  for (int kk = 0; kk < 4; ++kk) K[kk] = *(const LAS v4u*)(L + (off ^ (kk * 64)) + imm);
}
DI void read_v(LAS unsigned char* L, unsigned off, int imm, v4u (&V)[8]) {
#pragma unroll
  for (int blk = 0; blk < 8; ++blk) {
    LAS unsigned char* p = L + (off ^ ((blk >> 1) * 64)) + (blk & 1) * 32 + imm;
    const v2u x = trd(p), y = trd(p + 4096);
    V[blk] = v4u{x.x, x.y, y.x, y.y};
  }
  __builtin_amdgcn_sched_barrier(0);
}
DI v4f qk(const v4u (&K)[4], const v4u (&q)[4]) {
  v4f d = {0.f, 0.f, 0.f, 0.f};
#pragma unroll
  for (int kk = 0; kk < 4; ++kk) d = mfma(K[kk], q[kk], d);
  return d;
}
DI v4f round2(v4f d) {
  const unsigned x01 = pk_bf16(d[0], d[1]), x23 = pk_bf16(d[2], d[3]);
  const unsigned b01 = pk_bf16(bf_lo(x01) * SCALE, bf_hi(x01) * SCALE), b23 = pk_bf16(bf_lo(x23) * SCALE, bf_hi(x23) * SCALE);
  return v4f{bf_lo(b01), bf_hi(b01), bf_lo(b23), bf_hi(b23)};
}
DI float sel(unsigned mask, float a, float b) { return fbits(__builtin_amdgcn_bitop3_b32(mask, ubits(a), ubits(b), 0xCA)); }
DI v4f sel4(unsigned mask, v4f a, v4f b) { return v4f{sel(mask, a[0], b[0]), sel(mask, a[1], b[1]), sel(mask, a[2], b[2]), sel(mask, a[3], b[3])}; }
DI v2f sel2(unsigned mask, v2f a, v2f b) { return v2f{sel(mask, a[0], b[0]), sel(mask, a[1], b[1])}; }
DI unsigned bitmask(int x, int bit) { unsigned m = (unsigned)((x << (31 - bit)) >> 31); asm("" : "+v"(m)); return m; }
DI float max4(v4f d) { return fmaxf(fmaxf(d[0], d[1]), fmaxf(d[2], d[3])); }
DI float sum4(v4f d) { return (d[0] + d[1]) + (d[2] + d[3]); }
DI v4f expo(v4f s, float m2) { return v4f{ex2(fmaf(s[0], L2E, -m2)), ex2(fmaf(s[1], L2E, -m2)), ex2(fmaf(s[2], L2E, -m2)), ex2(fmaf(s[3], L2E, -m2))}; }

DI unsigned now32() { return (unsigned)__builtin_amdgcn_s_memrealtime(); }
DI void wait_until(unsigned t0, unsigned until) {
#pragma unroll 1
  for (unsigned sp = 0; sp < 64 && now32() - t0 < until; ++sp) { }
}
DI void hint_set(__amdgpu_buffer_rsrc_t ws, int off, unsigned t, unsigned tag) {
  asm volatile("buffer_atomic_swap_x2 %0, %1, %2, 0 offen" ::"v"(v2u{t, tag}), "v"(off), "s"(ws) : "memory");
}
template <bool TWO> DI unsigned hint_time(__amdgpu_buffer_rsrc_t ws, int off, int off2, unsigned tag, unsigned t0) {
  unsigned rel = 0;
#define HP1(A0, A1, o) asm volatile("v_mov_b32 v16" #A0 ", 0\n\tv_mov_b32 v16" #A1 ", 0\n\tbuffer_atomic_add_x2 v[16" #A0 ":16" #A1 "], %0, %1, 0 offen sc0" \
                                    ::"v"(o), "s"(ws) : "memory", "v16" #A0, "v16" #A1)
#define HC1(A0, A1, o, N) { unsigned t_, g_; asm volatile("s_waitcnt vmcnt(%2)\n\tv_mov_b32 %0, v16" #A0 "\n\tv_mov_b32 %1, v16" #A1 : "=v"(t_), "=v"(g_) : "n"(N) : "memory", "v16" #A0, "v16" #A1); \
                            const bool ok_ = g_ == tag; rel = ok_ ? max(rel, t_ - t0) : rel; o = ok_ ? OOB : o; }
  HP1(0, 1, off);
  if (TWO) HP1(2, 3, off2);
  __builtin_amdgcn_s_sleep(8);
#pragma unroll 1
  for (unsigned sp = 0; sp < 4096; ++sp) {
    HP1(4, 5, off);
    if (TWO) HP1(6, 7, off2);
    HC1(0, 1, off, TWO ? 3 : 1)
    if (TWO) HC1(2, 3, off2, 2)
    if (__all(off == OOB && off2 == OOB)) break;
    HP1(0, 1, off);
    if (TWO) HP1(2, 3, off2);
    HC1(4, 5, off, TWO ? 3 : 1)
    if (TWO) HC1(6, 7, off2, 2)
    if (__all(off == OOB && off2 == OOB)) break;
  }
  return rfl((unsigned)max8to64(max8((float)rel)));
}
#undef HP1
#undef HC1
DI bool park_rec(LAS unsigned char* L, ctl_t ctl, int slot, int adr, int dstr, int mladr, int gi, int lane, v2f o, float m, float l) {
  LAS unsigned char* h = L + L_HOLD + slot * HOLD_B;
  *(LAS v2f*)(h + 32 + gi * 8) = o;
  if (lane == 0) { *(LAS v4u*)h = v4u{(unsigned)adr, (unsigned)dstr, (unsigned)mladr, 0u}; *(LAS v2f*)(h + 16) = v2f{m, l}; }
  lgkm0();
  if (lane == 0) __hip_atomic_fetch_or((LAS unsigned*)(L + L_CTL) + C_HELD, 1u << slot, __ATOMIC_RELAXED, __HIP_MEMORY_SCOPE_WORKGROUP);
  lgkm0();
  return rfl(ctl[C_PHASE]) != 0;
}
template <int NR> DI v2f merge_rec(LAS unsigned char* rec, int lane, float& Mo, float& Lo) {
  v2f av[NR];
  const v2f ml = *(LAS v2f*)(rec + (lane & (NR - 1)) * REC_B);
#pragma unroll
  for (int w = 0; w < NR; ++w) av[w] = *(LAS v2f*)(rec + w * REC_B + 16 + lane * 8);
  const float M = max8(ml.x), f = ex2(ml.x - M);
  float Ls = f * ml.y; Ls += dpp<0xB1>(Ls); Ls += dpp<0x4E>(Ls);
  if (NR == 8) Ls += dpp<0x141>(Ls);
  float a0 = 0.f, a1 = 0.f;
#pragma unroll
  for (int w = 0; w < NR; ++w) { const float fw = fbits(__builtin_amdgcn_readlane(ubits(f), w)); a0 = fmaf(fw, av[w].x, a0); a1 = fmaf(fw, av[w].y, a1); }
  Mo = M; Lo = Ls;
  return v2f{a0, a1};
}

__global__ void __launch_bounds__(NTHR, 3) fused_decode(Args a) {
  __shared__ __attribute__((aligned(16))) unsigned char smem[L_END];
  LAS unsigned char* L = (LAS unsigned char*)smem;
  ctl_t ctl = (ctl_t)(L + L_CTL);
  asm volatile("" ::"s"(a.xA), "s"(a.kA), "s"(a.vA), "s"(a.wqA), "s"(a.woA), "s"(a.xB), "s"(a.kB), "s"(a.vB), "s"(a.wqB), "s"(a.woB), "s"(a.out), "s"(a.ws));
  const int wg = blockIdx.x, g = wg >> 3, j = wg & 7;
  const int lane = threadIdx.x & 63, w = rfl(threadIdx.x >> 6);

  if (w == 0) {
    __builtin_amdgcn_s_waitcnt(0);
    const unsigned lb = (unsigned)(uintptr_t)L;
    const unsigned voff = (lane * 16) ^ ((lane >> 4) << 6);
    const unsigned offA = g * (2u << 20) + (3u << 19) + j * 16384;
    const __amdgpu_buffer_rsrc_t rk = mkrs(a.kA, 64u << 20);
    dma8(rk, voff, offA, lb); dma8(rk, voff, offA + 8192, lb + 8192);
    asm volatile("s_barrier" ::: "memory");
    __builtin_amdgcn_s_setprio(3);
    int u = 4, pub = 0;
    unsigned sg = 1;
#define LWAIT(idx, v, out) _Pragma("unroll 1") for (unsigned sp = 0;; ++sp) { \
      out = rfl(ctl[idx]); if (out + 1u > (v)) break; \
      if (sp > SPIN_CAP) return; \
      if (sp == 6) { waitvm<0>(); pub = u; ctl[C_FULL] = pub; } \
      __builtin_amdgcn_s_sleep(1); }
#define HALF(rs, so, ld) do { waitvm<WLD>(); if (u - WINE > pub) { pub = u - WINE; ctl[C_FULL] = pub; } dma8(rs, voff, so, ld); u += 2; } while (0)
#define SEG(rs, so) do { unsigned fr_; LWAIT(C_FREE + (sg & 7), sg < 8 ? 0u : sg < 16 ? 2u : sg - 7, fr_) \
      const unsigned ld_ = lb + (sg & 7) * 16384; HALF(rs, so, ld_); HALF(rs, (so) + 8192, ld_ + 8192); ++sg; } while (0)
    const unsigned b1 = (32 + g) * (2u << 20);
    unsigned k = 0, dsc = 0, nk = NS;
    bool second = false;
#define POOLOFF(d) (b1 + ((NSTAT * 8 + ((d) >> 20) + g * 8) & 127) * 16384)
#define NEXT_ITEM(t, n) do { \
      if (second) { dsc += (1u << 20) + (1u << 10); second = false; } \
      else { second = t < (unsigned)TP; dsc = (8 + t) | ((NS + k) & 3) << 8 | (second ? 0u : 1u << 10) | (4 + k) << 12 | (second ? 2 * t : TP + t) << 20; ++k; } \
      ctl[C_DESC + (n) - NS] = dsc; } while (0)
    const __amdgpu_buffer_rsrc_t rkb = mkrs(a.kB, 128u << 20), rvb = mkrs(a.vB, 128u << 20);
#pragma unroll 1
    for (int run = 0; run < 4; ++run) {
      const __amdgpu_buffer_rsrc_t rs = mkrs(run == 0 ? a.kA : run == 1 ? a.vA : run == 2 ? a.kB : a.vB, run < 2 ? 64u << 20 : 128u << 20);
      const int cnt = run < 2 ? 4 : NS - 2 - run;
#pragma unroll 1
      for (int i = run == 0; i < cnt; ++i) {
        const unsigned so = run < 2 ? offA + i * 131072 : (g + (i & 16) * 2) * (2u << 20) + ((i * 8 + wg) & 127) * 16384;
        if (sg == NS + 4 - KAH) ctl[C_WANT] = 1;
        SEG(rs, so);
      }
      if (run == 2) {
#pragma unroll 1
        for (;;) {
          unsigned t = 0;
          if (!second) { LWAIT(C_TICK + k, 0u, t) if (t >= (unsigned)NT) break; }
          NEXT_ITEM(t, nk);
          const bool more = second && nk + 4 <= NKMAX;
          if (more) ctl[C_WANT] = k + 1;
          SEG(rkb, POOLOFF(dsc));
          ++nk;
          if (!second && (t >= (unsigned)TP || nk + 2 > NKMAX)) break;
        }
        ctl[C_NK] = nk;
      }
    }
    ctl[C_LATE] = 1;
    unsigned sv = b1 + (((NS - 5) * 8 + wg) & 127) * 16384;
#pragma unroll 1
    for (unsigned n = NS;; ++n) {
      unsigned t = 0, sk = 0;
      if (n + 2 >= nk) ctl[C_WANT] = k + 1;
      if (n < nk) sk = POOLOFF(rfl(ctl[C_DESC + n - NS]));
      else {
        if (!second) { LWAIT(C_TICK + k, 0u, t) }
        if (t < (unsigned)NT) {
          NEXT_ITEM(t, n);
          sk = POOLOFF(dsc);
          ctl[C_WANT] = k + 1;
          SEG(rkb, sk);
        } else {
          ctl[C_END] = n; ctl[C_PHASE] = 1; ++sg;
        }
      }
      SEG(rvb, sv);
      if (t >= (unsigned)NT) break;
      sv = sk;
    }
    ctl[C_PHASE] = 2;
#define DRAIN(k_) if (WINE >= k_) { waitvm<4 * k_>(); const int p_ = u - k_ + (k_ < 4 ? 4 : 0); if (p_ > pub) { pub = p_; ctl[C_FULL] = pub; } }
    DRAIN(8) DRAIN(6) DRAIN(4) DRAIN(2) DRAIN(0)
    return;
  }
  const __amdgpu_buffer_rsrc_t ws = mkrs(a.ws, W_ENDB);
  const unsigned tstart = now32();
  const int cb = (wg & ~15) + ((wg & 7) << 1) + ((wg >> 3) & 1);
  const int side = j >> 2, prod = g * 4 + (j & 3);

  if (w == 1) {
    const v2u ep = __builtin_amdgcn_raw_buffer_load_b64(ws, W_X, 0, SC1);
    if (cb >= 128 && (cb & 3) == 0 && lane == 0) __builtin_amdgcn_raw_buffer_store_b32(0u, ws, W_CTR + ((cb - 128) >> 2) * 256, 0, SC1);
    ctl[lane] = lane == C_END || lane == C_NK ? 0x7fffffffu : 0u;
    ctl[lane + 64] = 0xffffffffu; ctl[lane + 128] = 0xffffffffu; ctl[lane + 192] = 0xffffffffu;
    asm volatile("s_waitcnt lgkmcnt(0)\n\ts_barrier" ::: "memory");
    unsigned ta = rfl(ep.x) + 1u, tb = rfl(ep.y) + 1u;
    ta += ta == 0u; tb += tb == 0u;
    waitvm<0>();
    if (!wait_ge<1>(ctl, C_QCNT, 9)) return;
    if (lane < 16) {
      v4f s = {0.f, 0.f, 0.f, 0.f};
#pragma unroll
      for (int i = 0; i < 9; ++i) s += *(LAS v4f*)(L + L_QP + i * 256 + lane * 16);
      const int b = lane >> 3, quad = lane & 7;
      const int off = cb < 128 ? W_QA + ((b * 16 + (cb >> 3)) * 64 + (cb & 7) * 8 + quad) * 16 : W_QB + (((cb - 128) >> 2) * 64 + b * 32 + (cb & 3) * 8 + quad) * 16;
      gst(ws, off, pk_bf16(s.x, s.y), pk_bf16(s.z, s.w), ta, tb);
    }
    v4u qa, qb;
#pragma unroll 1
    for (unsigned sp = 0;; ++sp) {
      __builtin_amdgcn_s_sleep(8);
      qa = gld(ws, W_QA + g * 1024 + lane * 16);
      qb = gld(ws, W_QB + g * 1024 + lane * 16);
      if (__all(gbad(qb, ta, tb, gbad(qa, ta, tb, 0u)) == 0u)) break;
      if (sp > SPIN_CAP) return;
    }
    *(LAS v2u*)(L + L_Q + lane * 8) = v2u{qa.x, qa.z};
    *(LAS v2u*)(L + L_Q + 512 + lane * 8) = v2u{qb.x, qb.z};
    ctl[C_TAGA] = ta; ctl[C_TAGB] = tb;
    lgkm0();
    ctl[C_QRDY] = 1;
#pragma unroll 1
    for (int k = 0; k <= NT; ++k) {
      if (!wait_ge<2>(ctl, C_WANT, k + 1)) return;
      unsigned t = 0;
      if (lane == 0) t = __hip_atomic_fetch_add((GAS unsigned*)(a.ws + W_CTR + g * 256), 1u, __ATOMIC_RELAXED, __HIP_MEMORY_SCOPE_AGENT);
      t = rfl(t);
      ctl[C_TICK + k] = t;
      if (t >= (unsigned)NT) break;
    }
    if (!wait_ge<1>(ctl, C_T1, 1)) return;
    hint_set(ws, lane < NREP ? W_F2 + (side * NREP + lane) * 1024 + prod * 8 : OOB, ctl[C_T1], ta);
    if (prod < 72) {
      const int ho = W_F2 + (side * NREP + prod % NREP) * 1024 + lane * 8;
      ctl[C_T2] = (hint_time<true>(ws, ho, ho + 512, ta, tstart) + tstart + DT2) | 1u;
    }
    return;
  }
  if (w == 2) {
    {
      const size_t pa = (size_t)g * (2u << 20) + (3u << 19), pb = (size_t)g * (2u << 20), pc = pb + (64u << 20);
      const unsigned char* wq = cb < 128 ? a.wqA : a.wqB;
      const unsigned char* pw = (side ? a.woB : a.woA) + (size_t)((side ? g : 2 * (g & 15) + (g >> 4)) * 128 + (j & 3) * 32) * 1152;
      unsigned d0, d1, d2, d3, d4, d5, d6;
      asm volatile("s_load_dword %0, %6, 0\n\ts_load_dword %1, %7, 0\n\ts_load_dword %2, %8, 0\n\ts_load_dword %3, %9, 0\n\ts_load_dword %4, %10, 0\n\ts_load_dword %5, %11, 0\n\t"
                   "s_barrier\n\ts_waitcnt lgkmcnt(0)"
                   : "=&s"(d0), "=&s"(d1), "=&s"(d2), "=&s"(d3), "=&s"(d4), "=&s"(d5)
                   : "s"(wq), "s"(wq + (2u << 20)), "s"(wq + (4u << 20)), "s"(cb < 128 ? a.xA : a.xB), "s"(a.ws + 2048), "s"(a.vA + pa) : "memory");
      asm volatile("s_load_dword %0, %7, 0\n\ts_load_dword %1, %8, 0\n\ts_load_dword %2, %9, 0\n\ts_load_dword %3, %10, 0\n\ts_load_dword %4, %11, 0\n\t"
                   "s_load_dword %5, %12, 0\n\ts_load_dword %6, %13, 0\n\ts_waitcnt lgkmcnt(0)"
                   : "=&s"(d0), "=&s"(d1), "=&s"(d2), "=&s"(d3), "=&s"(d4), "=&s"(d5), "=&s"(d6)
                   : "s"(a.kB + pb), "s"(a.kB + pc), "s"(a.vB + pb), "s"(a.vB + pc), "s"(pw), "s"(a.ws + W_SAFE1), "s"(a.ws + W_SAFE2) : "memory");
    }
    const int nr1 = side ? 56 : 8;
    const int ta_ = (g & 15) * 2 + (g >> 4);
    const int b0 = side ? W_P1 + g * P1_STR : W_P + ta_ * P_STR, b1 = W_P + (side ? 64 + g : 32 + ta_) * P_STR, d0 = side ? 8192 : 1024, dest = 2 * (j & 3);
    int voff = b0 + dest * d0 + lane * 16;
    int voffb = voff + d0;
    int voff7 = b1 + dest * 1024 + lane * 16;
    int voff8 = lane >= 56 ? b1 + 8192 + (lane - 56) * 16 : b0 + 8 * d0 + (lane < nr1 ? lane : 0) * 16;
    asm volatile("" : "+v"(voff), "+v"(voffb), "+v"(voff7), "+v"(voff8));
    if (!wait_ge<16>(ctl, C_LATE, 1)) return;
    if (!wait_ge<2>(ctl, C_PHASE, 2)) return;
    const unsigned ta = rfl(ctl[C_TAGA]), tb = rfl(ctl[C_TAGB]);
    const int hoff = side ? (lane < 8 ? W_F1 + (g * 8 + lane) * 128 : OOB) : (lane < 16 ? W_F1 + ((g & 15) * 8 + (lane >> 3) * 128 + (lane & 7)) * 128 : OOB);
    wait_until(tstart, hint_time<false>(ws, hoff, OOB, ta, tstart) + DT1);
    v4u X[2][8], ML;
#pragma unroll 1
    for (unsigned sp = 0;; ++sp) {
      const unsigned tr = now32();
      ML = gld(ws, voff8);
#pragma unroll
      for (int i = 0; i < 7; ++i) { X[0][i] = gld(ws, voff, side ? i * 1024 : 0); X[1][i] = gld(ws, voffb, side ? i * 1024 : 0); }
      X[0][7] = gld(ws, voff7); X[1][7] = gld(ws, voff7 + 1024);
      ctl[C_T1] = tr | 1u;
      __builtin_amdgcn_sched_barrier(0);
      const bool seg1 = lane >= 56;
      const float mr = fbits(ML.x);
      const float M = max8(mr);
      const float M0 = max8to64(seg1 ? -INFINITY : M);
      const float f = (lane >= nr1 && !seg1) ? 0.f : ex2(mr - (seg1 ? M : M0));
      float fb[8];
#pragma unroll
      for (int b = 0; b < 8; ++b) fb[b] = fbits(__builtin_amdgcn_ds_bpermute((8 * b + (lane >> 3)) * 4, ubits(f)));
      float Ls = f * fbits(ML.z);
      Ls += dpp<0xB1>(Ls); Ls += dpp<0x4E>(Ls); Ls += dpp<0x141>(Ls);
      const float L1 = fbits(__builtin_amdgcn_readlane(ubits(Ls), 56));
      float L0 = seg1 ? 0.f : Ls;
      L0 += dpp<0x128>(L0); L0 = xsum(L0);
      const float inv = __builtin_amdgcn_rcpf(lane >= 32 ? L1 : L0);
      __builtin_amdgcn_sched_barrier(0);
      unsigned bad = gbad(ML, ta, tb, 0u);
#pragma unroll
      for (int i = 0; i < 8; ++i) bad = gbad(X[1][i], ta, tb, gbad(X[0][i], ta, tb, bad));
      float oe[2], oo[2];
#pragma unroll
      for (int h = 0; h < 2; ++h) {
        float o0 = 0.f, o1 = 0.f;
#pragma unroll
        for (int b = 0; b < 7; ++b) { o0 = fmaf(fb[b], fbits(X[h][b].x), o0); o1 = fmaf(fb[b], fbits(X[h][b].z), o1); }
        oe[h] = rs32(o0, fb[7] * fbits(X[h][7].x)); oo[h] = rs32(o1, fb[7] * fbits(X[h][7].z));
      }
      float e = rs16(oe[0], oe[1]), o = rs16(oo[0], oo[1]);
      e += dpp<0x128>(e); o += dpp<0x128>(o);
      *(volatile LAS unsigned*)(L + L_OP + (lane >> 4) * 32 + (lane & 7) * 4) = pk_bf16(e * inv, o * inv);
      if (__all(bad == 0u)) break;
      if (sp > SPIN_CAP) return;
      __builtin_amdgcn_s_sleep(4);
    }
    ctl[C_OSEQ] = 1;
    return;
  }
  {
    const int qw = w - 3;
    const int r0 = qw * 64 + (lane >> 2) * 4;
    const unsigned char* xp = (cb < 128 ? a.xA : a.xB) + r0 * 2;
    const unsigned char* wp = (cb < 128 ? a.wqA : a.wqB) + (size_t)r0 * 8192 + (cb & 127) * 64 + (lane & 3) * 16;
    v4u Wq[4];
#pragma unroll
    for (int i = 0; i < 4; ++i) Wq[i] = __builtin_nontemporal_load((const GAS v4u*)(wp + i * 8192));
    const v2u X0 = *(const GAS v2u*)xp, X1 = *(const GAS v2u*)(xp + 1152);
    asm volatile("s_barrier" ::: "memory");
    float ac[2][8];
#pragma unroll
    for (int k = 0; k < 8; ++k) { ac[0][k] = 0.f; ac[1][k] = 0.f; }
#pragma unroll
    for (int s = 0; s < 2; ++s)
#pragma unroll
      for (int d = 0; d < 4; ++d) {
        const unsigned lo = __builtin_amdgcn_perm(Wq[2 * s + 1][d], Wq[2 * s][d], 0x05040100u), hi = __builtin_amdgcn_perm(Wq[2 * s + 1][d], Wq[2 * s][d], 0x07060302u);
        ac[0][2 * d] = dot2(lo, X0[s], ac[0][2 * d]); ac[0][2 * d + 1] = dot2(hi, X0[s], ac[0][2 * d + 1]);
        ac[1][2 * d] = dot2(lo, X1[s], ac[1][2 * d]); ac[1][2 * d + 1] = dot2(hi, X1[s], ac[1][2 * d + 1]);
      }
    float b8[8], c4[4];
#pragma unroll
    for (int k = 0; k < 8; ++k) b8[k] = rs32(ac[0][k], ac[1][k]);
#pragma unroll
    for (int k = 0; k < 4; ++k) { float t = rs16(b8[k], b8[k + 4]); t += dpp<0x128>(t); t += dpp<0x124>(t); c4[k] = t; }
    if ((lane & 12) == 0) *(LAS v4f*)(L + L_QP + qw * 256 + (lane >> 5) * 128 + (lane & 3) * 32 + ((lane >> 4) & 1) * 16) = v4f{c4[0], c4[1], c4[2], c4[3]};
    lds_inc(L, C_QCNT, lane);
  }
  const int r = lane >> 4, c = lane & 15;
  const unsigned kof = c * 256 + r * 16 + (c & 3) * 64;
  const unsigned vof = (4 * r + (c >> 2)) * 256 + (c >> 2) * 64 + (c & 3) * 8;
  if (w >= 4) {
    const unsigned aw = w - 4, off = aw * 8192, an = aw >> 1;
    v4u K0[4], K1[4], V[8];
    if (!wait_ge<1>(ctl, C_FULL, 2 * aw + 2)) return;
    read_k(L, off + kof, 0, K0); read_k(L, off + kof, 4096, K1);
    lds_inc(L, C_FREE + an, lane);
    if (!wait_ge<1>(ctl, C_FULL, 2 * aw + 18)) return;
    read_v(L, 65536 + off + vof, 0, V);
    lds_inc(L, C_FREE + 4 + an, lane);
    if (!wait_ge<1>(ctl, C_QRDY, 1)) return;
    const LAS v4u* qp = (const LAS v4u*)(L + L_Q) + r + (c >> 3) * 16;
    const v4u q[4] = {qp[0], qp[4], qp[8], qp[12]};
    v4f s0 = round2(qk(K0, q)), s1 = round2(qk(K1, q));
    const float M = xmax(fmaxf(max4(s0), max4(s1))) * L2E;
    s0 = expo(s0, M); s1 = expo(s1, M);
    const v4u P = {pk_bf16(s0[0], s0[1]), pk_bf16(s0[2], s0[3]), pk_bf16(s1[0], s1[1]), pk_bf16(s1[2], s1[3])};
    const float Ls = xsum(sum4(s0) + sum4(s1));
    v4f acc[8];
#pragma unroll
    for (int blk = 0; blk < 8; ++blk) acc[blk] = mfma(V[blk], P, v4f{0.f, 0.f, 0.f, 0.f});
#pragma unroll
    for (int k = 0; k < 4; ++k) acc[k] = sel4(bitmask(c, 2), acc[k + 4], acc[k]);
#pragma unroll
    for (int k = 0; k < 2; ++k) acc[k] = sel4(bitmask(c, 1), acc[k + 2], acc[k]);
    acc[0] = sel4(bitmask(c, 0), acc[1], acc[0]);
    LAS unsigned char* rec = L + L_REC + ((c >> 3) * 8 + aw) * REC_B;
    *(LAS v4f*)(rec + 16 + ((c & 7) * 16 + r * 4) * 4) = acc[0];
    if ((lane & 0x37) == 0) *(LAS v2f*)rec = v2f{M, Ls};
    if (last_arriver(L, C_CNT, 8, lane)) {
#pragma unroll 1
      for (int e = 0; e < 2; ++e) {
        float Mo, Lo;
        const v2f o = merge_rec<8>(L + L_REC + e * 8 * REC_B, lane, Mo, Lo);
        const int base = W_P + (g * 2 + e) * P_STR;
        if (park_rec(L, ctl, e, base + j * 128, 1024, base + 8192 + j * 16, lane, lane, o, Mo, Lo)) {
          const unsigned ta_ = ctl[C_TAGA], tb_ = ctl[C_TAGB];
          gst(ws, base + j * 128 + (lane >> 3) * 1024 + (lane & 7) * 16, ubits(o.x), ubits(o.y), ta_, tb_);
          if (lane == 0) gst(ws, base + 8192 + j * 16, ubits(Mo), ubits(Lo), ta_, tb_);
        }
      }
    }
  }
  if (w < 8) {
    const int k = w - 3;
    const unsigned char* pw = (side ? a.woB : a.woA) + (size_t)((side ? g : 2 * (g & 15) + (g >> 4)) * 128 + (j & 3) * 32) * 1152;
    const bool live = k < 4 || c < 8;
    if (!wait_ge<16>(ctl, C_LATE, 1)) return;
    const unsigned ta = rfl(ctl[C_TAGA]), tb = rfl(ctl[C_TAGB]);
    if (k == 0) {
      const unsigned t0 = __builtin_nontemporal_load((const GAS unsigned*)(pw + (lane & 1) * (31 * 1152))), t1 = __builtin_amdgcn_raw_buffer_load_b32(ws, lane & 1 ? W_SAFE1 : W_SAFE2, 0, SC1);
      const unsigned t2 = __builtin_amdgcn_raw_buffer_load_b32(mkrs(a.out, 4608), (lane & 1) * 4096, 0, 2);
      asm volatile("" ::"v"(t0), "v"(t1), "v"(t2));
    }
    if (!wait_ge<2>(ctl, C_PHASE, 1)) return;
    const unsigned held = rfl(ctl[C_HELD]);
    asm volatile("" ::: "memory");
#pragma unroll 1
    for (int slot = k; slot < NHOLD; slot += 5) {
      if (!(held >> slot & 1)) continue;
      LAS unsigned char* h = L + L_HOLD + slot * HOLD_B;
      const v4u hd = *(LAS v4u*)h;
      const v2u ml = *(LAS v2u*)(h + 16), o = *(LAS v2u*)(h + 32 + lane * 8);
      gst(ws, hd.x + (lane >> 3) * hd.y + (lane & 7) * 16, o.x, o.y, ta, tb);
      gst(ws, lane == 0 ? hd.z : OOB, ml.x, ml.y, ta, tb);
    }
    if (!wait_ge<2>(ctl, C_PHASE, 2)) return;
    v4u Wf[8], Wr[8];
    const __amdgpu_buffer_rsrc_t rw = mkrs(pw, 32 * 1152);
#pragma unroll
    for (int i = 0; i < 8; ++i) Wr[i] = __builtin_amdgcn_raw_buffer_load_b128(rw, (live ? 8 * r * 1152 + k * 256 + c * 16 : OOB) + i * 1152, 0, 2);
#pragma unroll
    for (int e = 0; e < 4; ++e)
#pragma unroll
      for (int d = 0; d < 4; ++d) {
        Wf[2 * e][d] = __builtin_amdgcn_perm(Wr[2 * d + 1][e], Wr[2 * d][e], 0x05040100u);
        Wf[2 * e + 1][d] = __builtin_amdgcn_perm(Wr[2 * d + 1][e], Wr[2 * d][e], 0x07060302u);
      }
    int y0 = live ? W_Y + ((side ? 3 - (r & 1) : (r & 1)) * 36 + k * 8 + (c >> 2)) * YS + prod * 128 + (2 * (c & 3) + (r >> 1)) * 16 : OOB, y1 = y0 + (k < 4 ? 4 : 2) * YS;
    const unsigned mh = bitmask(r, 1);
    asm volatile("" : "+v"(y0), "+v"(y1), "+v"(Wf[0]), "+v"(Wf[1]), "+v"(Wf[2]), "+v"(Wf[3]), "+v"(Wf[4]), "+v"(Wf[5]), "+v"(Wf[6]), "+v"(Wf[7]));
    v4u oa;
#pragma unroll 1
    for (unsigned sp = 0;; ++sp) {
      const unsigned fl = ctl[C_OSEQ];
      oa = *(volatile LAS v4u*)(L + L_OP + ((c >> 2) & 1) * 64 + r * 16);
      if (rfl(fl)) break;
      if (sp > SPIN_CAP) return;
      __builtin_amdgcn_s_sleep(1);
    }
#pragma unroll
    for (int i = 0; i < 2; ++i) {
      v4f d[4];
#pragma unroll
      for (int f = 0; f < 4; ++f) d[f] = mfma(oa, Wf[(f >> 1) * 4 + 2 * i + (f & 1)], v4f{0.f, 0.f, 0.f, 0.f});
      gst(ws, i ? y1 : y0, ubits(sel(mh, d[2][0], d[0][0])), ubits(sel(mh, d[3][0], d[1][0])), ta, tb);
    }
    return;
  }
  const unsigned ta = rfl(ctl[C_TAGA]), tb = rfl(ctl[C_TAGB]);
  const int cw = w - 8;
  {
    float m = -INFINITY, l = 0.f;
    v4f acc[8];
    bool fresh = true, freshv = true;
    const unsigned mk0 = bitmask(c, 0), mk1 = bitmask(c, 1), mk2 = bitmask(c, 2), mk3 = bitmask(c, 3);
    const int gid = (c >> 1) * 8 + 2 * r + (c & 1);
    int mode = 1;
    unsigned nk = 0x7fffffffu;
#pragma unroll 1
    for (unsigned n = 4 + cw;; n = n + 4 < NS ? n + 4 : n < NS ? NS : n + 1) {
      bool stop = false;
      unsigned dsc = cw << 8;
#pragma unroll 1
      for (unsigned sp = 0; n >= NS; ++sp) {
        const unsigned dd = ctl[C_DESC + n - NS], en = ctl[mode == 1 ? C_NK : C_END];
        dsc = rfl(dd);
        if (n >= rfl(en) || sp > SPIN_CAP) {
          if (mode != 1) { stop = true; break; }
          nk = n; n = 4 + cw; mode = 2;
        } else if (dsc == 0xffffffffu) __builtin_amdgcn_s_sleep(2);
        else if ((dsc >> 8 & 3) == (unsigned)cw) break;
        else ++n;
      }
      if (stop) break;
      if (n >= nk) mode = 3;
      const bool dyn = n >= NS;
      const bool pubn = dyn ? (dsc >> 10 & 1) : (n >= NS - 4 || (n & ~3u) == 16);
      const int base = n >= 20 ? W_P1 + g * P1_STR : W_P + (64 + g) * P_STR, dstr = n >= 20 ? 8192 : 1024, rec = dyn ? dsc & 255 : j, slot = dyn ? dsc >> 12 & 255 : 2 + (n >= 20);
      const int gi = dyn ? gid : lane;
      const unsigned sk = n < nk ? n + 4 : 2 * n - 1, sv = n + 1 < nk ? n + nk : 2 * n + 2;
      int ad = base + rec * 128 + (gi >> 3) * dstr + (gi & 7) * 16, am = lane == 0 ? base + 8 * dstr + rec * 16 : OOB;
      unsigned ka = (sk & 7) * 16384 + kof, va = (sv & 7) * 16384 + vof;
      LAS unsigned char* pb = L + L_PB + (mode == 3 ? 0 : n - 4) * PB_B;
      v4u P0, P1, st;
      float al, Ls, mrec;
      const bool first = freshv;
      if ((mode & 2) && freshv) {
#pragma unroll
        for (int blk = 0; blk < 8; ++blk) acc[blk] = v4f{0.f, 0.f, 0.f, 0.f};
        freshv = false;
      }
      if (mode & 1) {
        const LAS v4u* qp = (const LAS v4u*)(L + L_Q) + r + (n < 20 ? 32 : 48);
        v4u q[4] = {qp[0], qp[4], qp[8], qp[12]};
        if (fresh) { m = -INFINITY; l = 0.f; }
        fresh = pubn;
        asm volatile("" : "+v"(ad), "+v"(am), "+v"(ka), "+v"(va), "+v"(q[0]), "+v"(q[1]), "+v"(q[2]), "+v"(q[3]));
        if (!wait_ge<1>(ctl, C_FULL, 4 * sk + 4)) return;
        v4f s[4];
        v4u K[4][4];
#pragma unroll
        for (int uu = 0; uu < 4; ++uu) read_k(L, ka, uu * 4096, K[uu]);
        __builtin_amdgcn_sched_barrier(0);
#pragma unroll
        for (int uu = 0; uu < 4; ++uu) s[uu] = qk(K[uu], q);
        __builtin_amdgcn_sched_barrier(0);
        lgkm0();
        ctl[C_FREE + (sk & 7)] = sk + 1;
        v4f sc = round2(sel4(mk3, sel4(mk2, s[3], s[2]), sel4(mk2, s[1], s[0])));
        const float mn = fmaxf(max4to64(max4(sc)) * L2E, m);
        st = *(volatile LAS v4u*)(L + L_CTL);
        sc = expo(sc, mn);
        const unsigned px = pk_bf16(sc[0], sc[1]), py = pk_bf16(sc[2], sc[3]);
        P0 = v4u{bcast16<0>(px), bcast16<0>(py), bcast16<4>(px), bcast16<4>(py)}; P1 = v4u{bcast16<8>(px), bcast16<8>(py), bcast16<12>(px), bcast16<12>(py)};
        al = ex2(m - mn);
        mrec = m = mn;
        l = fmaf(l, al, sum4(sc));
        Ls = l + dpp<0x124>(l);
        Ls += dpp<0x128>(Ls);
        Ls = xsum(Ls);
        if (mode == 1) {
          *(LAS v4u*)(pb + r * 16) = P0; *(LAS v4u*)(pb + 64 + r * 16) = P1; *(LAS v4f*)(pb + 128) = v4f{al, Ls, mn, 0.f};
        }
      } else {
        P0 = *(LAS v4u*)(pb + r * 16); P1 = *(LAS v4u*)(pb + 64 + r * 16);
        const v4f t = *(LAS v4f*)(pb + 128);
        al = t.x; Ls = t.y; mrec = t.z;
        st = *(volatile LAS v4u*)(L + L_CTL);
      }
      if (mode == 1) continue;
      if (!first) {
#pragma unroll
        for (int blk = 0; blk < 8; ++blk) acc[blk] *= al;
      }
      asm volatile("" : "+v"(P0), "+v"(P1), "+v"(Ls), "+v"(acc[0]), "+v"(acc[1]), "+v"(acc[2]), "+v"(acc[3]), "+v"(acc[4]), "+v"(acc[5]), "+v"(acc[6]), "+v"(acc[7]));
      v4u V[8];
#define WAITV(need) _Pragma("unroll 1") for (unsigned sp = 0; rfl(st.x) < (need); ++sp) { if (sp > SPIN_CAP) return; if (sp) __builtin_amdgcn_s_sleep(1); st = *(volatile LAS v4u*)(L + L_CTL); } \
      asm volatile("" ::: "memory");
      WAITV(4 * sv + 2)
      read_v(L, va, 0, V);
#pragma unroll
      for (int blk = 0; blk < 8; ++blk) acc[blk] = mfma(V[blk], P0, acc[blk]);
      WAITV(4 * sv + 4)
      const unsigned tl = now32(), nend = rfl(st.y);
      read_v(L, va, 8192, V);
      hint_set(ws, lane == 0 && n + 1 == nend ? W_F1 + wg * 128 : OOB, tl, ta);
#pragma unroll
      for (int blk = 0; blk < 8; ++blk) acc[blk] = mfma(V[blk], P1, acc[blk]);
      if (pubn) {
        v2f o[8];
#pragma unroll
        for (int k = 0; k < 8; ++k) o[k] = sel2(mk0, v2f{acc[k][2], acc[k][3]}, v2f{acc[k][0], acc[k][1]});
#pragma unroll
        for (int k = 0; k < 4; ++k) o[k] = sel2(mk3, o[k + 4], o[k]);
#pragma unroll
        for (int k = 0; k < 2; ++k) o[k] = sel2(mk2, o[k + 2], o[k]);
        o[0] = sel2(mk1, o[1], o[0]);
        bool now = dyn && (slot >= NHOLD || rfl(st.z) != 0);
        if (!now) {
          bool emit = true;
          if (!dyn) {
            LAS unsigned char* rp = L + L_REC + (8 + slot * 4) * REC_B;
            *(LAS v2f*)(rp + cw * REC_B + 16 + gid * 8) = o[0];
            if (lane == 0) *(LAS v2f*)(rp + cw * REC_B) = v2f{mrec, Ls};
            emit = last_arriver(L, C_CNT + slot - 1, 4, lane);
            if (emit) o[0] = merge_rec<4>(rp, lane, mrec, Ls);
          }
          if (emit) now = park_rec(L, ctl, slot, base + rec * 128, dstr, base + 8 * dstr + rec * 16, gi, lane, o[0], mrec, Ls);
          if (!now) { ad = OOB; am = OOB; }
        }
        gst(ws, ad, ubits(o[0].x), ubits(o[0].y), ta, tb);
        gst(ws, am, ubits(mrec), ubits(Ls), ta, tb);
        freshv = true;
      }
      lgkm0();
      ctl[C_FREE + (sv & 7)] = sv + 1;
    }
  }
  if (prod >= 72) return;
  const int cmb = side * 2 + (prod & 1), rho = prod >> 1;
  int voff = W_Y + (cmb * 36 + rho) * YS + cw * 4096 + lane * 16;
  const int yi = rho < 32 ? (rho >> 2) & 1 : (rho >> 1) & 1, yq = rho < 32 ? rho & 3 : rho & 1;
  int oo = lane < 16 ? ((cmb & 1) * 1152 + (cmb >> 1) * 576 + (rho >> 3) * 128 + (yq * 4 + (lane >> 2)) * 8 + ((lane >> 1) & 1) * 4 + yi * 2 + (lane & 1)) * 2 : OOB;
  asm volatile("" : "+v"(voff), "+v"(oo));
  if (!wait_ge<4>(ctl, C_T1, 1)) return;
  if (!wait_ge<1>(ctl, C_T2, 1)) return;
  wait_until(tstart, rfl(ctl[C_T2]) - tstart);
  v4u X[4];
#pragma unroll 1
  for (unsigned sp = 0;; ++sp) {
#pragma unroll
    for (int i = 0; i < 4; ++i) X[i] = gld(ws, voff, i * 1024);
    unsigned bad = 0;
#pragma unroll
    for (int i = 0; i < 4; ++i) bad = gbad(X[i], ta, tb, bad);
    if (__all(bad == 0u)) break;
    if (sp > SPIN_CAP) return;
    __builtin_amdgcn_s_sleep(4);
  }
  float s = rs32((fbits(X[0].x) + fbits(X[1].x)) + (fbits(X[2].x) + fbits(X[3].x)), (fbits(X[0].z) + fbits(X[1].z)) + (fbits(X[2].z) + fbits(X[3].z)));
  s = rs16(s, s);
  s += dpp<0x128>(s);
  *(volatile LAS unsigned*)(L + L_FP + (((lane & 7) * 2 + (lane >> 5)) * 4 + cw) * 4) = min(ubits(s), 0xfffffffeu);
  if (cw != 0) return;
#pragma unroll 1
  for (unsigned sp = 0;; ++sp) {
    const v4u p = *(volatile LAS v4u*)(L + L_FP + (lane & 15) * 16);
    s = (fbits(p.x) + fbits(p.y)) + (fbits(p.z) + fbits(p.w));
    if (__all(p.y != ~0u && p.z != ~0u && p.w != ~0u)) break;
    if (sp > SPIN_CAP) return;
  }
  __builtin_amdgcn_raw_buffer_store_b16((unsigned short)pk_bf16(s, s), mkrs(a.out, 4608), oo, 0, 0);
  __builtin_amdgcn_raw_buffer_store_b64(v2u{ta, tb}, ws, cmb == 3 && rho == 0 && lane == 0 ? W_X : OOB, 0, SC1);
}

extern "C" void kernel_launch(void* const* d_in, const int* in_sizes, int n_in, void* d_out, int out_size, void* d_ws, size_t ws_size,
                              hipStream_t stream) {
  Args a;
  a.xA = (const unsigned char*)d_in[0]; a.kA = (const unsigned char*)d_in[1]; a.vA = (const unsigned char*)d_in[2];
  a.wqA = (const unsigned char*)d_in[3]; a.woA = (const unsigned char*)d_in[4];
  a.xB = (const unsigned char*)d_in[5]; a.kB = (const unsigned char*)d_in[6]; a.vB = (const unsigned char*)d_in[7];
  a.wqB = (const unsigned char*)d_in[8]; a.woB = (const unsigned char*)d_in[9];
  a.out = (unsigned char*)d_out; a.ws = (unsigned char*)d_ws;
  fused_decode<<<256, NTHR, 0, stream>>>(a);
}
```

```cpp
#include <hip/hip_runtime.h>
#include <stdint.h>

typedef unsigned v4u __attribute__((ext_vector_type(4)));
typedef unsigned v2u __attribute__((ext_vector_type(2)));
typedef float v4f __attribute__((ext_vector_type(4)));
typedef float v2f __attribute__((ext_vector_type(2)));
typedef short v4s __attribute__((ext_vector_type(4)));
typedef __bf16 bf16x2 __attribute__((ext_vector_type(2)));
typedef __bf16 bf16x8 __attribute__((ext_vector_type(8)));
#define LAS __attribute__((address_space(3)))
#define GAS __attribute__((address_space(1)))
#define DI __device__ __forceinline__

constexpr int WLD = 22;
constexpr int DT1 = 50;
constexpr int DT2 = 100;
constexpr int KAH = 3;
constexpr int NTHR = 12 * 64;
constexpr int NSTAT = 9;
constexpr int NS = 4 + 16 + NSTAT;
constexpr int POOLC = (16 - NSTAT) * 8;
constexpr int NT = 48, TP = POOLC - NT;
constexpr int WINE = (WLD + 7) / 8 * 2;
constexpr int NREP = 16;
constexpr int NKMAX = 52;
constexpr unsigned SPIN_CAP = 1u << 20;
constexpr int NHOLD = 16, HOLD_B = 544, REC_B = 528, PB_B = 144;
constexpr int L_CTL = 131072  , L_FP = L_CTL + 768  , L_Q = L_CTL + 1024, L_OP = L_Q + 1024, L_QP = L_OP + 128,
              L_REC = L_QP + 9 * 256, L_HOLD = L_REC + 24 * REC_B, L_PB = L_HOLD + NHOLD * HOLD_B, L_END = L_PB + (NKMAX - 4) * PB_B;
static_assert(L_END <= 163840 && TP >= 0 && TP <= NT && NT + 2 <= 64 && POOLC <= 64 && WINE >= 2 && WINE <= 8, "");
enum { C_FULL = 0  , C_END = 1  , C_PHASE = 2  , C_NK = 3, C_WANT = 4  ,
       C_T1 = 5  , C_TAGA = 6, C_TAGB = 7, C_FREE = 8  , C_CNT = 16  , C_QCNT = 19, C_QRDY = 20, C_HELD = 21, C_LATE = 23, C_OSEQ = 22,
       C_T2 = 24  , C_TICK = 64, C_DESC = 128 };
constexpr int W_X = 0, W_CTR = 4096, W_F1 = 16384  , W_F2 = W_F1 + 32768  , W_QA = W_F2 + 2 * NREP * 1024, W_QB = W_QA + 32768,
              W_P = W_QB + 32768, P_STR = 9216  , W_P1 = W_P + 96 * P_STR, P1_STR = 66560  ,
              W_Y = W_P1 + 32 * P1_STR, YS = 16384, W_ENDB = 256 + W_Y + 4 * 36 * YS  ;
constexpr int W_SAFE1 = W_P1 + 24 * P1_STR + 7424, W_SAFE2 = W_ENDB - 128;
constexpr float SCALE = 0.088388347648318440f, L2E = 1.4426950408889634f;
constexpr int SC1 = 16, OOB = 0x70000000;

struct Args {
  const unsigned char *xA, *kA, *vA, *wqA, *woA, *xB, *kB, *vB, *wqB, *woB;
  unsigned char *out, *ws;
};

DI float dot2(unsigned a, unsigned b, float c) { return __builtin_amdgcn_fdot2_f32_bf16(__builtin_bit_cast(bf16x2, a), __builtin_bit_cast(bf16x2, b), c, false); }
template <int CTRL> DI float dpp(float x) { return __builtin_bit_cast(float, __builtin_amdgcn_mov_dpp(__builtin_bit_cast(int, x), CTRL, 0xf, 0xf, true)); }
template <int LANE> DI unsigned bcast16(unsigned x) { return __builtin_amdgcn_mov_dpp(x, 0x150 + LANE, 0xf, 0xf, true); }
DI unsigned pk_bf16(float lo, float hi) {
  unsigned r = __builtin_bit_cast(unsigned, bf16x2{(__bf16)lo, (__bf16)hi});
  asm("" : "+v"(r));
  return r;
}
DI float bf_lo(unsigned u) { return __builtin_bit_cast(float, u << 16); }
DI float bf_hi(unsigned u) { return __builtin_bit_cast(float, u & 0xffff0000u); }
DI float fbits(unsigned u) { return __builtin_bit_cast(float, u); }
DI unsigned ubits(float f) { return __builtin_bit_cast(unsigned, f); }
DI unsigned rfl(unsigned x) { return __builtin_amdgcn_readfirstlane(x); }
DI float ex2(float x) { return __builtin_amdgcn_exp2f(x); }
DI v4f mfma(v4u a, v4u b, v4f c) { return __builtin_amdgcn_mfma_f32_16x16x32_bf16(__builtin_bit_cast(bf16x8, a), __builtin_bit_cast(bf16x8, b), c, 0, 0, 0); }
DI v2u trd(LAS unsigned char* p) { return __builtin_bit_cast(v2u, __builtin_amdgcn_ds_read_tr16_b64_v4i16((LAS v4s*)p)); }
DI float rs32(float x, float y) { auto s = __builtin_amdgcn_permlane32_swap(ubits(x), ubits(y), false, false); return fbits(s[0]) + fbits(s[1]); }
DI float rs16(float x, float y) { auto s = __builtin_amdgcn_permlane16_swap(ubits(x), ubits(y), false, false); return fbits(s[0]) + fbits(s[1]); }
DI float xsum(float x) { x = rs16(x, x); return rs32(x, x); }
DI float xmax(float x) {
  auto s = __builtin_amdgcn_permlane16_swap(ubits(x), ubits(x), false, false); x = fmaxf(fbits(s[0]), fbits(s[1]));
  auto t = __builtin_amdgcn_permlane32_swap(ubits(x), ubits(x), false, false); return fmaxf(fbits(t[0]), fbits(t[1]));
}
DI float max8(float x) {
  asm("s_nop 1\n\tv_max_f32_dpp %0, %0, %0 quad_perm:[1,0,3,2] row_mask:0xf bank_mask:0xf\n\ts_nop 1\n\tv_max_f32_dpp %0, %0, %0 quad_perm:[2,3,0,1] row_mask:0xf bank_mask:0xf\n\t"
      "s_nop 1\n\tv_max_f32_dpp %0, %0, %0 row_half_mirror row_mask:0xf bank_mask:0xf" : "+v"(x));
  return x;
}
DI float max8to64(float x) {
  float t;
  asm("s_nop 1\n\tv_max_f32_dpp %0, %0, %0 row_ror:8 row_mask:0xf bank_mask:0xf\n\tv_mov_b32 %1, %0\n\ts_nop 1\n\tv_permlane16_swap_b32 %0, %1\n\tv_max_f32 %0, %0, %1\n\t"
      "v_mov_b32 %1, %0\n\ts_nop 1\n\tv_permlane32_swap_b32 %0, %1\n\tv_max_f32 %0, %0, %1" : "+v"(x), "=&v"(t));
  return x;
}
DI float max4to64(float x) {
  asm("s_nop 1\n\tv_max_f32_dpp %0, %0, %0 row_ror:4 row_mask:0xf bank_mask:0xf" : "+v"(x));
  return max8to64(x);
}
DI void lgkm0() { asm volatile("s_waitcnt lgkmcnt(0)" ::: "memory"); }
template <int N> DI void waitvm() { asm volatile("s_waitcnt vmcnt(%0)" ::"n"(N) : "memory"); }
DI __amdgpu_buffer_rsrc_t mkrs(const void* p, unsigned bytes) { return __builtin_amdgcn_make_buffer_rsrc((void*)p, 0, bytes, 0x00020000); }
DI void dma8(__amdgpu_buffer_rsrc_t rs, unsigned voff, unsigned soff, unsigned lds) {
  unsigned keep, s2;
  asm volatile("s_mov_b32 %0, m0\n\ts_mov_b32 m0, %5\n\ts_add_u32 %1, %4, 0x1000\n\ts_nop 2\n\t"
               "buffer_load_dwordx4 %2, %3, %4 offen nt lds\n\t"
               "buffer_load_dwordx4 %2, %3, %4 offen offset:1024 nt lds\n\t"
               "buffer_load_dwordx4 %2, %3, %4 offen offset:2048 nt lds\n\t"
               "buffer_load_dwordx4 %2, %3, %4 offen offset:3072 nt lds\n\t"
               "s_add_u32 m0, m0, 0x1000\n\ts_nop 0\n\t"
               "buffer_load_dwordx4 %2, %3, %1 offen nt lds\n\t"
               "buffer_load_dwordx4 %2, %3, %1 offen offset:1024 nt lds\n\t"
               "buffer_load_dwordx4 %2, %3, %1 offen offset:2048 nt lds\n\t"
               "buffer_load_dwordx4 %2, %3, %1 offen offset:3072 nt lds\n\t"
               "s_mov_b32 m0, %0"
               : "=&s"(keep), "=&s"(s2) : "v"(voff), "s"(rs), "s"(soff), "s"(lds) : "memory", "scc");
}
DI v4u gld(__amdgpu_buffer_rsrc_t rs, int off, int soff = 0) { return __builtin_amdgcn_raw_buffer_load_b128(rs, off, soff, SC1); }
DI void gst(__amdgpu_buffer_rsrc_t rs, int off, unsigned a, unsigned b, unsigned ta, unsigned tb) { __builtin_amdgcn_raw_buffer_store_b128(v4u{a, ta, b, tb}, rs, off, 0, SC1); }
DI unsigned gbad(v4u g, unsigned ta, unsigned tb, unsigned acc) { return __builtin_amdgcn_bitop3_b32(g.w, tb, __builtin_amdgcn_bitop3_b32(g.y, ta, acc, 0xBE), 0xBE); }

typedef volatile LAS unsigned* ctl_t;
template <int SLP> DI bool wait_ge(ctl_t ctl, int idx, unsigned v) {
#pragma unroll 1
  for (unsigned s = 0; rfl(ctl[idx]) < v; ++s) {
    if (s > SPIN_CAP) return false;
    __builtin_amdgcn_s_sleep(SLP);
  }
  asm volatile("" ::: "memory");
  return true;
}
DI void lds_inc(LAS unsigned char* L, int idx, int lane) {
  lgkm0();
  if (lane == 0) __hip_atomic_fetch_add((LAS unsigned*)(L + L_CTL) + idx, 1u, __ATOMIC_RELAXED, __HIP_MEMORY_SCOPE_WORKGROUP);
}
DI bool last_arriver(LAS unsigned char* L, int idx, unsigned n, int lane) {
  lgkm0();
  unsigned old = 0;
  if (lane == 0) old = __hip_atomic_fetch_add((LAS unsigned*)(L + L_CTL) + idx, 1u, __ATOMIC_RELAXED, __HIP_MEMORY_SCOPE_WORKGROUP);
  old = rfl(old);
  asm volatile("" ::: "memory");
  return old == n - 1;
}
DI void read_k(LAS unsigned char* L, unsigned off, int imm, v4u (&K)[4]) {
#pragma unroll
  for (int kk = 0; kk < 4; ++kk) K[kk] = *(const LAS v4u*)(L + (off ^ (kk * 64)) + imm);
}
DI void read_v(LAS unsigned char* L, unsigned off, int imm, v4u (&V)[8]) {
#pragma unroll
  for (int blk = 0; blk < 8; ++blk) {
    LAS unsigned char* p = L + (off ^ ((blk >> 1) * 64)) + (blk & 1) * 32 + imm;
    const v2u x = trd(p), y = trd(p + 4096);
    V[blk] = v4u{x.x, x.y, y.x, y.y};
  }
  __builtin_amdgcn_sched_barrier(0);
}
DI v4f qk(const v4u (&K)[4], const v4u (&q)[4]) {
  v4f d = {0.f, 0.f, 0.f, 0.f};
#pragma unroll
  for (int kk = 0; kk < 4; ++kk) d = mfma(K[kk], q[kk], d);
  return d;
}
DI v4f round2(v4f d) {
  const unsigned x01 = pk_bf16(d[0], d[1]), x23 = pk_bf16(d[2], d[3]);
  const unsigned b01 = pk_bf16(bf_lo(x01) * SCALE, bf_hi(x01) * SCALE), b23 = pk_bf16(bf_lo(x23) * SCALE, bf_hi(x23) * SCALE);
  return v4f{bf_lo(b01), bf_hi(b01), bf_lo(b23), bf_hi(b23)};
}
DI float sel(unsigned mask, float a, float b) { return fbits(__builtin_amdgcn_bitop3_b32(mask, ubits(a), ubits(b), 0xCA)); }
DI v4f sel4(unsigned mask, v4f a, v4f b) { return v4f{sel(mask, a[0], b[0]), sel(mask, a[1], b[1]), sel(mask, a[2], b[2]), sel(mask, a[3], b[3])}; }
DI v2f sel2(unsigned mask, v2f a, v2f b) { return v2f{sel(mask, a[0], b[0]), sel(mask, a[1], b[1])}; }
DI unsigned bitmask(int x, int bit) { unsigned m = (unsigned)((x << (31 - bit)) >> 31); asm("" : "+v"(m)); return m; }
DI float max4(v4f d) { return fmaxf(fmaxf(d[0], d[1]), fmaxf(d[2], d[3])); }
DI float sum4(v4f d) { return (d[0] + d[1]) + (d[2] + d[3]); }
DI v4f expo(v4f s, float m2) { return v4f{ex2(fmaf(s[0], L2E, -m2)), ex2(fmaf(s[1], L2E, -m2)), ex2(fmaf(s[2], L2E, -m2)), ex2(fmaf(s[3], L2E, -m2))}; }

DI unsigned now32() { return (unsigned)__builtin_amdgcn_s_memrealtime(); }
DI void wait_until(unsigned t0, unsigned until) {
#pragma unroll 1
  for (unsigned sp = 0; sp < 64 && now32() - t0 < until; ++sp) { }
}
DI void hint_set(__amdgpu_buffer_rsrc_t ws, int off, unsigned t, unsigned tag) {
  asm volatile("buffer_atomic_swap_x2 %0, %1, %2, 0 offen" ::"v"(v2u{t, tag}), "v"(off), "s"(ws) : "memory");
}
template <bool TWO> DI unsigned hint_time(__amdgpu_buffer_rsrc_t ws, int off, int off2, unsigned tag, unsigned t0) {
  unsigned rel = 0;
#define HP1(A0, A1, o) asm volatile("v_mov_b32 v16" #A0 ", 0\n\tv_mov_b32 v16" #A1 ", 0\n\tbuffer_atomic_add_x2 v[16" #A0 ":16" #A1 "], %0, %1, 0 offen sc0" \
                                    ::"v"(o), "s"(ws) : "memory", "v16" #A0, "v16" #A1)
#define HC1(A0, A1, o, N) { unsigned t_, g_; asm volatile("s_waitcnt vmcnt(%2)\n\tv_mov_b32 %0, v16" #A0 "\n\tv_mov_b32 %1, v16" #A1 : "=v"(t_), "=v"(g_) : "n"(N) : "memory", "v16" #A0, "v16" #A1); \
                            const bool ok_ = g_ == tag; rel = ok_ ? max(rel, t_ - t0) : rel; o = ok_ ? OOB : o; }
  HP1(0, 1, off);
  if (TWO) HP1(2, 3, off2);
  __builtin_amdgcn_s_sleep(8);
#pragma unroll 1
  for (unsigned sp = 0; sp < 4096; ++sp) {
    HP1(4, 5, off);
    if (TWO) HP1(6, 7, off2);
    HC1(0, 1, off, TWO ? 3 : 1)
    if (TWO) HC1(2, 3, off2, 2)
    if (__all(off == OOB && off2 == OOB)) break;
    HP1(0, 1, off);
    if (TWO) HP1(2, 3, off2);
    HC1(4, 5, off, TWO ? 3 : 1)
    if (TWO) HC1(6, 7, off2, 2)
    if (__all(off == OOB && off2 == OOB)) break;
  }
  return rfl((unsigned)max8to64(max8((float)rel)));
}
#undef HP1
#undef HC1
DI bool park_rec(LAS unsigned char* L, ctl_t ctl, int slot, int adr, int dstr, int mladr, int gi, int lane, v2f o, float m, float l) {
  LAS unsigned char* h = L + L_HOLD + slot * HOLD_B;
  *(LAS v2f*)(h + 32 + gi * 8) = o;
  if (lane == 0) { *(LAS v4u*)h = v4u{(unsigned)adr, (unsigned)dstr, (unsigned)mladr, 0u}; *(LAS v2f*)(h + 16) = v2f{m, l}; }
  lgkm0();
  if (lane == 0) __hip_atomic_fetch_or((LAS unsigned*)(L + L_CTL) + C_HELD, 1u << slot, __ATOMIC_RELAXED, __HIP_MEMORY_SCOPE_WORKGROUP);
  lgkm0();
  return rfl(ctl[C_PHASE]) != 0;
}
template <int NR> DI v2f merge_rec(LAS unsigned char* rec, int lane, float& Mo, float& Lo) {
  v2f av[NR];
  const v2f ml = *(LAS v2f*)(rec + (lane & (NR - 1)) * REC_B);
#pragma unroll
  for (int w = 0; w < NR; ++w) av[w] = *(LAS v2f*)(rec + w * REC_B + 16 + lane * 8);
  const float M = max8(ml.x), f = ex2(ml.x - M);
  float Ls = f * ml.y; Ls += dpp<0xB1>(Ls); Ls += dpp<0x4E>(Ls);
  if (NR == 8) Ls += dpp<0x141>(Ls);
  float a0 = 0.f, a1 = 0.f;
#pragma unroll
  for (int w = 0; w < NR; ++w) { const float fw = fbits(__builtin_amdgcn_readlane(ubits(f), w)); a0 = fmaf(fw, av[w].x, a0); a1 = fmaf(fw, av[w].y, a1); }
  Mo = M; Lo = Ls;
  return v2f{a0, a1};
}

__global__ void __launch_bounds__(NTHR, 3) fused_decode(Args a) {
  __shared__ __attribute__((aligned(16))) unsigned char smem[L_END];
  LAS unsigned char* L = (LAS unsigned char*)smem;
  ctl_t ctl = (ctl_t)(L + L_CTL);
  asm volatile("" ::"s"(a.xA), "s"(a.kA), "s"(a.vA), "s"(a.wqA), "s"(a.woA), "s"(a.xB), "s"(a.kB), "s"(a.vB), "s"(a.wqB), "s"(a.woB), "s"(a.out), "s"(a.ws));
  const int wg = blockIdx.x, g = wg >> 3, j = wg & 7;
  const int lane = threadIdx.x & 63, w = rfl(threadIdx.x >> 6);

  {
    const unsigned voff0 = (lane * 16) ^ ((lane >> 4) << 6), off0 = g * (2u << 20) + (3u << 19) + j * 16384;
    unsigned keep, so;
    asm volatile("s_cmp_lg_u32 %5, 0\n\ts_cbranch_scc1 1f\n\ts_mov_b32 %0, m0\n\ts_mov_b32 m0, %6\n\ts_mov_b32 %1, %4\n\ts_nop 1\n"
                 "0:\n\t"
                 "buffer_load_dwordx4 %2, %3, %1 offen nt lds\n\t"
                 "buffer_load_dwordx4 %2, %3, %1 offen offset:1024 nt lds\n\t"
                 "buffer_load_dwordx4 %2, %3, %1 offen offset:2048 nt lds\n\t"
                 "buffer_load_dwordx4 %2, %3, %1 offen offset:3072 nt lds\n\t"
                 "s_add_u32 m0, m0, 0x1000\n\ts_add_u32 %1, %1, 0x1000\n\ts_and_b32 vcc_lo, %1, 0x3000\n\ts_cbranch_scc1 0b\n\t"
                 "s_mov_b32 m0, %0\n"
                 "1:"
                 : "=&s"(keep), "=&s"(so) : "v"(voff0), "s"(mkrs(a.kA, 64u << 20)), "s"(off0), "s"(w), "s"((unsigned)(uintptr_t)L) : "memory", "scc", "vcc");
  }
  if (w == 0) {
    const unsigned lb = (unsigned)(uintptr_t)L;
    const unsigned voff = (lane * 16) ^ ((lane >> 4) << 6);
    const unsigned offA = g * (2u << 20) + (3u << 19) + j * 16384;
    asm volatile("s_barrier" ::: "memory");
    __builtin_amdgcn_s_setprio(3);
    int u = 4, pub = 0;
    unsigned sg = 1;
#define LWAIT(idx, v, out) _Pragma("unroll 1") for (unsigned sp = 0;; ++sp) { \
      out = rfl(ctl[idx]); if (out + 1u > (v)) break; \
      if (sp > SPIN_CAP) return; \
      if (sp == 6) { waitvm<0>(); pub = u; ctl[C_FULL] = pub; } \
      __builtin_amdgcn_s_sleep(1); }
#define HALF(rs, so, ld) do { waitvm<WLD>(); if (u - WINE > pub) { pub = u - WINE; ctl[C_FULL] = pub; } dma8(rs, voff, so, ld); u += 2; } while (0)
#define SEG(rs, so) do { unsigned fr_; LWAIT(C_FREE + (sg & 7), sg < 8 ? 0u : sg < 16 ? 2u : sg - 7, fr_) \
      const unsigned ld_ = lb + (sg & 7) * 16384; HALF(rs, so, ld_); HALF(rs, (so) + 8192, ld_ + 8192); ++sg; } while (0)
    const unsigned b1 = (32 + g) * (2u << 20);
    unsigned k = 0, dsc = 0, nk = NS;
    bool second = false;
#define POOLOFF(d) (b1 + ((NSTAT * 8 + ((d) >> 20) + g * 8) & 127) * 16384)
#define NEXT_ITEM(t, n) do { \
      if (second) { dsc += (1u << 20) + (1u << 10); second = false; } \
      else { second = t < (unsigned)TP; dsc = (8 + t) | ((NS + k) & 3) << 8 | (second ? 0u : 1u << 10) | (4 + k) << 12 | (second ? 2 * t : TP + t) << 20; ++k; } \
      ctl[C_DESC + (n) - NS] = dsc; } while (0)
    const __amdgpu_buffer_rsrc_t rkb = mkrs(a.kB, 128u << 20), rvb = mkrs(a.vB, 128u << 20);
#pragma unroll 1
    for (int run = 0; run < 4; ++run) {
      const __amdgpu_buffer_rsrc_t rs = mkrs(run == 0 ? a.kA : run == 1 ? a.vA : run == 2 ? a.kB : a.vB, run < 2 ? 64u << 20 : 128u << 20);
      const int cnt = run < 2 ? 4 : NS - 2 - run;
#pragma unroll 1
      for (int i = run == 0; i < cnt; ++i) {
        const unsigned so = run < 2 ? offA + i * 131072 : (g + (i & 16) * 2) * (2u << 20) + ((i * 8 + wg) & 127) * 16384;
        if (sg == NS + 4 - KAH) ctl[C_WANT] = 1;
        SEG(rs, so);
      }
      if (run == 2) {
#pragma unroll 1
        for (;;) {
          unsigned t = 0;
          if (!second) { LWAIT(C_TICK + k, 0u, t) if (t >= (unsigned)NT) break; }
          NEXT_ITEM(t, nk);
          const bool more = second && nk + 4 <= NKMAX;
          if (more) ctl[C_WANT] = k + 1;
          SEG(rkb, POOLOFF(dsc));
          ++nk;
          if (!second && (t >= (unsigned)TP || nk + 2 > NKMAX)) break;
        }
        ctl[C_NK] = nk;
      }
    }
    ctl[C_LATE] = 1;
    unsigned sv = b1 + (((NS - 5) * 8 + wg) & 127) * 16384;
#pragma unroll 1
    for (unsigned n = NS;; ++n) {
      unsigned t = 0, sk = 0;
      if (n + 2 >= nk) ctl[C_WANT] = k + 1;
      if (n < nk) sk = POOLOFF(rfl(ctl[C_DESC + n - NS]));
      else {
        if (!second) { LWAIT(C_TICK + k, 0u, t) }
        if (t < (unsigned)NT) {
          NEXT_ITEM(t, n);
          sk = POOLOFF(dsc);
          ctl[C_WANT] = k + 1;
          SEG(rkb, sk);
        } else {
          ctl[C_END] = n; ctl[C_PHASE] = 1; ++sg;
        }
      }
      SEG(rvb, sv);
      if (t >= (unsigned)NT) break;
      sv = sk;
    }
    ctl[C_PHASE] = 2;
#define DRAIN(k_) if (WINE >= k_) { waitvm<4 * k_>(); const int p_ = u - k_ + (k_ < 4 ? 4 : 0); if (p_ > pub) { pub = p_; ctl[C_FULL] = pub; } }
    DRAIN(8) DRAIN(6) DRAIN(4) DRAIN(2) DRAIN(0)
    return;
  }
  const __amdgpu_buffer_rsrc_t ws = mkrs(a.ws, W_ENDB);
  const unsigned tstart = now32();
  const int cb = (wg & ~15) + ((wg & 7) << 1) + ((wg >> 3) & 1);
  const int side = j >> 2, prod = g * 4 + (j & 3);

  if (w == 1) {
    const v2u ep = __builtin_amdgcn_raw_buffer_load_b64(ws, W_X, 0, SC1);
    if (cb >= 128 && (cb & 3) == 0 && lane == 0) __builtin_amdgcn_raw_buffer_store_b32(0u, ws, W_CTR + ((cb - 128) >> 2) * 256, 0, SC1);
    ctl[lane] = lane == C_END || lane == C_NK ? 0x7fffffffu : 0u;
    ctl[lane + 64] = 0xffffffffu; ctl[lane + 128] = 0xffffffffu; ctl[lane + 192] = 0xffffffffu;
    asm volatile("s_waitcnt lgkmcnt(0)\n\ts_barrier" ::: "memory");
    unsigned ta = rfl(ep.x) + 1u, tb = rfl(ep.y) + 1u;
    ta += ta == 0u; tb += tb == 0u;
    waitvm<0>();
    if (!wait_ge<1>(ctl, C_QCNT, 9)) return;
    if (lane < 16) {
      v4f s = {0.f, 0.f, 0.f, 0.f};
#pragma unroll
      for (int i = 0; i < 9; ++i) s += *(LAS v4f*)(L + L_QP + i * 256 + lane * 16);
      const int b = lane >> 3, quad = lane & 7;
      const int off = cb < 128 ? W_QA + ((b * 16 + (cb >> 3)) * 64 + (cb & 7) * 8 + quad) * 16 : W_QB + (((cb - 128) >> 2) * 64 + b * 32 + (cb & 3) * 8 + quad) * 16;
      gst(ws, off, pk_bf16(s.x, s.y), pk_bf16(s.z, s.w), ta, tb);
    }
    v4u qa, qb;
#pragma unroll 1
    for (unsigned sp = 0;; ++sp) {
      __builtin_amdgcn_s_sleep(8);
      qa = gld(ws, W_QA + g * 1024 + lane * 16);
      qb = gld(ws, W_QB + g * 1024 + lane * 16);
      if (__all(gbad(qb, ta, tb, gbad(qa, ta, tb, 0u)) == 0u)) break;
      if (sp > SPIN_CAP) return;
    }
    *(LAS v2u*)(L + L_Q + lane * 8) = v2u{qa.x, qa.z};
    *(LAS v2u*)(L + L_Q + 512 + lane * 8) = v2u{qb.x, qb.z};
    ctl[C_TAGA] = ta; ctl[C_TAGB] = tb;
    lgkm0();
    ctl[C_QRDY] = 1;
#pragma unroll 1
    for (int k = 0; k <= NT; ++k) {
      if (!wait_ge<2>(ctl, C_WANT, k + 1)) return;
      unsigned t = 0;
      if (lane == 0) t = __hip_atomic_fetch_add((GAS unsigned*)(a.ws + W_CTR + g * 256), 1u, __ATOMIC_RELAXED, __HIP_MEMORY_SCOPE_AGENT);
      t = rfl(t);
      ctl[C_TICK + k] = t;
      if (t >= (unsigned)NT) break;
    }
    if (!wait_ge<1>(ctl, C_T1, 1)) return;
    hint_set(ws, lane < NREP ? W_F2 + (side * NREP + lane) * 1024 + prod * 8 : OOB, ctl[C_T1], ta);
    if (prod < 72) {
      const int ho = W_F2 + (side * NREP + prod % NREP) * 1024 + lane * 8;
      ctl[C_T2] = (hint_time<true>(ws, ho, ho + 512, ta, tstart) + tstart + DT2) | 1u;
    }
    return;
  }
  if (w == 2) {
    {
      const size_t pa = (size_t)g * (2u << 20) + (3u << 19), pb = (size_t)g * (2u << 20), pc = pb + (64u << 20);
      const unsigned char* wq = cb < 128 ? a.wqA : a.wqB;
      const unsigned char* pw = (side ? a.woB : a.woA) + (size_t)((side ? g : 2 * (g & 15) + (g >> 4)) * 128 + (j & 3) * 32) * 1152;
      unsigned d0, d1, d2, d3, d4, d5, d6;
      asm volatile("s_load_dword %0, %7, 0\n\ts_load_dword %1, %8, 0\n\ts_load_dword %2, %9, 0\n\ts_load_dword %3, %10, 0\n\ts_load_dword %4, %11, 0\n\t"
                   "s_load_dword %5, %12, 0\n\ts_load_dword %6, %13, 0\n\ts_barrier\n\ts_waitcnt lgkmcnt(0)"
                   : "=&s"(d0), "=&s"(d1), "=&s"(d2), "=&s"(d3), "=&s"(d4), "=&s"(d5), "=&s"(d6)
                   : "s"(a.kA + pa), "s"(wq), "s"(wq + (2u << 20)), "s"(wq + (4u << 20)), "s"(cb < 128 ? a.xA : a.xB), "s"(a.ws + 2048), "s"(a.vA + pa) : "memory");
      asm volatile("s_load_dword %0, %7, 0\n\ts_load_dword %1, %8, 0\n\ts_load_dword %2, %9, 0\n\ts_load_dword %3, %10, 0\n\ts_load_dword %4, %11, 0\n\t"
                   "s_load_dword %5, %12, 0\n\ts_load_dword %6, %13, 0\n\ts_waitcnt lgkmcnt(0)"
                   : "=&s"(d0), "=&s"(d1), "=&s"(d2), "=&s"(d3), "=&s"(d4), "=&s"(d5), "=&s"(d6)
                   : "s"(a.kB + pb), "s"(a.kB + pc), "s"(a.vB + pb), "s"(a.vB + pc), "s"(pw), "s"(a.ws + W_SAFE1), "s"(a.ws + W_SAFE2) : "memory");
    }
    const int nr1 = side ? 56 : 8;
    const int ta_ = (g & 15) * 2 + (g >> 4);
    const int b0 = side ? W_P1 + g * P1_STR : W_P + ta_ * P_STR, b1 = W_P + (side ? 64 + g : 32 + ta_) * P_STR, d0 = side ? 8192 : 1024, dest = 2 * (j & 3);
    int voff = b0 + dest * d0 + lane * 16;
    int voffb = voff + d0;
    int voff7 = b1 + dest * 1024 + lane * 16;
    int voff8 = lane >= 56 ? b1 + 8192 + (lane - 56) * 16 : b0 + 8 * d0 + (lane < nr1 ? lane : 0) * 16;
    asm volatile("" : "+v"(voff), "+v"(voffb), "+v"(voff7), "+v"(voff8));
    if (!wait_ge<16>(ctl, C_LATE, 1)) return;
    if (!wait_ge<2>(ctl, C_PHASE, 2)) return;
    const unsigned ta = rfl(ctl[C_TAGA]), tb = rfl(ctl[C_TAGB]);
    const int hoff = side ? (lane < 8 ? W_F1 + (g * 8 + lane) * 128 : OOB) : (lane < 16 ? W_F1 + ((g & 15) * 8 + (lane >> 3) * 128 + (lane & 7)) * 128 : OOB);
    wait_until(tstart, hint_time<false>(ws, hoff, OOB, ta, tstart) + DT1);
    v4u X[2][8], ML;
#pragma unroll 1
    for (unsigned sp = 0;; ++sp) {
      const unsigned tr = now32();
      ML = gld(ws, voff8);
#pragma unroll
      for (int i = 0; i < 7; ++i) { X[0][i] = gld(ws, voff, side ? i * 1024 : 0); X[1][i] = gld(ws, voffb, side ? i * 1024 : 0); }
      X[0][7] = gld(ws, voff7); X[1][7] = gld(ws, voff7 + 1024);
      ctl[C_T1] = tr | 1u;
      __builtin_amdgcn_sched_barrier(0);
      const bool seg1 = lane >= 56;
      const float mr = fbits(ML.x);
      const float M = max8(mr);
      const float M0 = max8to64(seg1 ? -INFINITY : M);
      const float f = (lane >= nr1 && !seg1) ? 0.f : ex2(mr - (seg1 ? M : M0));
      float fb[8];
#pragma unroll
      for (int b = 0; b < 8; ++b) fb[b] = fbits(__builtin_amdgcn_ds_bpermute((8 * b + (lane >> 3)) * 4, ubits(f)));
      float Ls = f * fbits(ML.z);
      Ls += dpp<0xB1>(Ls); Ls += dpp<0x4E>(Ls); Ls += dpp<0x141>(Ls);
      const float L1 = fbits(__builtin_amdgcn_readlane(ubits(Ls), 56));
      float L0 = seg1 ? 0.f : Ls;
      L0 += dpp<0x128>(L0); L0 = xsum(L0);
      const float inv = __builtin_amdgcn_rcpf(lane >= 32 ? L1 : L0);
      __builtin_amdgcn_sched_barrier(0);
      unsigned bad = gbad(ML, ta, tb, 0u);
#pragma unroll
      for (int i = 0; i < 8; ++i) bad = gbad(X[1][i], ta, tb, gbad(X[0][i], ta, tb, bad));
      float oe[2], oo[2];
#pragma unroll
      for (int h = 0; h < 2; ++h) {
        float o0 = 0.f, o1 = 0.f;
#pragma unroll
        for (int b = 0; b < 7; ++b) { o0 = fmaf(fb[b], fbits(X[h][b].x), o0); o1 = fmaf(fb[b], fbits(X[h][b].z), o1); }
        oe[h] = rs32(o0, fb[7] * fbits(X[h][7].x)); oo[h] = rs32(o1, fb[7] * fbits(X[h][7].z));
      }
      float e = rs16(oe[0], oe[1]), o = rs16(oo[0], oo[1]);
      e += dpp<0x128>(e); o += dpp<0x128>(o);
      *(volatile LAS unsigned*)(L + L_OP + (lane >> 4) * 32 + (lane & 7) * 4) = pk_bf16(e * inv, o * inv);
      if (__all(bad == 0u)) break;
      if (sp > SPIN_CAP) return;
      __builtin_amdgcn_s_sleep(4);
    }
    ctl[C_OSEQ] = 1;
    return;
  }
  {
    const int qw = w - 3;
    const int r0 = qw * 64 + (lane >> 2) * 4;
    const unsigned char* xp = (cb < 128 ? a.xA : a.xB) + r0 * 2;
    const unsigned char* wp = (cb < 128 ? a.wqA : a.wqB) + (size_t)r0 * 8192 + (cb & 127) * 64 + (lane & 3) * 16;
    v4u Wq[4];
#pragma unroll
    for (int i = 0; i < 4; ++i) Wq[i] = __builtin_nontemporal_load((const GAS v4u*)(wp + i * 8192));
    const v2u X0 = *(const GAS v2u*)xp, X1 = *(const GAS v2u*)(xp + 1152);
    asm volatile("s_barrier" ::: "memory");
    float ac[2][8];
#pragma unroll
    for (int k = 0; k < 8; ++k) { ac[0][k] = 0.f; ac[1][k] = 0.f; }
#pragma unroll
    for (int s = 0; s < 2; ++s)
#pragma unroll
      for (int d = 0; d < 4; ++d) {
        const unsigned lo = __builtin_amdgcn_perm(Wq[2 * s + 1][d], Wq[2 * s][d], 0x05040100u), hi = __builtin_amdgcn_perm(Wq[2 * s + 1][d], Wq[2 * s][d], 0x07060302u);
        ac[0][2 * d] = dot2(lo, X0[s], ac[0][2 * d]); ac[0][2 * d + 1] = dot2(hi, X0[s], ac[0][2 * d + 1]);
        ac[1][2 * d] = dot2(lo, X1[s], ac[1][2 * d]); ac[1][2 * d + 1] = dot2(hi, X1[s], ac[1][2 * d + 1]);
      }
    float b8[8], c4[4];
#pragma unroll
    for (int k = 0; k < 8; ++k) b8[k] = rs32(ac[0][k], ac[1][k]);
#pragma unroll
    for (int k = 0; k < 4; ++k) { float t = rs16(b8[k], b8[k + 4]); t += dpp<0x128>(t); t += dpp<0x124>(t); c4[k] = t; }
    if ((lane & 12) == 0) *(LAS v4f*)(L + L_QP + qw * 256 + (lane >> 5) * 128 + (lane & 3) * 32 + ((lane >> 4) & 1) * 16) = v4f{c4[0], c4[1], c4[2], c4[3]};
    lds_inc(L, C_QCNT, lane);
  }
  const int r = lane >> 4, c = lane & 15;
  const unsigned kof = c * 256 + r * 16 + (c & 3) * 64;
  const unsigned vof = (4 * r + (c >> 2)) * 256 + (c >> 2) * 64 + (c & 3) * 8;
  if (w >= 4) {
    const unsigned aw = w - 4, off = aw * 8192, an = aw >> 1;
    v4u K0[4], K1[4], V[8];
    if (!wait_ge<1>(ctl, C_FULL, 2 * aw + 2)) return;
    read_k(L, off + kof, 0, K0); read_k(L, off + kof, 4096, K1);
    lds_inc(L, C_FREE + an, lane);
    if (!wait_ge<1>(ctl, C_FULL, 2 * aw + 18)) return;
    read_v(L, 65536 + off + vof, 0, V);
    lds_inc(L, C_FREE + 4 + an, lane);
    if (!wait_ge<1>(ctl, C_QRDY, 1)) return;
    const LAS v4u* qp = (const LAS v4u*)(L + L_Q) + r + (c >> 3) * 16;
    const v4u q[4] = {qp[0], qp[4], qp[8], qp[12]};
    v4f s0 = round2(qk(K0, q)), s1 = round2(qk(K1, q));
    const float M = xmax(fmaxf(max4(s0), max4(s1))) * L2E;
    s0 = expo(s0, M); s1 = expo(s1, M);
    const v4u P = {pk_bf16(s0[0], s0[1]), pk_bf16(s0[2], s0[3]), pk_bf16(s1[0], s1[1]), pk_bf16(s1[2], s1[3])};
    const float Ls = xsum(sum4(s0) + sum4(s1));
    v4f acc[8];
#pragma unroll
    for (int blk = 0; blk < 8; ++blk) acc[blk] = mfma(V[blk], P, v4f{0.f, 0.f, 0.f, 0.f});
#pragma unroll
    for (int k = 0; k < 4; ++k) acc[k] = sel4(bitmask(c, 2), acc[k + 4], acc[k]);
#pragma unroll
    for (int k = 0; k < 2; ++k) acc[k] = sel4(bitmask(c, 1), acc[k + 2], acc[k]);
    acc[0] = sel4(bitmask(c, 0), acc[1], acc[0]);
    LAS unsigned char* rec = L + L_REC + ((c >> 3) * 8 + aw) * REC_B;
    *(LAS v4f*)(rec + 16 + ((c & 7) * 16 + r * 4) * 4) = acc[0];
    if ((lane & 0x37) == 0) *(LAS v2f*)rec = v2f{M, Ls};
    if (last_arriver(L, C_CNT, 8, lane)) {
#pragma unroll 1
      for (int e = 0; e < 2; ++e) {
        float Mo, Lo;
        const v2f o = merge_rec<8>(L + L_REC + e * 8 * REC_B, lane, Mo, Lo);
        const int base = W_P + (g * 2 + e) * P_STR;
        if (park_rec(L, ctl, e, base + j * 128, 1024, base + 8192 + j * 16, lane, lane, o, Mo, Lo)) {
          const unsigned ta_ = ctl[C_TAGA], tb_ = ctl[C_TAGB];
          gst(ws, base + j * 128 + (lane >> 3) * 1024 + (lane & 7) * 16, ubits(o.x), ubits(o.y), ta_, tb_);
          if (lane == 0) gst(ws, base + 8192 + j * 16, ubits(Mo), ubits(Lo), ta_, tb_);
        }
      }
    }
  }
  if (w < 8) {
    const int k = w - 3;
    const unsigned char* pw = (side ? a.woB : a.woA) + (size_t)((side ? g : 2 * (g & 15) + (g >> 4)) * 128 + (j & 3) * 32) * 1152;
    const bool live = k < 4 || c < 8;
    if (!wait_ge<16>(ctl, C_LATE, 1)) return;
    const unsigned ta = rfl(ctl[C_TAGA]), tb = rfl(ctl[C_TAGB]);
    if (k == 0) {
      const unsigned t0 = __builtin_nontemporal_load((const GAS unsigned*)(pw + (lane & 1) * (31 * 1152))), t1 = __builtin_amdgcn_raw_buffer_load_b32(ws, lane & 1 ? W_SAFE1 : W_SAFE2, 0, SC1);
      const unsigned t2 = __builtin_amdgcn_raw_buffer_load_b32(mkrs(a.out, 4608), (lane & 1) * 4096, 0, 2);
      asm volatile("" ::"v"(t0), "v"(t1), "v"(t2));
    }
    if (!wait_ge<2>(ctl, C_PHASE, 1)) return;
    const unsigned held = rfl(ctl[C_HELD]);
    asm volatile("" ::: "memory");
#pragma unroll 1
    for (int slot = k; slot < NHOLD; slot += 5) {
      if (!(held >> slot & 1)) continue;
      LAS unsigned char* h = L + L_HOLD + slot * HOLD_B;
      const v4u hd = *(LAS v4u*)h;
      const v2u ml = *(LAS v2u*)(h + 16), o = *(LAS v2u*)(h + 32 + lane * 8);
      gst(ws, hd.x + (lane >> 3) * hd.y + (lane & 7) * 16, o.x, o.y, ta, tb);
      gst(ws, lane == 0 ? hd.z : OOB, ml.x, ml.y, ta, tb);
    }
    if (!wait_ge<2>(ctl, C_PHASE, 2)) return;
    v4u Wf[8], Wr[8];
    const __amdgpu_buffer_rsrc_t rw = mkrs(pw, 32 * 1152);
#pragma unroll
    for (int i = 0; i < 8; ++i) Wr[i] = __builtin_amdgcn_raw_buffer_load_b128(rw, (live ? 8 * r * 1152 + k * 256 + c * 16 : OOB) + i * 1152, 0, 2);
#pragma unroll
    for (int e = 0; e < 4; ++e)
#pragma unroll
      for (int d = 0; d < 4; ++d) {
        Wf[2 * e][d] = __builtin_amdgcn_perm(Wr[2 * d + 1][e], Wr[2 * d][e], 0x05040100u);
        Wf[2 * e + 1][d] = __builtin_amdgcn_perm(Wr[2 * d + 1][e], Wr[2 * d][e], 0x07060302u);
      }
    int y0 = live ? W_Y + ((side ? 3 - (r & 1) : (r & 1)) * 36 + k * 8 + (c >> 2)) * YS + prod * 128 + (2 * (c & 3) + (r >> 1)) * 16 : OOB, y1 = y0 + (k < 4 ? 4 : 2) * YS;
    const unsigned mh = bitmask(r, 1);
    asm volatile("" : "+v"(y0), "+v"(y1), "+v"(Wf[0]), "+v"(Wf[1]), "+v"(Wf[2]), "+v"(Wf[3]), "+v"(Wf[4]), "+v"(Wf[5]), "+v"(Wf[6]), "+v"(Wf[7]));
    v4u oa;
#pragma unroll 1
    for (unsigned sp = 0;; ++sp) {
      const unsigned fl = ctl[C_OSEQ];
      oa = *(volatile LAS v4u*)(L + L_OP + ((c >> 2) & 1) * 64 + r * 16);
      if (rfl(fl)) break;
      if (sp > SPIN_CAP) return;
      __builtin_amdgcn_s_sleep(1);
    }
#pragma unroll
    for (int i = 0; i < 2; ++i) {
      v4f d[4];
#pragma unroll
      for (int f = 0; f < 4; ++f) d[f] = mfma(oa, Wf[(f >> 1) * 4 + 2 * i + (f & 1)], v4f{0.f, 0.f, 0.f, 0.f});
      gst(ws, i ? y1 : y0, ubits(sel(mh, d[2][0], d[0][0])), ubits(sel(mh, d[3][0], d[1][0])), ta, tb);
    }
    return;
  }
  const unsigned ta = rfl(ctl[C_TAGA]), tb = rfl(ctl[C_TAGB]);
  const int cw = w - 8;
  {
    float m = -INFINITY, l = 0.f;
    v4f acc[8];
    bool fresh = true, freshv = true;
    const unsigned mk0 = bitmask(c, 0), mk1 = bitmask(c, 1), mk2 = bitmask(c, 2), mk3 = bitmask(c, 3);
    const int gid = (c >> 1) * 8 + 2 * r + (c & 1);
    int mode = 1;
    unsigned nk = 0x7fffffffu;
#pragma unroll 1
    for (unsigned n = 4 + cw;; n = n + 4 < NS ? n + 4 : n < NS ? NS : n + 1) {
      bool stop = false;
      unsigned dsc = cw << 8;
#pragma unroll 1
      for (unsigned sp = 0; n >= NS; ++sp) {
        const unsigned dd = ctl[C_DESC + n - NS], en = ctl[mode == 1 ? C_NK : C_END];
        dsc = rfl(dd);
        if (n >= rfl(en) || sp > SPIN_CAP) {
          if (mode != 1) { stop = true; break; }
          nk = n; n = 4 + cw; mode = 2;
        } else if (dsc == 0xffffffffu) __builtin_amdgcn_s_sleep(2);
        else if ((dsc >> 8 & 3) == (unsigned)cw) break;
        else ++n;
      }
      if (stop) break;
      if (n >= nk) mode = 3;
      const bool dyn = n >= NS;
      const bool pubn = dyn ? (dsc >> 10 & 1) : (n >= NS - 4 || (n & ~3u) == 16);
      const int base = n >= 20 ? W_P1 + g * P1_STR : W_P + (64 + g) * P_STR, dstr = n >= 20 ? 8192 : 1024, rec = dyn ? dsc & 255 : j, slot = dyn ? dsc >> 12 & 255 : 2 + (n >= 20);
      const int gi = dyn ? gid : lane;
      const unsigned sk = n < nk ? n + 4 : 2 * n - 1, sv = n + 1 < nk ? n + nk : 2 * n + 2;
      int ad = base + rec * 128 + (gi >> 3) * dstr + (gi & 7) * 16, am = lane == 0 ? base + 8 * dstr + rec * 16 : OOB;
      unsigned ka = (sk & 7) * 16384 + kof, va = (sv & 7) * 16384 + vof;
      LAS unsigned char* pb = L + L_PB + (mode == 3 ? 0 : n - 4) * PB_B;
      v4u P0, P1, st;
      float al, Ls, mrec;
      const bool first = freshv;
      if ((mode & 2) && freshv) {
#pragma unroll
        for (int blk = 0; blk < 8; ++blk) acc[blk] = v4f{0.f, 0.f, 0.f, 0.f};
        freshv = false;
      }
      if (mode & 1) {
        const LAS v4u* qp = (const LAS v4u*)(L + L_Q) + r + (n < 20 ? 32 : 48);
        v4u q[4] = {qp[0], qp[4], qp[8], qp[12]};
        if (fresh) { m = -INFINITY; l = 0.f; }
        fresh = pubn;
        asm volatile("" : "+v"(ad), "+v"(am), "+v"(ka), "+v"(va), "+v"(q[0]), "+v"(q[1]), "+v"(q[2]), "+v"(q[3]));
        if (!wait_ge<1>(ctl, C_FULL, 4 * sk + 4)) return;
        v4f s[4];
        v4u K[4][4];
#pragma unroll
        for (int uu = 0; uu < 4; ++uu) read_k(L, ka, uu * 4096, K[uu]);
        __builtin_amdgcn_sched_barrier(0);
#pragma unroll
        for (int uu = 0; uu < 4; ++uu) s[uu] = qk(K[uu], q);
        __builtin_amdgcn_sched_barrier(0);
        lgkm0();
        ctl[C_FREE + (sk & 7)] = sk + 1;
        v4f sc = round2(sel4(mk3, sel4(mk2, s[3], s[2]), sel4(mk2, s[1], s[0])));
        const float mn = fmaxf(max4to64(max4(sc)) * L2E, m);
        st = *(volatile LAS v4u*)(L + L_CTL);
        sc = expo(sc, mn);
        const unsigned px = pk_bf16(sc[0], sc[1]), py = pk_bf16(sc[2], sc[3]);
        P0 = v4u{bcast16<0>(px), bcast16<0>(py), bcast16<4>(px), bcast16<4>(py)}; P1 = v4u{bcast16<8>(px), bcast16<8>(py), bcast16<12>(px), bcast16<12>(py)};
        al = ex2(m - mn);
        mrec = m = mn;
        l = fmaf(l, al, sum4(sc));
        Ls = l + dpp<0x124>(l);
        Ls += dpp<0x128>(Ls);
        Ls = xsum(Ls);
        if (mode == 1) {
          *(LAS v4u*)(pb + r * 16) = P0; *(LAS v4u*)(pb + 64 + r * 16) = P1; *(LAS v4f*)(pb + 128) = v4f{al, Ls, mn, 0.f};
        }
      } else {
        P0 = *(LAS v4u*)(pb + r * 16); P1 = *(LAS v4u*)(pb + 64 + r * 16);
        const v4f t = *(LAS v4f*)(pb + 128);
        al = t.x; Ls = t.y; mrec = t.z;
        st = *(volatile LAS v4u*)(L + L_CTL);
      }
      if (mode == 1) continue;
      if (!first) {
#pragma unroll
        for (int blk = 0; blk < 8; ++blk) acc[blk] *= al;
      }
      asm volatile("" : "+v"(P0), "+v"(P1), "+v"(Ls), "+v"(acc[0]), "+v"(acc[1]), "+v"(acc[2]), "+v"(acc[3]), "+v"(acc[4]), "+v"(acc[5]), "+v"(acc[6]), "+v"(acc[7]));
      v4u V[8];
#define WAITV(need) _Pragma("unroll 1") for (unsigned sp = 0; rfl(st.x) < (need); ++sp) { if (sp > SPIN_CAP) return; if (sp) __builtin_amdgcn_s_sleep(1); st = *(volatile LAS v4u*)(L + L_CTL); } \
      asm volatile("" ::: "memory");
      WAITV(4 * sv + 2)
      read_v(L, va, 0, V);
#pragma unroll
      for (int blk = 0; blk < 8; ++blk) acc[blk] = mfma(V[blk], P0, acc[blk]);
      WAITV(4 * sv + 4)
      const unsigned tl = now32(), nend = rfl(st.y);
      read_v(L, va, 8192, V);
      hint_set(ws, lane == 0 && n + 1 == nend ? W_F1 + wg * 128 : OOB, tl, ta);
#pragma unroll
      for (int blk = 0; blk < 8; ++blk) acc[blk] = mfma(V[blk], P1, acc[blk]);
      if (pubn) {
        v2f o[8];
#pragma unroll
        for (int k = 0; k < 8; ++k) o[k] = sel2(mk0, v2f{acc[k][2], acc[k][3]}, v2f{acc[k][0], acc[k][1]});
#pragma unroll
        for (int k = 0; k < 4; ++k) o[k] = sel2(mk3, o[k + 4], o[k]);
#pragma unroll
        for (int k = 0; k < 2; ++k) o[k] = sel2(mk2, o[k + 2], o[k]);
        o[0] = sel2(mk1, o[1], o[0]);
        bool now = dyn && (slot >= NHOLD || rfl(st.z) != 0);
        if (!now) {
          bool emit = true;
          if (!dyn) {
            LAS unsigned char* rp = L + L_REC + (8 + slot * 4) * REC_B;
            *(LAS v2f*)(rp + cw * REC_B + 16 + gid * 8) = o[0];
            if (lane == 0) *(LAS v2f*)(rp + cw * REC_B) = v2f{mrec, Ls};
            emit = last_arriver(L, C_CNT + slot - 1, 4, lane);
            if (emit) o[0] = merge_rec<4>(rp, lane, mrec, Ls);
          }
          if (emit) now = park_rec(L, ctl, slot, base + rec * 128, dstr, base + 8 * dstr + rec * 16, gi, lane, o[0], mrec, Ls);
          if (!now) { ad = OOB; am = OOB; }
        }
        gst(ws, ad, ubits(o[0].x), ubits(o[0].y), ta, tb);
        gst(ws, am, ubits(mrec), ubits(Ls), ta, tb);
        freshv = true;
      }
      lgkm0();
      ctl[C_FREE + (sv & 7)] = sv + 1;
    }
  }
  if (prod >= 72) return;
  const int cmb = side * 2 + (prod & 1), rho = prod >> 1;
  int voff = W_Y + (cmb * 36 + rho) * YS + cw * 4096 + lane * 16;
  const int yi = rho < 32 ? (rho >> 2) & 1 : (rho >> 1) & 1, yq = rho < 32 ? rho & 3 : rho & 1;
  int oo = lane < 16 ? ((cmb & 1) * 1152 + (cmb >> 1) * 576 + (rho >> 3) * 128 + (yq * 4 + (lane >> 2)) * 8 + ((lane >> 1) & 1) * 4 + yi * 2 + (lane & 1)) * 2 : OOB;
  asm volatile("" : "+v"(voff), "+v"(oo));
  if (!wait_ge<4>(ctl, C_T1, 1)) return;
  if (!wait_ge<1>(ctl, C_T2, 1)) return;
  wait_until(tstart, rfl(ctl[C_T2]) - tstart);
  v4u X[4];
#pragma unroll 1
  for (unsigned sp = 0;; ++sp) {
#pragma unroll
    for (int i = 0; i < 4; ++i) X[i] = gld(ws, voff, i * 1024);
    unsigned bad = 0;
#pragma unroll
    for (int i = 0; i < 4; ++i) bad = gbad(X[i], ta, tb, bad);
    if (__all(bad == 0u)) break;
    if (sp > SPIN_CAP) return;
    __builtin_amdgcn_s_sleep(4);
  }
  float s = rs32((fbits(X[0].x) + fbits(X[1].x)) + (fbits(X[2].x) + fbits(X[3].x)), (fbits(X[0].z) + fbits(X[1].z)) + (fbits(X[2].z) + fbits(X[3].z)));
  s = rs16(s, s);
  s += dpp<0x128>(s);
  *(volatile LAS unsigned*)(L + L_FP + (((lane & 7) * 2 + (lane >> 5)) * 4 + cw) * 4) = min(ubits(s), 0xfffffffeu);
  if (cw != 0) return;
#pragma unroll 1
  for (unsigned sp = 0;; ++sp) {
    const v4u p = *(volatile LAS v4u*)(L + L_FP + (lane & 15) * 16);
    s = (fbits(p.x) + fbits(p.y)) + (fbits(p.z) + fbits(p.w));
    if (__all(p.y != ~0u && p.z != ~0u && p.w != ~0u)) break;
    if (sp > SPIN_CAP) return;
  }
  __builtin_amdgcn_raw_buffer_store_b16((unsigned short)pk_bf16(s, s), mkrs(a.out, 4608), oo, 0, SC1);
  __builtin_amdgcn_raw_buffer_store_b64(v2u{ta, tb}, ws, cmb == 3 && rho == 0 && lane == 0 ? W_X : OOB, 0, SC1);
}

extern "C" void kernel_launch(void* const* d_in, const int* in_sizes, int n_in, void* d_out, int out_size, void* d_ws, size_t ws_size,
                              hipStream_t stream) {
  Args a;
  a.xA = (const unsigned char*)d_in[0]; a.kA = (const unsigned char*)d_in[1]; a.vA = (const unsigned char*)d_in[2];
  a.wqA = (const unsigned char*)d_in[3]; a.woA = (const unsigned char*)d_in[4];
  a.xB = (const unsigned char*)d_in[5]; a.kB = (const unsigned char*)d_in[6]; a.vB = (const unsigned char*)d_in[7];
  a.wqB = (const unsigned char*)d_in[8]; a.woB = (const unsigned char*)d_in[9];
  a.out = (unsigned char*)d_out; a.ws = (unsigned char*)d_ws;
  fused_decode<<<256, NTHR, 0, stream>>>(a);
}
```
